```python
import torch

def _softmax(x, axis=-1):
    return torch.softmax(x, dim=axis)
def _gelu(x):
    return torch.nn.functional.gelu(x, approximate="tanh")
def _silu(x):
    return torch.nn.functional.silu(x)
def _sigmoid(x):
    return torch.sigmoid(x)
def _layernorm(x, axis=-1, eps=1e-5):
    m = x.mean(dim=axis, keepdim=True)
    v = x.var(dim=axis, keepdim=True, unbiased=False)
    return (x - m) / torch.sqrt(v + eps)
def _scatter_add(tgt, idx, src, axis=0):
    return tgt.clone().index_add_(axis, idx, src)
def _segment_sum(src, seg, n):
    out = torch.zeros((n,) + src.shape[1:], dtype=src.dtype)
    return out.index_add_(0, seg, src)
def _pad(x, pw):
    flat = tuple(v for pair in reversed(pw) for v in pair)
    return torch.nn.functional.pad(x, flat)

kernel_name = 'recurrent_scan__mha_block_cached__glu_gated_swiglu__prompt'

def setup_inputs():

    _w = (lambda a: (lambda t: t.to(torch.bfloat16) if t.is_floating_point() else t)(torch.as_tensor(a)))
    def _A_setup():
        B, Sq, D = 1, 2048, 640
        H, Sk, Dh = 32, 2048, 192
        return {
            "x": _w((lambda *s: torch.randn(*s, dtype=torch.bfloat16) * 0.05)(B, Sq, D)),
            "k_cache": _w((lambda *s: torch.randn(*s, dtype=torch.bfloat16) * 1.6)(B, H, Sk, Dh)),
            "v_cache": _w((lambda *s: torch.randn(*s, dtype=torch.bfloat16) * 1.6)(B, H, Sk, Dh)),
            "wq": _w((lambda *s: torch.randn(*s, dtype=torch.bfloat16) * 1.6)(D, H * Dh) * (D ** -0.5)),
            "wo": _w((lambda *s: torch.randn(*s, dtype=torch.bfloat16) * 1.6)(H * Dh, D) * ((H * Dh) ** -0.5)),
        }
    _A_in = _A_setup()

    def _B_setup():
        B, S, D, F = 1, 2048, 640, 1536
        return {
            "x": _w((lambda *s: torch.randn(*s, dtype=torch.bfloat16) * 0.05)(B, S, D)),
            "w1": _w((lambda *s: torch.randn(*s, dtype=torch.bfloat16) * 0.2)(D, F) * (2.0 / D) ** 0.5),
            "w3": _w((lambda *s: torch.randn(*s, dtype=torch.bfloat16) * 0.2)(D, F) * (2.0 / D) ** 0.5),
            "w2": _w((lambda *s: torch.randn(*s, dtype=torch.bfloat16) * 0.2)(F, D) * (2.0 / F) ** 0.5),
        }
    _B_in = _B_setup()

    return {
        "A_x": _A_in["x"],
        "A_k_cache": _A_in["k_cache"],
        "A_v_cache": _A_in["v_cache"],
        "A_wq": _A_in["wq"],
        "A_wo": _A_in["wo"],
        "B_x": _B_in["x"],
        "B_w1": _B_in["w1"],
        "B_w3": _B_in["w3"],
        "B_w2": _B_in["w2"],
    }

def reference(A_x, A_k_cache, A_v_cache, A_wq, A_wo, B_x, B_w1, B_w3, B_w2):
    def _A_ref(x, k_cache, v_cache, wq, wo):
        B, Sq, D = x.shape
        H, Dh = 32, 192
        q = (lambda x, *d: x.permute(*d))((x @ wq).reshape(B, Sq, H, Dh), 0, 2, 1, 3)
        scores = torch.einsum("bhqd,bhkd->bhqk", q, k_cache) * (Dh ** -0.5)
        p = _softmax(scores, axis=-1)
        o = torch.einsum("bhqk,bhkd->bhqd", p, v_cache)
        return (lambda x, *d: x.permute(*d))(o, 0, 2, 1, 3).reshape(B, Sq, H * Dh) @ wo

    def _B_ref(x, w1, w3, w2):
        gate = torch.einsum("bsd,df->bsf", x, w1)
        up = torch.einsum("bsd,df->bsf", x, w3)
        h = _silu(gate) * up
        out = torch.einsum("bsf,fd->bsd", h, w2)
        return out

    x = A_x
    xn = _layernorm(x, axis=-1)
    s = _A_ref(xn, A_k_cache, A_v_cache, A_wq, A_wo)
    g = _silu(_B_ref(xn, B_w1, B_w3, B_w2))
    return x + s * g
```

```cpp
#include <hip/hip_runtime.h>
#include <hip/hip_bf16.h>
typedef unsigned short u16;
#define DI __device__ __forceinline__
DI float bf2f(u16 v){ return __uint_as_float(((unsigned)v)<<16); }
DI u16 f2bf(float f){ unsigned u=__float_as_uint(f); u += 0x7fffu + ((u>>16)&1u); return (u16)(u>>16); }
DI float rbf(float f){ return bf2f(f2bf(f)); }
constexpr int S=2048, D=640, H=32, DH=192, F=1536, HD=H*DH;

__global__ void k_ln(const u16* x, u16* xn){
  int row = blockIdx.x*4 + (threadIdx.x>>6), lane = threadIdx.x&63;
  float v[10]; float s=0;
  for(int i=0;i<10;i++){ v[i]=bf2f(x[row*D+lane+64*i]); s+=v[i]; }
  for(int o=32;o;o>>=1) s+=__shfl_xor(s,o);
  float mean=s/D; float q=0;
  for(int i=0;i<10;i++){ float d=v[i]-mean; q+=d*d; }
  for(int o=32;o;o>>=1) q+=__shfl_xor(q,o);
  float var=rbf(q/D); float m=rbf(mean);
  float den=rbf(sqrtf(rbf(var+1e-5f)));
  for(int i=0;i<10;i++){ float d=rbf(v[i]-m); xn[row*D+lane+64*i]=f2bf(d/den); }
}
__global__ void k_gemm(const u16* A, const u16* B, u16* C, int M, int N, int K){
  __shared__ float As[32][33], Bs[32][33];
  int tx=threadIdx.x&31, ty=threadIdx.x>>5;
  int m0=blockIdx.y*32, n0=blockIdx.x*32;
  float acc[4]={0,0,0,0};
  for(int k0=0;k0<K;k0+=32){
    for(int i=0;i<4;i++){ As[ty+8*i][tx]=bf2f(A[(long)(m0+ty+8*i)*K+k0+tx]); Bs[ty+8*i][tx]=bf2f(B[(long)(k0+ty+8*i)*N+n0+tx]); }
    __syncthreads();
    for(int k=0;k<32;k++){ float b=Bs[k][tx]; for(int i=0;i<4;i++) acc[i]+=As[ty+8*i][k]*b; }
    __syncthreads();
  }
  for(int i=0;i<4;i++) C[(long)(m0+ty+8*i)*N+n0+tx]=f2bf(acc[i]);
}
__global__ void k_attn(const u16* q, const u16* kc, const u16* vc, u16* o){
  __shared__ float qs[4][DH]; __shared__ float ps[4][S];
  int w=threadIdx.x>>6, lane=threadIdx.x&63;
  int row=blockIdx.x*4+w, h=blockIdx.y;
  for(int i=lane;i<DH;i+=64) qs[w][i]=bf2f(q[(long)row*HD+h*DH+i]);
  __syncthreads();
  const u16* K=kc+(long)h*S*DH; const u16* V=vc+(long)h*S*DH;
  float mx=-1e30f;
  for(int j=lane;j<S;j+=64){ float s=0; const uint4* kr=(const uint4*)(K+(long)j*DH);

#pragma unroll 2
for(int c=0;c<DH/8;c++){ uint4 u=kr[c];
#define ST(e,W) s+=qs[w][c*8+2*e]*__uint_as_float(W<<16); s+=qs[w][c*8+2*e+1]*__uint_as_float(W&0xffff0000u);
      ST(0,u.x) ST(1,u.y) ST(2,u.z) ST(3,u.w) }
    s*=0.07216878364870322f; ps[w][j]=s; mx=fmaxf(mx,s); }
  for(int o2=32;o2;o2>>=1) mx=fmaxf(mx,__shfl_xor(mx,o2));
  float l=0; for(int j=lane;j<S;j+=64){ float p=__expf(ps[w][j]-mx); ps[w][j]=p; l+=p; }
  for(int o2=32;o2;o2>>=1) l+=__shfl_xor(l,o2);
  __syncthreads();
  float a0=0,a1=0,a2=0;
  for(int j=0;j<S;j++){ float p=ps[w][j]; const u16* vr=V+(long)j*DH; a0+=p*bf2f(vr[lane]); a1+=p*bf2f(vr[lane+64]); a2+=p*bf2f(vr[lane+128]); }
  float il=1.f/l; u16* orow=o+(long)row*HD+h*DH;
  orow[lane]=f2bf(a0*il); orow[lane+64]=f2bf(a1*il); orow[lane+128]=f2bf(a2*il);
}
__global__ void k_h(const u16* g, const u16* u, u16* h, int n){ int i=blockIdx.x*256+threadIdx.x; if(i<n){ float a=bf2f(g[i]); float s=rbf(a/(1.f+__expf(-a))); h[i]=f2bf(s*bf2f(u[i])); } }
__global__ void k_fin(const u16* x, const u16* s, const u16* o2, u16* out, int n){ int i=blockIdx.x*256+threadIdx.x; if(i<n){ float a=bf2f(o2[i]); float g=rbf(a/(1.f+__expf(-a))); out[i]=f2bf(bf2f(x[i])+rbf(bf2f(s[i])*g)); } }

extern "C" void kernel_launch(void* const* d_in, const int* in_sizes, int n_in, void* d_out, int out_size, void* d_ws, size_t ws_size, hipStream_t stream) {
  const u16* x=(const u16*)d_in[0]; const u16* kc=(const u16*)d_in[1]; const u16* vc=(const u16*)d_in[2];
  const u16* wq=(const u16*)d_in[3]; const u16* wo=(const u16*)d_in[4];
  const u16* w1=(const u16*)d_in[6]; const u16* w3=(const u16*)d_in[7]; const u16* w2=(const u16*)d_in[8];
  char* ws=(char*)d_ws;
  u16* xn=(u16*)ws; ws+= (size_t)S*D*2;
  u16* q=(u16*)ws; ws+=(size_t)S*HD*2;
  u16* o=(u16*)ws; ws+=(size_t)S*HD*2;
  u16* s=(u16*)ws; ws+=(size_t)S*D*2;
  u16* g=(u16*)ws; ws+=(size_t)S*F*2;
  u16* u=(u16*)ws; ws+=(size_t)S*F*2;
  u16* hh=(u16*)ws; ws+=(size_t)S*F*2;
  u16* o2=(u16*)ws; ws+=(size_t)S*D*2;
  k_ln<<<S/4,256,0,stream>>>(x,xn);
  k_gemm<<<dim3(HD/32,S/32),256,0,stream>>>(xn,wq,q,S,HD,D);
  k_attn<<<dim3(S/4,H),256,0,stream>>>(q,kc,vc,o);
  k_gemm<<<dim3(D/32,S/32),256,0,stream>>>(o,wo,s,S,D,HD);
  k_gemm<<<dim3(F/32,S/32),256,0,stream>>>(xn,w1,g,S,F,D);
  k_gemm<<<dim3(F/32,S/32),256,0,stream>>>(xn,w3,u,S,F,D);
  k_h<<<S*F/256,256,0,stream>>>(g,u,hh,S*F);
  k_gemm<<<dim3(D/32,S/32),256,0,stream>>>(hh,w2,o2,S,D,F);
  k_fin<<<S*D/256,256,0,stream>>>(x,s,o2,(u16*)d_out,S*D);
}
```

```cpp
#include <hip/hip_runtime.h>
#include <hip/hip_bf16.h>
typedef unsigned short u16;
#define DI __device__ __forceinline__
constexpr int S=2048, D=640, H=32, DH=192, F=1536, HD=H*DH;

constexpr float QSCALE = 0.07216878364870322f * 1.4426950408889634f;
typedef unsigned u32x4 __attribute__((ext_vector_type(4)));
DI float row16_sum(float v) {
  v += __builtin_bit_cast(float, __builtin_amdgcn_update_dpp(0, __builtin_bit_cast(int, v), 0x128, 0xf, 0xf, true));
  v += __builtin_bit_cast(float, __builtin_amdgcn_update_dpp(0, __builtin_bit_cast(int, v), 0x124, 0xf, 0xf, true));
  v += __builtin_bit_cast(float, __builtin_amdgcn_update_dpp(0, __builtin_bit_cast(int, v), 0x122, 0xf, 0xf, true));
  v += __builtin_bit_cast(float, __builtin_amdgcn_update_dpp(0, __builtin_bit_cast(int, v), 0x121, 0xf, 0xf, true));
  return v;
}
typedef float f32x2 __attribute__((ext_vector_type(2)));
typedef __bf16 bf16x2 __attribute__((ext_vector_type(2)));
DI unsigned pk_bf16(float lo, float hi) { f32x2 v = {lo, hi}; return __builtin_bit_cast(unsigned, __builtin_convertvector(v, bf16x2)); }
DI float rbf1(float f) { return __uint_as_float(pk_bf16(f, f) << 16); }
__global__ __launch_bounds__(128) void k_ln(const u16* __restrict__ x, u16* __restrict__ xnt, unsigned* __restrict__ cnt) {
  const int row = blockIdx.x * 8 + (threadIdx.x >> 4), rb = row >> 4, c = row & 15, j = threadIdx.x & 15;
  if (blockIdx.x == 0 && threadIdx.x < 64) cnt[threadIdx.x * 64] = 0u;
  const u32x4* src = (const u32x4*)(x + (size_t)row * D);
  u32x4 w[5];
#pragma unroll
  for (int i = 0; i < 5; ++i) w[i] = src[j + 16 * i];
  float v[40];
#pragma unroll
  for (int i = 0; i < 5; ++i)
#pragma unroll
    for (int e = 0; e < 4; ++e) { v[8 * i + 2 * e] = __uint_as_float(w[i][e] << 16); v[8 * i + 2 * e + 1] = __uint_as_float(w[i][e] & 0xffff0000u); }
  float s0 = 0.f, s1 = 0.f;
#pragma unroll
  for (int i = 0; i < 40; i += 2) { s0 += v[i]; s1 += v[i + 1]; }
  const float mean = row16_sum(s0 + s1) * (1.f / D);
  float q0 = 0.f, q1 = 0.f;
#pragma unroll
  for (int i = 0; i < 40; i += 2) { const float a = v[i] - mean, b = v[i + 1] - mean; q0 = fmaf(a, a, q0); q1 = fmaf(b, b, q1); }
  const float var = rbf1(row16_sum(q0 + q1) * (1.f / D)), m = rbf1(mean);
  const float den = rbf1(sqrtf(rbf1(var + 1e-5f)));
  const float r = QSCALE / den;
#pragma unroll
  for (int i = 0; i < 5; ++i) {
    u32x4 o;
#pragma unroll
    for (int e = 0; e < 4; ++e) {
      const unsigned d = pk_bf16(v[8 * i + 2 * e] - m, v[8 * i + 2 * e + 1] - m);
      o[e] = pk_bf16(__uint_as_float(d << 16) * r, __uint_as_float(d & 0xffff0000u) * r);
    }
    const int ch = j + 16 * i, ks = ch >> 2, g = ch & 3;
    *(u32x4*)(xnt + ((size_t)(rb * 20 + ks) * 64 + g * 16 + c) * 8) = o;
  }
}

__global__ __launch_bounds__(256, 1) void k2_attn(const u16* __restrict__ xn, const u16* __restrict__ wq, const u16* __restrict__ kc,
                                                   const u16* __restrict__ vc, u16* __restrict__ ot, unsigned* __restrict__ dbg,
   const u16* __restrict__ w1, const u16* __restrict__ w3, u16* __restrict__ ht,
   const u16* __restrict__ wo, const u16* __restrict__ w2, float* __restrict__ slab,
   const u16* __restrict__ x, u16* __restrict__ out, unsigned* __restrict__ cnt) {
  asm volatile(
    "s_load_dwordx16 s[80:95], %1, 0\n"
    "s_load_dwordx4 s[4:7], %1, 72\n"
    "s_load_dwordx2 s[10:11], %1, 64\n"
    "s_load_dwordx2 s[30:31], %1, 88\n"
    "s_load_dwordx4 s[16:19], %1, 96\n"
    "s_load_dwordx2 s[34:35], %1, 112\n"
    "s_waitcnt lgkmcnt(0)\n"
    "s_mov_b64 s[8:9], s[90:91]\n"
    "s_mov_b64 s[36:37], s[80:81]\n"
    "s_mov_b64 s[40:41], s[82:83]\n"
    "s_mov_b64 s[60:61], s[88:89]\n"
    "s_mov_b64 s[44:45], s[84:85]\n"
    "s_mov_b64 s[52:53], s[86:87]\n"
    "s_mov_b64 s[20:21], s[92:93]\n"
    "s_mov_b64 s[24:25], s[94:95]\n"
    "v_and_b32 v228, 63, v0\n"
    "v_lshrrev_b32 v231, 6, v0\n"
    "s_nop 0\n"
    "v_readfirstlane_b32 s64, v231\n"
    "s_mov_b32 s81, %0\n"
    "s_and_b32 s82, s81, 7\n"
    "s_lshl_b32 s82, s82, 2\n"
    "s_lshr_b32 s83, s81, 3\n"
    "s_and_b32 s83, s83, 3\n"
    "s_add_u32 s79, s82, s83\n"
    "s_lshr_b32 s99, s81, 5\n"
    "s_mul_i32 s65, s64, 6144\n"
    "s_and_b32 s37, s37, 0xffff\n"
    "s_mov_b32 s38, 2621440\n"
    "s_mov_b32 s39, 0x00020000\n"
    "s_and_b32 s41, s41, 0xffff\n"
    "s_mov_b32 s42, 7864320\n"
    "s_mov_b32 s43, 0x00020000\n"
    "s_and_b32 s61, s61, 0xffff\n"
    "s_mov_b32 s62, 25165824\n"
    "s_mov_b32 s63, 0x00020000\n"
    "s_cmp_ge_u32 s64, 2\n"
    "s_cselect_b64 s[20:21], s[24:25], s[20:21]\n"
    "s_and_b32 s21, s21, 0xffff\n"
    "s_mov_b32 s22, 1966080\n"
    "s_mov_b32 s23, 0x00020000\n"
    "s_lshl_b32 s29, s64, 12\n"
    "s_and_b32 s85, s64, 1\n"
    "s_mul_i32 s85, s85, 98304\n"
    "s_mul_i32 s86, s79, 96\n"
    "s_add_u32 s28, s85, s86\n"
    "s_lshl_b32 s85, s99, 4\n"
    "s_lshl_b32 s86, s64, 2\n"
    "s_add_u32 s85, s85, s86\n"
    "s_add_u32 s86, s85, 0\n"
    "s_mul_i32 s69, s86, 20480\n"
    "s_mul_i32 s87, s86, 192\n"
    "s_mul_i32 s88, s79, 6\n"
    "s_add_u32 s87, s87, s88\n"
    "s_lshl_b32 s75, s87, 10\n"
    "s_add_u32 s86, s85, 1\n"
    "s_mul_i32 s70, s86, 20480\n"
    "s_mul_i32 s87, s86, 192\n"
    "s_mul_i32 s88, s79, 6\n"
    "s_add_u32 s87, s87, s88\n"
    "s_lshl_b32 s76, s87, 10\n"
    "s_add_u32 s86, s85, 2\n"
    "s_mul_i32 s71, s86, 20480\n"
    "s_mul_i32 s87, s86, 192\n"
    "s_mul_i32 s88, s79, 6\n"
    "s_add_u32 s87, s87, s88\n"
    "s_lshl_b32 s77, s87, 10\n"
    "s_add_u32 s86, s85, 3\n"
    "s_mul_i32 s72, s86, 20480\n"
    "s_mul_i32 s87, s86, 192\n"
    "s_mul_i32 s88, s79, 6\n"
    "s_add_u32 s87, s87, s88\n"
    "s_lshl_b32 s78, s87, 10\n"
    "s_mul_i32 s86, s64, 196608\n"
    "s_mul_i32 s87, s79, 384\n"
    "s_add_u32 s73, s86, s87\n"
    "s_add_u32 s74, s73, 98304\n"
    "v_lshrrev_b32 v229, 4, v228\n"
    "v_and_b32 v230, 15, v228\n"
    "v_lshlrev_b32 v226, 4, v228\n"
    "v_lshrrev_b32 v238, 3, v228\n"
    "v_and_b32 v239, 7, v228\n"
    "v_add_u32 v231, 0, v238\n"
    "v_mul_u32_u24 v232, 43, v231\n"
    "v_lshrrev_b32 v232, 7, v232\n"
    "v_and_b32 v233, 6, v232\n"
    "v_xor_b32 v233, v233, v239\n"
    "v_lshlrev_b32 v233, 4, v233\n"
    "v_lshl_add_u32 v218, v238, 7, v233\n"
    "v_mul_u32_u24 v233, 3, v232\n"
    "v_sub_u32 v233, v231, v233\n"
    "v_lshlrev_b32 v233, 7, v233\n"
    "v_mul_u32_u24 v234, 12288, v232\n"
    "v_add_u32 v233, v233, v234\n"
    "v_and_b32 v234, 2, v232\n"
    "v_lshlrev_b32 v234, 1, v234\n"
    "v_xor_b32 v234, v234, v239\n"
    "v_lshl_add_u32 v223, v234, 4, v233\n"
    "v_add_u32 v231, 8, v238\n"
    "v_mul_u32_u24 v232, 43, v231\n"
    "v_lshrrev_b32 v232, 7, v232\n"
    "v_and_b32 v233, 6, v232\n"
    "v_xor_b32 v233, v233, v239\n"
    "v_lshlrev_b32 v233, 4, v233\n"
    "v_lshl_add_u32 v219, v238, 7, v233\n"
    "v_mul_u32_u24 v233, 3, v232\n"
    "v_sub_u32 v233, v231, v233\n"
    "v_lshlrev_b32 v233, 7, v233\n"
    "v_mul_u32_u24 v234, 12288, v232\n"
    "v_add_u32 v233, v233, v234\n"
    "v_and_b32 v234, 2, v232\n"
    "v_lshlrev_b32 v234, 1, v234\n"
    "v_xor_b32 v234, v234, v239\n"
    "v_lshl_add_u32 v224, v234, 4, v233\n"
    "v_add_u32 v231, 16, v238\n"
    "v_mul_u32_u24 v232, 43, v231\n"
    "v_lshrrev_b32 v232, 7, v232\n"
    "v_and_b32 v233, 6, v232\n"
    "v_xor_b32 v233, v233, v239\n"
    "v_lshlrev_b32 v233, 4, v233\n"
    "v_lshl_add_u32 v220, v238, 7, v233\n"
    "v_mul_u32_u24 v233, 3, v232\n"
    "v_sub_u32 v233, v231, v233\n"
    "v_lshlrev_b32 v233, 7, v233\n"
    "v_mul_u32_u24 v234, 12288, v232\n"
    "v_add_u32 v233, v233, v234\n"
    "v_and_b32 v234, 2, v232\n"
    "v_lshlrev_b32 v234, 1, v234\n"
    "v_xor_b32 v234, v234, v239\n"
    "v_lshl_add_u32 v225, v234, 4, v233\n"
    "v_lshrrev_b32 v231, 1, v238\n"
    "v_and_b32 v231, 1, v231\n"
    "v_add_u32 v231, 0, v231\n"
    "v_lshlrev_b32 v231, 1, v231\n"
    "v_xor_b32 v231, v231, v239\n"
    "v_mul_u32_u24 v232, 3072, v238\n"
    "v_lshl_add_u32 v232, v231, 4, v232\n"
    "v_add_u32 v232, 0, v232\n"
    "v_cmp_gt_u32 vcc, 6, v231\n"
    "v_mov_b32 v233, 0x40000000\n"
    "s_nop 0\n"
    "v_cndmask_b32 v5, v233, v232, vcc\n"
    "v_lshrrev_b32 v231, 1, v238\n"
    "v_and_b32 v231, 1, v231\n"
    "v_add_u32 v231, 2, v231\n"
    "v_lshlrev_b32 v231, 1, v231\n"
    "v_xor_b32 v231, v231, v239\n"
    "v_mul_u32_u24 v232, 3072, v238\n"
    "v_lshl_add_u32 v232, v231, 4, v232\n"
    "v_add_u32 v232, 23552, v232\n"
    "v_cmp_gt_u32 vcc, 6, v231\n"
    "v_mov_b32 v233, 0x40000000\n"
    "s_nop 0\n"
    "v_cndmask_b32 v6, v233, v232, vcc\n"
    "v_lshrrev_b32 v231, 1, v238\n"
    "v_and_b32 v231, 1, v231\n"
    "v_add_u32 v231, 0, v231\n"
    "v_lshlrev_b32 v231, 1, v231\n"
    "v_xor_b32 v231, v231, v239\n"
    "v_mul_u32_u24 v232, 3072, v238\n"
    "v_lshl_add_u32 v232, v231, 4, v232\n"
    "v_add_u32 v232, 47104, v232\n"
    "v_cmp_gt_u32 vcc, 6, v231\n"
    "v_mov_b32 v233, 0x40000000\n"
    "s_nop 0\n"
    "v_cndmask_b32 v7, v233, v232, vcc\n"
    "v_lshrrev_b32 v231, 1, v238\n"
    "v_and_b32 v231, 1, v231\n"
    "v_add_u32 v231, 2, v231\n"
    "v_lshlrev_b32 v231, 1, v231\n"
    "v_xor_b32 v231, v231, v239\n"
    "v_mul_u32_u24 v232, 3072, v238\n"
    "v_lshl_add_u32 v232, v231, 4, v232\n"
    "v_add_u32 v232, 70656, v232\n"
    "v_cmp_gt_u32 vcc, 6, v231\n"
    "v_mov_b32 v233, 0x40000000\n"
    "s_nop 0\n"
    "v_cndmask_b32 v8, v233, v232, vcc\n"
    "buffer_load_dwordx4 v[16:19], v226, s[36:39], s69 offen offset:0\n"
    "buffer_load_dwordx4 v[20:23], v226, s[36:39], s69 offen offset:1024\n"
    "buffer_load_dwordx4 v[24:27], v226, s[36:39], s70 offen offset:0\n"
    "buffer_load_dwordx4 v[28:31], v226, s[36:39], s70 offen offset:1024\n"
    "buffer_load_dwordx4 v[32:35], v226, s[36:39], s71 offen offset:0\n"
    "buffer_load_dwordx4 v[36:39], v226, s[36:39], s71 offen offset:1024\n"
    "buffer_load_dwordx4 v[40:43], v226, s[36:39], s72 offen offset:0\n"
    "buffer_load_dwordx4 v[44:47], v226, s[36:39], s72 offen offset:1024\n"
    "s_add_u32 s69, s69, 2048\n"
    "s_add_u32 s70, s70, 2048\n"
    "s_add_u32 s71, s71, 2048\n"
    "s_add_u32 s72, s72, 2048\n"
    "s_add_u32 m0, s65, 73728\n"
    "s_nop 0\n"
    "buffer_load_dwordx4 v223, s[40:43], s73 offen offset:0 lds\n"
    "s_add_u32 m0, s65, 74752\n"
    "s_nop 0\n"
    "buffer_load_dwordx4 v224, s[40:43], s73 offen offset:0 lds\n"
    "s_add_u32 m0, s65, 75776\n"
    "s_nop 0\n"
    "buffer_load_dwordx4 v225, s[40:43], s73 offen offset:0 lds\n"
    "s_add_u32 m0, s65, 76800\n"
    "s_nop 0\n"
    "buffer_load_dwordx4 v223, s[40:43], s74 offen offset:0 lds\n"
    "s_add_u32 m0, s65, 77824\n"
    "s_nop 0\n"
    "buffer_load_dwordx4 v224, s[40:43], s74 offen offset:0 lds\n"
    "s_add_u32 m0, s65, 78848\n"
    "s_nop 0\n"
    "buffer_load_dwordx4 v225, s[40:43], s74 offen offset:0 lds\n"
    "s_add_u32 m0, s29, 98304\n"
    "s_nop 0\n"
    "buffer_load_dwordx4 v5, s[20:23], s28 offen offset:0 lds\n"
    "buffer_load_dwordx4 v6, s[20:23], s28 offen offset:1024 lds\n"
    "buffer_load_dwordx4 v7, s[20:23], s28 offen offset:2048 lds\n"
    "buffer_load_dwordx4 v8, s[20:23], s28 offen offset:3072 lds\n"
    "s_add_u32 s73, s73, 786432\n"
    "s_add_u32 s74, s74, 786432\n"
    "s_add_u32 s28, s28, 196608\n"
    "s_mov_b64 s[24:25], s[10:11]\n"
    "s_and_b32 s25, s25, 0xffff\n"
    "s_mov_b32 s26, 6291456\n"
    "s_mov_b32 s27, 0x00020000\n"
    "s_mul_i32 s84, s79, 786432\n"
    "s_add_u32 s44, s44, s84\n"
    "s_addc_u32 s45, s45, 0\n"
    "s_add_u32 s48, s44, 4096\n"
    "s_addc_u32 s49, s45, 0\n"
    "s_and_b32 s45, s45, 0xffff\n"
    "s_and_b32 s49, s49, 0xffff\n"
    "s_mov_b32 s46, 786432\n"
    "s_mov_b32 s50, 782336\n"
    "s_mov_b32 s47, 0x00020000\n"
    "s_mov_b32 s51, 0x00020000\n"
    "s_add_u32 s52, s52, s84\n"
    "s_addc_u32 s53, s53, 0\n"
    "s_add_u32 s56, s52, 4096\n"
    "s_addc_u32 s57, s53, 0\n"
    "s_and_b32 s53, s53, 0xffff\n"
    "s_and_b32 s57, s57, 0xffff\n"
    "s_mov_b32 s54, 786432\n"
    "s_mov_b32 s58, 782336\n"
    "s_mov_b32 s55, 0x00020000\n"
    "s_mov_b32 s59, 0x00020000\n"
    "v_mul_u32_u24 v231, 384, v230\n"
    "v_and_b32 v232, 6, v230\n"
    "v_add_u32 v233, 0, v229\n"
    "v_xor_b32 v233, v233, v232\n"
    "v_lshl_add_u32 v212, v233, 4, v231\n"
    "v_add_u32 v233, 4, v229\n"
    "v_xor_b32 v233, v233, v232\n"
    "v_lshl_add_u32 v213, v233, 4, v231\n"
    "v_lshrrev_b32 v235, 2, v230\n"
    "v_and_b32 v236, 3, v228\n"
    "v_lshl_add_u32 v237, v229, 2, v235\n"
    "v_mul_u32_u24 v231, 384, v237\n"
    "v_and_b32 v232, 1, v236\n"
    "v_lshl_add_u32 v231, v232, 3, v231\n"
    "v_add_u32 v231, 49152, v231\n"
    "v_and_b32 v232, 6, v237\n"
    "v_lshrrev_b32 v234, 1, v236\n"
    "v_add_u32 v233, 0, v234\n"
    "v_xor_b32 v233, v233, v232\n"
    "v_lshl_add_u32 v214, v233, 4, v231\n"
    "v_add_u32 v233, 2, v234\n"
    "v_xor_b32 v233, v233, v232\n"
    "v_lshl_add_u32 v215, v233, 4, v231\n"
    "v_add_u32 v233, 4, v234\n"
    "v_xor_b32 v233, v233, v232\n"
    "v_lshl_add_u32 v216, v233, 4, v231\n"
    "v_add_u32 v233, 6, v234\n"
    "v_xor_b32 v233, v233, v232\n"
    "v_lshl_add_u32 v217, v233, 4, v231\n"
    "v_lshl_add_u32 v237, v229, 3, v235\n"
    "v_mul_u32_u24 v231, 384, v237\n"
    "v_add_u32 v231, 73728, v231\n"
    "v_and_b32 v232, 2, v235\n"
    "v_lshlrev_b32 v232, 1, v232\n"
    "v_add_u32 v233, 0, v236\n"
    "v_xor_b32 v233, v233, v232\n"
    "v_lshl_add_u32 v221, v233, 4, v231\n"
    "v_add_u32 v233, 4, v236\n"
    "v_xor_b32 v233, v233, v232\n"
    "v_lshl_add_u32 v222, v233, 4, v231\n"
    "v_lshl_add_u32 v237, v229, 3, v235\n"
    "v_lshlrev_b32 v231, 7, v237\n"
    "v_lshlrev_b32 v232, 3, v236\n"
    "v_add_u32 v231, v231, v232\n"
    "v_add_u32 v231, 98304, v231\n"
    "v_lshrrev_b32 v232, 1, v235\n"
    "v_and_b32 v233, 1, v229\n"
    "v_lshl_add_u32 v232, v233, 1, v232\n"
    "v_xor_b32 v233, 0, v232\n"
    "v_lshl_add_u32 v2, v233, 5, v231\n"
    "v_xor_b32 v233, 1, v232\n"
    "v_lshl_add_u32 v3, v233, 5, v231\n"
    "v_xor_b32 v233, 2, v232\n"
    "v_lshl_add_u32 v4, v233, 5, v231\n"
    "v_mov_b32 v234, 0x40000000\n"
    "v_mul_u32_u24 v231, 43, v228\n"
    "v_lshrrev_b32 v231, 7, v231\n"
    "v_mul_u32_u24 v232, 3, v231\n"
    "v_sub_u32 v232, v228, v232\n"
    "v_lshlrev_b32 v232, 7, v232\n"
    "v_mul_u32_u24 v231, 12288, v231\n"
    "v_add_u32 v231, v231, v232\n"
    "v_cmp_gt_u32 vcc, 6, v228\n"
    "s_nop 0\n"
    "s_nop 0\n"
    "v_cndmask_b32 v10, v234, v231, vcc\n"
    "v_lshrrev_b32 v231, 1, v228\n"
    "v_mul_u32_u24 v231, 3072, v231\n"
    "v_and_b32 v232, 1, v228\n"
    "v_mul_u32_u24 v232, 92, v232\n"
    "v_add_u32 v231, v231, v232\n"
    "v_cmp_gt_u32 vcc, 8, v228\n"
    "s_nop 0\n"
    "s_nop 0\n"
    "v_cndmask_b32 v11, v234, v231, vcc\n"
    "s_lshl_b32 s85, s99, 3\n"
    "s_add_u32 s85, s85, 64\n"
    "s_lshl_b32 s86, s64, 1\n"
    "s_add_u32 s86, s86, s85\n"
    "s_mul_i32 s86, s86, 12288\n"
    "s_mul_i32 s87, s79, 384\n"
    "s_add_u32 s96, s86, s87\n"
    "s_and_b32 s86, s64, 1\n"
    "s_lshl_b32 s86, s86, 2\n"
    "s_add_u32 s86, s86, s85\n"
    "s_mul_i32 s86, s86, 3072\n"
    "s_mul_i32 s87, s79, 96\n"
    "s_add_u32 s97, s86, s87\n"
    "v_lshlrev_b32 v231, 7, v228\n"
    "v_cmp_lt_u32 vcc, 11, v228\n"
    "v_add_u32 v232, 23040, v231\n"
    "s_nop 0\n"
    "v_cndmask_b32 v232, v231, v232, vcc\n"
    "v_cmp_gt_u32 vcc, 24, v228\n"
    "s_nop 0\n"
    "s_nop 0\n"
    "v_cndmask_b32 v9, v234, v232, vcc\n"
    "v_cmp_gt_u32 vcc, 4, v228\n"
    "s_nop 0\n"
    "s_nop 0\n"
    "v_cndmask_b32 v12, v234, v231, vcc\n"
    "v_cmp_gt_u32 vcc, 1, v228\n"
    "s_nop 0\n"
    "s_nop 0\n"
    "v_cndmask_b32 v13, v234, v231, vcc\n"
    "v_cmp_gt_u32 vcc, 11, v228\n"
    "s_nop 0\n"
    "s_nop 0\n"
    "v_cndmask_b32 v14, v234, v231, vcc\n"
    "s_cmp_ge_u32 s64, 2\n"
    "s_cselect_b64 s[12:13], s[52:53], s[44:45]\n"
    "s_cselect_b32 s85, 1, 2\n"
    "s_mul_i32 s85, s85, 24576\n"
    "s_mov_b32 s14, 786432\n"
    "s_mov_b32 s15, 0x00020000\n"
    "s_mul_i32 s86, s99, 3072\n"
    "s_add_u32 s85, s85, s86\n"
    "s_and_b32 s86, s64, 1\n"
    "s_mul_i32 s86, s86, 1536\n"
    "s_add_u32 s3, s85, s86\n"
    "v_mov_b32 v208, 0x3f803f80\n"
    "v_mov_b32 v209, 0x3f803f80\n"
    "v_mov_b32 v210, 0x3f803f80\n"
    "v_mov_b32 v211, 0x3f803f80\n"
    "s_add_u32 s66, s65, 49152\n"
    "s_add_u32 s67, s65, 24576\n"
    "s_add_u32 s80, s65, 24576\n"
    "s_mov_b32 s98, 0\n"
    "buffer_load_dword v15, v10, s[40:43], s96 offen offset:0\n"
    "buffer_load_dword v15, v11, s[20:23], s97 offen offset:0\n"
    "s_add_u32 s96, s96, 786432\n"
    "s_add_u32 s97, s97, 196608\n"
    "buffer_load_dword v15, v10, s[40:43], s96 offen offset:0\n"
    "buffer_load_dword v15, v11, s[20:23], s97 offen offset:0\n"
    "s_add_u32 s96, s96, 786432\n"
    "s_add_u32 s97, s97, 196608\n"
    "s_waitcnt vmcnt(4)\n"
    "s_barrier\n"
    "ds_read_b64_tr_b16 a[192:193], v221 offset:0\n"
    "ds_read_b64_tr_b16 a[194:195], v221 offset:1536\n"
    "ds_read_b64_tr_b16 a[196:197], v221 offset:12288\n"
    "ds_read_b64_tr_b16 a[198:199], v221 offset:13824\n"
    "ds_read_b64_tr_b16 a[200:201], v221 offset:8\n"
    "ds_read_b64_tr_b16 a[202:203], v221 offset:1544\n"
    "ds_read_b64_tr_b16 a[204:205], v221 offset:12296\n"
    "ds_read_b64_tr_b16 a[206:207], v221 offset:13832\n"
    "; Q chunk 0\n"
    "s_waitcnt lgkmcnt(4)\n"
    "ds_read_b64_tr_b16 a[208:209], v222 offset:0\n"
    "v_mfma_f32_16x16x32_bf16 a[0:3], a[192:195], v[16:19], 0\n"
    "ds_read_b64_tr_b16 a[210:211], v222 offset:1536\n"
    "s_add_u32 m0, s65, 114688\n"
    "v_mfma_f32_16x16x32_bf16 a[0:3], a[196:199], v[20:23], a[0:3]\n"
    "ds_read_b64_tr_b16 a[212:213], v222 offset:12288\n"
    "buffer_load_dwordx4 v223, s[40:43], s73 offen offset:0 lds\n"
    "v_mfma_f32_16x16x32_bf16 a[4:7], a[192:195], v[24:27], 0\n"
    "ds_read_b64_tr_b16 a[214:215], v222 offset:13824\n"
    "v_mfma_f32_16x16x32_bf16 a[4:7], a[196:199], v[28:31], a[4:7]\n"
    "ds_read_b64_tr_b16 a[216:217], v222 offset:8\n"
    "v_mfma_f32_16x16x32_bf16 a[8:11], a[192:195], v[32:35], 0\n"
    "ds_read_b64_tr_b16 a[218:219], v222 offset:1544\n"
    "s_add_u32 m0, s65, 115712\n"
    "v_mfma_f32_16x16x32_bf16 a[8:11], a[196:199], v[36:39], a[8:11]\n"
    "ds_read_b64_tr_b16 a[220:221], v222 offset:12296\n"
    "buffer_load_dwordx4 v224, s[40:43], s73 offen offset:0 lds\n"
    "v_mfma_f32_16x16x32_bf16 a[12:15], a[192:195], v[40:43], 0\n"
    "ds_read_b64_tr_b16 a[222:223], v222 offset:13832\n"
    "v_mfma_f32_16x16x32_bf16 a[12:15], a[196:199], v[44:47], a[12:15]\n"
    "s_waitcnt lgkmcnt(8)\n"
    "v_mfma_f32_16x16x32_bf16 a[16:19], a[200:203], v[16:19], 0\n"
    "ds_read_b64_tr_b16 a[224:225], v221 offset:128\n"
    "v_mfma_f32_16x16x32_bf16 a[16:19], a[204:207], v[20:23], a[16:19]\n"
    "ds_read_b64_tr_b16 a[226:227], v221 offset:1664\n"
    "s_add_u32 m0, s65, 116736\n"
    "v_mfma_f32_16x16x32_bf16 a[20:23], a[200:203], v[24:27], 0\n"
    "ds_read_b64_tr_b16 a[228:229], v221 offset:12416\n"
    "v_mfma_f32_16x16x32_bf16 a[20:23], a[204:207], v[28:31], a[20:23]\n"
    "ds_read_b64_tr_b16 a[230:231], v221 offset:13952\n"
    "v_mfma_f32_16x16x32_bf16 a[24:27], a[200:203], v[32:35], 0\n"
    "buffer_load_dwordx4 v225, s[40:43], s73 offen offset:0 lds\n"
    "v_mfma_f32_16x16x32_bf16 a[24:27], a[204:207], v[36:39], a[24:27]\n"
    "v_mfma_f32_16x16x32_bf16 a[28:31], a[200:203], v[40:43], 0\n"
    "s_add_u32 m0, s65, 117760\n"
    "s_nop 0\n"
    "buffer_load_dwordx4 v223, s[40:43], s74 offen offset:0 lds\n"
    "v_mfma_f32_16x16x32_bf16 a[28:31], a[204:207], v[44:47], a[28:31]\n"
    "s_waitcnt lgkmcnt(8)\n"
    "v_mfma_f32_16x16x32_bf16 a[32:35], a[208:211], v[16:19], 0\n"
    "ds_read_b64_tr_b16 a[232:233], v221 offset:136\n"
    "v_mfma_f32_16x16x32_bf16 a[32:35], a[212:215], v[20:23], a[32:35]\n"
    "ds_read_b64_tr_b16 a[234:235], v221 offset:1672\n"
    "v_mfma_f32_16x16x32_bf16 a[36:39], a[208:211], v[24:27], 0\n"
    "ds_read_b64_tr_b16 a[236:237], v221 offset:12424\n"
    "v_mfma_f32_16x16x32_bf16 a[36:39], a[212:215], v[28:31], a[36:39]\n"
    "ds_read_b64_tr_b16 a[238:239], v221 offset:13960\n"
    "s_add_u32 m0, s65, 118784\n"
    "v_mfma_f32_16x16x32_bf16 a[40:43], a[208:211], v[32:35], 0\n"
    "buffer_load_dwordx4 v224, s[40:43], s74 offen offset:0 lds\n"
    "v_mfma_f32_16x16x32_bf16 a[40:43], a[212:215], v[36:39], a[40:43]\n"
    "v_mfma_f32_16x16x32_bf16 a[44:47], a[208:211], v[40:43], 0\n"
    "v_mfma_f32_16x16x32_bf16 a[44:47], a[212:215], v[44:47], a[44:47]\n"
    "s_waitcnt lgkmcnt(8)\n"
    "v_mfma_f32_16x16x32_bf16 a[48:51], a[216:219], v[16:19], 0\n"
    "ds_read_b64_tr_b16 a[192:193], v222 offset:128\n"
    "s_add_u32 m0, s65, 119808\n"
    "v_mfma_f32_16x16x32_bf16 a[48:51], a[220:223], v[20:23], a[48:51]\n"
    "ds_read_b64_tr_b16 a[194:195], v222 offset:1664\n"
    "v_mfma_f32_16x16x32_bf16 a[52:55], a[216:219], v[24:27], 0\n"
    "ds_read_b64_tr_b16 a[196:197], v222 offset:12416\n"
    "v_mfma_f32_16x16x32_bf16 a[52:55], a[220:223], v[28:31], a[52:55]\n"
    "ds_read_b64_tr_b16 a[198:199], v222 offset:13952\n"
    "v_mfma_f32_16x16x32_bf16 a[56:59], a[216:219], v[32:35], 0\n"
    "buffer_load_dwordx4 v225, s[40:43], s74 offen offset:0 lds\n"
    "v_mfma_f32_16x16x32_bf16 a[56:59], a[220:223], v[36:39], a[56:59]\n"
    "s_add_u32 m0, s29, 139264\n"
    "v_mfma_f32_16x16x32_bf16 a[60:63], a[216:219], v[40:43], 0\n"
    "buffer_load_dwordx4 v5, s[20:23], s28 offen offset:0 lds\n"
    "v_mfma_f32_16x16x32_bf16 a[60:63], a[220:223], v[44:47], a[60:63]\n"
    "s_waitcnt lgkmcnt(8)\n"
    "v_mfma_f32_16x16x32_bf16 a[64:67], a[224:227], v[16:19], 0\n"
    "ds_read_b64_tr_b16 a[200:201], v222 offset:136\n"
    "v_mfma_f32_16x16x32_bf16 a[64:67], a[228:231], v[20:23], a[64:67]\n"
    "ds_read_b64_tr_b16 a[202:203], v222 offset:1672\n"
    "v_mfma_f32_16x16x32_bf16 a[68:71], a[224:227], v[24:27], 0\n"
    "ds_read_b64_tr_b16 a[204:205], v222 offset:12424\n"
    "v_mfma_f32_16x16x32_bf16 a[68:71], a[228:231], v[28:31], a[68:71]\n"
    "ds_read_b64_tr_b16 a[206:207], v222 offset:13960\n"
    "v_mfma_f32_16x16x32_bf16 a[72:75], a[224:227], v[32:35], 0\n"
    "buffer_load_dwordx4 v6, s[20:23], s28 offen offset:1024 lds\n"
    "v_mfma_f32_16x16x32_bf16 a[72:75], a[228:231], v[36:39], a[72:75]\n"
    "v_mfma_f32_16x16x32_bf16 a[76:79], a[224:227], v[40:43], 0\n"
    "buffer_load_dwordx4 v7, s[20:23], s28 offen offset:2048 lds\n"
    "v_mfma_f32_16x16x32_bf16 a[76:79], a[228:231], v[44:47], a[76:79]\n"
    "s_waitcnt lgkmcnt(8)\n"
    "v_mfma_f32_16x16x32_bf16 a[80:83], a[232:235], v[16:19], 0\n"
    "ds_read_b64_tr_b16 a[208:209], v221 offset:256\n"
    "v_mfma_f32_16x16x32_bf16 a[80:83], a[236:239], v[20:23], a[80:83]\n"
    "ds_read_b64_tr_b16 a[210:211], v221 offset:1792\n"
    "v_mfma_f32_16x16x32_bf16 a[84:87], a[232:235], v[24:27], 0\n"
    "ds_read_b64_tr_b16 a[212:213], v221 offset:12544\n"
    "v_mfma_f32_16x16x32_bf16 a[84:87], a[236:239], v[28:31], a[84:87]\n"
    "ds_read_b64_tr_b16 a[214:215], v221 offset:14080\n"
    "v_mfma_f32_16x16x32_bf16 a[88:91], a[232:235], v[32:35], 0\n"
    "buffer_load_dwordx4 v8, s[20:23], s28 offen offset:3072 lds\n"
    "v_mfma_f32_16x16x32_bf16 a[88:91], a[236:239], v[36:39], a[88:91]\n"
    "v_mfma_f32_16x16x32_bf16 a[92:95], a[232:235], v[40:43], 0\n"
    "v_mfma_f32_16x16x32_bf16 a[92:95], a[236:239], v[44:47], a[92:95]\n"
    "s_waitcnt lgkmcnt(8)\n"
    "v_mfma_f32_16x16x32_bf16 a[96:99], a[192:195], v[16:19], 0\n"
    "ds_read_b64_tr_b16 a[216:217], v221 offset:264\n"
    "s_add_u32 s73, s73, 786432\n"
    "v_mfma_f32_16x16x32_bf16 a[96:99], a[196:199], v[20:23], a[96:99]\n"
    "ds_read_b64_tr_b16 a[218:219], v221 offset:1800\n"
    "s_add_u32 s74, s74, 786432\n"
    "v_mfma_f32_16x16x32_bf16 a[100:103], a[192:195], v[24:27], 0\n"
    "ds_read_b64_tr_b16 a[220:221], v221 offset:12552\n"
    "s_add_u32 s28, s28, 196608\n"
    "v_mfma_f32_16x16x32_bf16 a[100:103], a[196:199], v[28:31], a[100:103]\n"
    "ds_read_b64_tr_b16 a[222:223], v221 offset:14088\n"
    "v_mfma_f32_16x16x32_bf16 a[104:107], a[192:195], v[32:35], 0\n"
    "buffer_load_dwordx4 v[48:51], v226, s[36:39], s69 offen offset:0\n"
    "v_mfma_f32_16x16x32_bf16 a[104:107], a[196:199], v[36:39], a[104:107]\n"
    "buffer_load_dwordx4 v[52:55], v226, s[36:39], s69 offen offset:1024\n"
    "v_mfma_f32_16x16x32_bf16 a[108:111], a[192:195], v[40:43], 0\n"
    "v_mfma_f32_16x16x32_bf16 a[108:111], a[196:199], v[44:47], a[108:111]\n"
    "s_waitcnt lgkmcnt(8)\n"
    "v_mfma_f32_16x16x32_bf16 a[112:115], a[200:203], v[16:19], 0\n"
    "ds_read_b64_tr_b16 a[224:225], v222 offset:256\n"
    "v_mfma_f32_16x16x32_bf16 a[112:115], a[204:207], v[20:23], a[112:115]\n"
    "ds_read_b64_tr_b16 a[226:227], v222 offset:1792\n"
    "v_mfma_f32_16x16x32_bf16 a[116:119], a[200:203], v[24:27], 0\n"
    "ds_read_b64_tr_b16 a[228:229], v222 offset:12544\n"
    "v_mfma_f32_16x16x32_bf16 a[116:119], a[204:207], v[28:31], a[116:119]\n"
    "ds_read_b64_tr_b16 a[230:231], v222 offset:14080\n"
    "v_mfma_f32_16x16x32_bf16 a[120:123], a[200:203], v[32:35], 0\n"
    "buffer_load_dwordx4 v[56:59], v226, s[36:39], s70 offen offset:0\n"
    "v_mfma_f32_16x16x32_bf16 a[120:123], a[204:207], v[36:39], a[120:123]\n"
    "v_mfma_f32_16x16x32_bf16 a[124:127], a[200:203], v[40:43], 0\n"
    "s_waitcnt lgkmcnt(8)\n"
    "v_mfma_f32_16x16x32_bf16 a[124:127], a[204:207], v[44:47], a[124:127]\n"
    "buffer_load_dwordx4 v[60:63], v226, s[36:39], s70 offen offset:1024\n"
    "v_mfma_f32_16x16x32_bf16 a[128:131], a[208:211], v[16:19], 0\n"
    "ds_read_b64_tr_b16 a[232:233], v222 offset:264\n"
    "v_mfma_f32_16x16x32_bf16 a[128:131], a[212:215], v[20:23], a[128:131]\n"
    "ds_read_b64_tr_b16 a[234:235], v222 offset:1800\n"
    "v_mfma_f32_16x16x32_bf16 a[132:135], a[208:211], v[24:27], 0\n"
    "ds_read_b64_tr_b16 a[236:237], v222 offset:12552\n"
    "v_mfma_f32_16x16x32_bf16 a[132:135], a[212:215], v[28:31], a[132:135]\n"
    "ds_read_b64_tr_b16 a[238:239], v222 offset:14088\n"
    "v_mfma_f32_16x16x32_bf16 a[136:139], a[208:211], v[32:35], 0\n"
    "buffer_load_dwordx4 v[64:67], v226, s[36:39], s71 offen offset:0\n"
    "v_mfma_f32_16x16x32_bf16 a[136:139], a[212:215], v[36:39], a[136:139]\n"
    "v_mfma_f32_16x16x32_bf16 a[140:143], a[208:211], v[40:43], 0\n"
    "v_mfma_f32_16x16x32_bf16 a[140:143], a[212:215], v[44:47], a[140:143]\n"
    "s_waitcnt lgkmcnt(8)\n"
    "v_mfma_f32_16x16x32_bf16 a[144:147], a[216:219], v[16:19], 0\n"
    "ds_read_b64_tr_b16 a[192:193], v2 offset:0\n"
    "v_mfma_f32_16x16x32_bf16 a[144:147], a[220:223], v[20:23], a[144:147]\n"
    "ds_read_b64_tr_b16 a[194:195], v2 offset:512\n"
    "v_mfma_f32_16x16x32_bf16 a[148:151], a[216:219], v[24:27], 0\n"
    "ds_read_b64_tr_b16 a[196:197], v2 offset:4096\n"
    "v_mfma_f32_16x16x32_bf16 a[148:151], a[220:223], v[28:31], a[148:151]\n"
    "ds_read_b64_tr_b16 a[198:199], v2 offset:4608\n"
    "v_mfma_f32_16x16x32_bf16 a[152:155], a[216:219], v[32:35], 0\n"
    "buffer_load_dwordx4 v[68:71], v226, s[36:39], s71 offen offset:1024\n"
    "v_mfma_f32_16x16x32_bf16 a[152:155], a[220:223], v[36:39], a[152:155]\n"
    "buffer_load_dwordx4 v[72:75], v226, s[36:39], s72 offen offset:0\n"
    "v_mfma_f32_16x16x32_bf16 a[156:159], a[216:219], v[40:43], 0\n"
    "v_mfma_f32_16x16x32_bf16 a[156:159], a[220:223], v[44:47], a[156:159]\n"
    "s_waitcnt lgkmcnt(8)\n"
    "v_mfma_f32_16x16x32_bf16 a[160:163], a[224:227], v[16:19], 0\n"
    "ds_read_b64_tr_b16 a[200:201], v2 offset:8192\n"
    "v_mfma_f32_16x16x32_bf16 a[160:163], a[228:231], v[20:23], a[160:163]\n"
    "ds_read_b64_tr_b16 a[202:203], v2 offset:8704\n"
    "v_mfma_f32_16x16x32_bf16 a[164:167], a[224:227], v[24:27], 0\n"
    "ds_read_b64_tr_b16 a[204:205], v2 offset:12288\n"
    "v_mfma_f32_16x16x32_bf16 a[164:167], a[228:231], v[28:31], a[164:167]\n"
    "ds_read_b64_tr_b16 a[206:207], v2 offset:12800\n"
    "v_mfma_f32_16x16x32_bf16 a[168:171], a[224:227], v[32:35], 0\n"
    "buffer_load_dwordx4 v[76:79], v226, s[36:39], s72 offen offset:1024\n"
    "v_mfma_f32_16x16x32_bf16 a[168:171], a[228:231], v[36:39], a[168:171]\n"
    "v_mfma_f32_16x16x32_bf16 a[172:175], a[224:227], v[40:43], 0\n"
    "v_mfma_f32_16x16x32_bf16 a[172:175], a[228:231], v[44:47], a[172:175]\n"
    "s_waitcnt lgkmcnt(8)\n"
    "s_add_u32 s69, s69, 2048\n"
    "v_mfma_f32_16x16x32_bf16 a[176:179], a[232:235], v[16:19], 0\n"
    "ds_read_b64_tr_b16 a[208:209], v3 offset:0\n"
    "s_add_u32 s70, s70, 2048\n"
    "v_mfma_f32_16x16x32_bf16 a[176:179], a[236:239], v[20:23], a[176:179]\n"
    "ds_read_b64_tr_b16 a[210:211], v3 offset:512\n"
    "s_add_u32 s71, s71, 2048\n"
    "v_mfma_f32_16x16x32_bf16 a[180:183], a[232:235], v[24:27], 0\n"
    "ds_read_b64_tr_b16 a[212:213], v3 offset:4096\n"
    "s_add_u32 s72, s72, 2048\n"
    "v_mfma_f32_16x16x32_bf16 a[180:183], a[236:239], v[28:31], a[180:183]\n"
    "ds_read_b64_tr_b16 a[214:215], v3 offset:4608\n"
    "buffer_load_dword v15, v10, s[40:43], s96 offen offset:0\n"
    "v_mfma_f32_16x16x32_bf16 a[184:187], a[232:235], v[32:35], 0\n"
    "buffer_load_dword v15, v11, s[20:23], s97 offen offset:0\n"
    "v_mfma_f32_16x16x32_bf16 a[184:187], a[236:239], v[36:39], a[184:187]\n"
    "v_mfma_f32_16x16x32_bf16 a[188:191], a[232:235], v[40:43], 0\n"
    "v_mfma_f32_16x16x32_bf16 a[188:191], a[236:239], v[44:47], a[188:191]\n"
    "s_waitcnt lgkmcnt(8)\n"
    "v_mfma_f32_16x16x32_bf16 v[112:115], a[192:195], v[16:19], 0\n"
    "ds_read_b64_tr_b16 a[216:217], v3 offset:8192\n"
    "v_mfma_f32_16x16x32_bf16 v[112:115], a[196:199], v[20:23], v[112:115]\n"
    "ds_read_b64_tr_b16 a[218:219], v3 offset:8704\n"
    "s_add_u32 s96, s96, 786432\n"
    "v_mfma_f32_16x16x32_bf16 v[116:119], a[192:195], v[24:27], 0\n"
    "ds_read_b64_tr_b16 a[220:221], v3 offset:12288\n"
    "s_add_u32 s97, s97, 196608\n"
    "v_mfma_f32_16x16x32_bf16 v[116:119], a[196:199], v[28:31], v[116:119]\n"
    "ds_read_b64_tr_b16 a[222:223], v3 offset:12800\n"
    "v_mfma_f32_16x16x32_bf16 v[120:123], a[192:195], v[32:35], 0\n"
    "v_mfma_f32_16x16x32_bf16 v[120:123], a[196:199], v[36:39], v[120:123]\n"
    "v_mfma_f32_16x16x32_bf16 v[124:127], a[192:195], v[40:43], 0\n"
    "v_mfma_f32_16x16x32_bf16 v[124:127], a[196:199], v[44:47], v[124:127]\n"
    "s_waitcnt lgkmcnt(8)\n"
    "v_mfma_f32_16x16x32_bf16 v[128:131], a[200:203], v[16:19], 0\n"
    "ds_read_b64_tr_b16 a[224:225], v4 offset:0\n"
    "v_mfma_f32_16x16x32_bf16 v[128:131], a[204:207], v[20:23], v[128:131]\n"
    "ds_read_b64_tr_b16 a[226:227], v4 offset:512\n"
    "v_mfma_f32_16x16x32_bf16 v[132:135], a[200:203], v[24:27], 0\n"
    "ds_read_b64_tr_b16 a[228:229], v4 offset:4096\n"
    "v_mfma_f32_16x16x32_bf16 v[132:135], a[204:207], v[28:31], v[132:135]\n"
    "ds_read_b64_tr_b16 a[230:231], v4 offset:4608\n"
    "v_mfma_f32_16x16x32_bf16 v[136:139], a[200:203], v[32:35], 0\n"
    "v_mfma_f32_16x16x32_bf16 v[136:139], a[204:207], v[36:39], v[136:139]\n"
    "v_mfma_f32_16x16x32_bf16 v[140:143], a[200:203], v[40:43], 0\n"
    "v_mfma_f32_16x16x32_bf16 v[140:143], a[204:207], v[44:47], v[140:143]\n"
    "s_waitcnt lgkmcnt(8)\n"
    "v_mfma_f32_16x16x32_bf16 v[144:147], a[208:211], v[16:19], 0\n"
    "ds_read_b64_tr_b16 a[232:233], v4 offset:8192\n"
    "v_mfma_f32_16x16x32_bf16 v[144:147], a[212:215], v[20:23], v[144:147]\n"
    "ds_read_b64_tr_b16 a[234:235], v4 offset:8704\n"
    "v_mfma_f32_16x16x32_bf16 v[148:151], a[208:211], v[24:27], 0\n"
    "ds_read_b64_tr_b16 a[236:237], v4 offset:12288\n"
    "v_mfma_f32_16x16x32_bf16 v[148:151], a[212:215], v[28:31], v[148:151]\n"
    "ds_read_b64_tr_b16 a[238:239], v4 offset:12800\n"
    "v_mfma_f32_16x16x32_bf16 v[152:155], a[208:211], v[32:35], 0\n"
    "v_mfma_f32_16x16x32_bf16 v[152:155], a[212:215], v[36:39], v[152:155]\n"
    "v_mfma_f32_16x16x32_bf16 v[156:159], a[208:211], v[40:43], 0\n"
    "v_mfma_f32_16x16x32_bf16 v[156:159], a[212:215], v[44:47], v[156:159]\n"
    "s_waitcnt lgkmcnt(8)\n"
    "v_mfma_f32_16x16x32_bf16 v[160:163], a[216:219], v[16:19], 0\n"
    "v_mfma_f32_16x16x32_bf16 v[160:163], a[220:223], v[20:23], v[160:163]\n"
    "v_mfma_f32_16x16x32_bf16 v[164:167], a[216:219], v[24:27], 0\n"
    "v_mfma_f32_16x16x32_bf16 v[164:167], a[220:223], v[28:31], v[164:167]\n"
    "v_mfma_f32_16x16x32_bf16 v[168:171], a[216:219], v[32:35], 0\n"
    "v_mfma_f32_16x16x32_bf16 v[168:171], a[220:223], v[36:39], v[168:171]\n"
    "v_mfma_f32_16x16x32_bf16 v[172:175], a[216:219], v[40:43], 0\n"
    "v_mfma_f32_16x16x32_bf16 v[172:175], a[220:223], v[44:47], v[172:175]\n"
    "s_waitcnt vmcnt(10)\n"
    "s_waitcnt lgkmcnt(0)\n"
    "s_barrier\n"
    "v_mfma_f32_16x16x32_bf16 v[176:179], a[224:227], v[16:19], 0\n"
    "ds_read_b64_tr_b16 a[192:193], v221 offset:40960\n"
    "v_mfma_f32_16x16x32_bf16 v[176:179], a[228:231], v[20:23], v[176:179]\n"
    "ds_read_b64_tr_b16 a[194:195], v221 offset:42496\n"
    "v_mfma_f32_16x16x32_bf16 v[180:183], a[224:227], v[24:27], 0\n"
    "ds_read_b64_tr_b16 a[196:197], v221 offset:53248\n"
    "v_mfma_f32_16x16x32_bf16 v[180:183], a[228:231], v[28:31], v[180:183]\n"
    "ds_read_b64_tr_b16 a[198:199], v221 offset:54784\n"
    "v_mfma_f32_16x16x32_bf16 v[184:187], a[224:227], v[32:35], 0\n"
    "ds_read_b64_tr_b16 a[200:201], v221 offset:40968\n"
    "v_mfma_f32_16x16x32_bf16 v[184:187], a[228:231], v[36:39], v[184:187]\n"
    "ds_read_b64_tr_b16 a[202:203], v221 offset:42504\n"
    "v_mfma_f32_16x16x32_bf16 v[188:191], a[224:227], v[40:43], 0\n"
    "ds_read_b64_tr_b16 a[204:205], v221 offset:53256\n"
    "v_mfma_f32_16x16x32_bf16 v[188:191], a[228:231], v[44:47], v[188:191]\n"
    "ds_read_b64_tr_b16 a[206:207], v221 offset:54792\n"
    "v_mfma_f32_16x16x32_bf16 v[192:195], a[232:235], v[16:19], 0\n"
    "v_mfma_f32_16x16x32_bf16 v[192:195], a[236:239], v[20:23], v[192:195]\n"
    "v_mfma_f32_16x16x32_bf16 v[196:199], a[232:235], v[24:27], 0\n"
    "v_mfma_f32_16x16x32_bf16 v[196:199], a[236:239], v[28:31], v[196:199]\n"
    "v_mfma_f32_16x16x32_bf16 v[200:203], a[232:235], v[32:35], 0\n"
    "v_mfma_f32_16x16x32_bf16 v[200:203], a[236:239], v[36:39], v[200:203]\n"
    "v_mfma_f32_16x16x32_bf16 v[204:207], a[232:235], v[40:43], 0\n"
    "v_mfma_f32_16x16x32_bf16 v[204:207], a[236:239], v[44:47], v[204:207]\n"
    "s_mov_b32 s68, 2\n"
    "L_qloop:\n"
    "s_waitcnt lgkmcnt(4)\n"
    "ds_read_b64_tr_b16 a[208:209], v222 offset:40960\n"
    "s_waitcnt vmcnt(2)\n"
    "v_mfma_f32_16x16x32_bf16 a[0:3], a[192:195], v[48:51], a[0:3]\n"
    "ds_read_b64_tr_b16 a[210:211], v222 offset:42496\n"
    "s_add_u32 m0, s65, 73728\n"
    "v_mfma_f32_16x16x32_bf16 a[0:3], a[196:199], v[52:55], a[0:3]\n"
    "ds_read_b64_tr_b16 a[212:213], v222 offset:53248\n"
    "buffer_load_dwordx4 v223, s[40:43], s73 offen offset:0 lds\n"
    "v_mfma_f32_16x16x32_bf16 a[4:7], a[192:195], v[56:59], a[4:7]\n"
    "ds_read_b64_tr_b16 a[214:215], v222 offset:54784\n"
    "v_mfma_f32_16x16x32_bf16 a[4:7], a[196:199], v[60:63], a[4:7]\n"
    "ds_read_b64_tr_b16 a[216:217], v222 offset:40968\n"
    "v_mfma_f32_16x16x32_bf16 a[8:11], a[192:195], v[64:67], a[8:11]\n"
    "ds_read_b64_tr_b16 a[218:219], v222 offset:42504\n"
    "s_add_u32 m0, s65, 74752\n"
    "v_mfma_f32_16x16x32_bf16 a[8:11], a[196:199], v[68:71], a[8:11]\n"
    "ds_read_b64_tr_b16 a[220:221], v222 offset:53256\n"
    "buffer_load_dwordx4 v224, s[40:43], s73 offen offset:0 lds\n"
    "v_mfma_f32_16x16x32_bf16 a[12:15], a[192:195], v[72:75], a[12:15]\n"
    "ds_read_b64_tr_b16 a[222:223], v222 offset:54792\n"
    "v_mfma_f32_16x16x32_bf16 a[12:15], a[196:199], v[76:79], a[12:15]\n"
    "s_waitcnt lgkmcnt(8)\n"
    "v_mfma_f32_16x16x32_bf16 a[16:19], a[200:203], v[48:51], a[16:19]\n"
    "ds_read_b64_tr_b16 a[224:225], v221 offset:41088\n"
    "v_mfma_f32_16x16x32_bf16 a[16:19], a[204:207], v[52:55], a[16:19]\n"
    "ds_read_b64_tr_b16 a[226:227], v221 offset:42624\n"
    "s_add_u32 m0, s65, 75776\n"
    "v_mfma_f32_16x16x32_bf16 a[20:23], a[200:203], v[56:59], a[20:23]\n"
    "ds_read_b64_tr_b16 a[228:229], v221 offset:53376\n"
    "v_mfma_f32_16x16x32_bf16 a[20:23], a[204:207], v[60:63], a[20:23]\n"
    "ds_read_b64_tr_b16 a[230:231], v221 offset:54912\n"
    "v_mfma_f32_16x16x32_bf16 a[24:27], a[200:203], v[64:67], a[24:27]\n"
    "buffer_load_dwordx4 v225, s[40:43], s73 offen offset:0 lds\n"
    "v_mfma_f32_16x16x32_bf16 a[24:27], a[204:207], v[68:71], a[24:27]\n"
    "v_mfma_f32_16x16x32_bf16 a[28:31], a[200:203], v[72:75], a[28:31]\n"
    "s_add_u32 m0, s65, 76800\n"
    "s_nop 0\n"
    "buffer_load_dwordx4 v223, s[40:43], s74 offen offset:0 lds\n"
    "v_mfma_f32_16x16x32_bf16 a[28:31], a[204:207], v[76:79], a[28:31]\n"
    "s_waitcnt lgkmcnt(8)\n"
    "v_mfma_f32_16x16x32_bf16 a[32:35], a[208:211], v[48:51], a[32:35]\n"
    "ds_read_b64_tr_b16 a[232:233], v221 offset:41096\n"
    "v_mfma_f32_16x16x32_bf16 a[32:35], a[212:215], v[52:55], a[32:35]\n"
    "ds_read_b64_tr_b16 a[234:235], v221 offset:42632\n"
    "v_mfma_f32_16x16x32_bf16 a[36:39], a[208:211], v[56:59], a[36:39]\n"
    "ds_read_b64_tr_b16 a[236:237], v221 offset:53384\n"
    "v_mfma_f32_16x16x32_bf16 a[36:39], a[212:215], v[60:63], a[36:39]\n"
    "ds_read_b64_tr_b16 a[238:239], v221 offset:54920\n"
    "s_add_u32 m0, s65, 77824\n"
    "v_mfma_f32_16x16x32_bf16 a[40:43], a[208:211], v[64:67], a[40:43]\n"
    "buffer_load_dwordx4 v224, s[40:43], s74 offen offset:0 lds\n"
    "v_mfma_f32_16x16x32_bf16 a[40:43], a[212:215], v[68:71], a[40:43]\n"
    "v_mfma_f32_16x16x32_bf16 a[44:47], a[208:211], v[72:75], a[44:47]\n"
    "v_mfma_f32_16x16x32_bf16 a[44:47], a[212:215], v[76:79], a[44:47]\n"
    "s_waitcnt lgkmcnt(8)\n"
    "v_mfma_f32_16x16x32_bf16 a[48:51], a[216:219], v[48:51], a[48:51]\n"
    "ds_read_b64_tr_b16 a[192:193], v222 offset:41088\n"
    "s_add_u32 m0, s65, 78848\n"
    "v_mfma_f32_16x16x32_bf16 a[48:51], a[220:223], v[52:55], a[48:51]\n"
    "ds_read_b64_tr_b16 a[194:195], v222 offset:42624\n"
    "v_mfma_f32_16x16x32_bf16 a[52:55], a[216:219], v[56:59], a[52:55]\n"
    "ds_read_b64_tr_b16 a[196:197], v222 offset:53376\n"
    "v_mfma_f32_16x16x32_bf16 a[52:55], a[220:223], v[60:63], a[52:55]\n"
    "ds_read_b64_tr_b16 a[198:199], v222 offset:54912\n"
    "v_mfma_f32_16x16x32_bf16 a[56:59], a[216:219], v[64:67], a[56:59]\n"
    "buffer_load_dwordx4 v225, s[40:43], s74 offen offset:0 lds\n"
    "v_mfma_f32_16x16x32_bf16 a[56:59], a[220:223], v[68:71], a[56:59]\n"
    "s_add_u32 m0, s29, 98304\n"
    "v_mfma_f32_16x16x32_bf16 a[60:63], a[216:219], v[72:75], a[60:63]\n"
    "buffer_load_dwordx4 v5, s[20:23], s28 offen offset:0 lds\n"
    "v_mfma_f32_16x16x32_bf16 a[60:63], a[220:223], v[76:79], a[60:63]\n"
    "s_waitcnt lgkmcnt(8)\n"
    "v_mfma_f32_16x16x32_bf16 a[64:67], a[224:227], v[48:51], a[64:67]\n"
    "ds_read_b64_tr_b16 a[200:201], v222 offset:41096\n"
    "v_mfma_f32_16x16x32_bf16 a[64:67], a[228:231], v[52:55], a[64:67]\n"
    "ds_read_b64_tr_b16 a[202:203], v222 offset:42632\n"
    "v_mfma_f32_16x16x32_bf16 a[68:71], a[224:227], v[56:59], a[68:71]\n"
    "ds_read_b64_tr_b16 a[204:205], v222 offset:53384\n"
    "v_mfma_f32_16x16x32_bf16 a[68:71], a[228:231], v[60:63], a[68:71]\n"
    "ds_read_b64_tr_b16 a[206:207], v222 offset:54920\n"
    "v_mfma_f32_16x16x32_bf16 a[72:75], a[224:227], v[64:67], a[72:75]\n"
    "buffer_load_dwordx4 v6, s[20:23], s28 offen offset:1024 lds\n"
    "v_mfma_f32_16x16x32_bf16 a[72:75], a[228:231], v[68:71], a[72:75]\n"
    "v_mfma_f32_16x16x32_bf16 a[76:79], a[224:227], v[72:75], a[76:79]\n"
    "buffer_load_dwordx4 v7, s[20:23], s28 offen offset:2048 lds\n"
    "v_mfma_f32_16x16x32_bf16 a[76:79], a[228:231], v[76:79], a[76:79]\n"
    "s_waitcnt lgkmcnt(8)\n"
    "v_mfma_f32_16x16x32_bf16 a[80:83], a[232:235], v[48:51], a[80:83]\n"
    "ds_read_b64_tr_b16 a[208:209], v221 offset:41216\n"
    "v_mfma_f32_16x16x32_bf16 a[80:83], a[236:239], v[52:55], a[80:83]\n"
    "ds_read_b64_tr_b16 a[210:211], v221 offset:42752\n"
    "v_mfma_f32_16x16x32_bf16 a[84:87], a[232:235], v[56:59], a[84:87]\n"
    "ds_read_b64_tr_b16 a[212:213], v221 offset:53504\n"
    "v_mfma_f32_16x16x32_bf16 a[84:87], a[236:239], v[60:63], a[84:87]\n"
    "ds_read_b64_tr_b16 a[214:215], v221 offset:55040\n"
    "v_mfma_f32_16x16x32_bf16 a[88:91], a[232:235], v[64:67], a[88:91]\n"
    "buffer_load_dwordx4 v8, s[20:23], s28 offen offset:3072 lds\n"
    "v_mfma_f32_16x16x32_bf16 a[88:91], a[236:239], v[68:71], a[88:91]\n"
    "v_mfma_f32_16x16x32_bf16 a[92:95], a[232:235], v[72:75], a[92:95]\n"
    "v_mfma_f32_16x16x32_bf16 a[92:95], a[236:239], v[76:79], a[92:95]\n"
    "s_waitcnt lgkmcnt(8)\n"
    "v_mfma_f32_16x16x32_bf16 a[96:99], a[192:195], v[48:51], a[96:99]\n"
    "ds_read_b64_tr_b16 a[216:217], v221 offset:41224\n"
    "s_add_u32 s73, s73, 786432\n"
    "v_mfma_f32_16x16x32_bf16 a[96:99], a[196:199], v[52:55], a[96:99]\n"
    "ds_read_b64_tr_b16 a[218:219], v221 offset:42760\n"
    "s_add_u32 s74, s74, 786432\n"
    "v_mfma_f32_16x16x32_bf16 a[100:103], a[192:195], v[56:59], a[100:103]\n"
    "ds_read_b64_tr_b16 a[220:221], v221 offset:53512\n"
    "s_add_u32 s28, s28, 196608\n"
    "v_mfma_f32_16x16x32_bf16 a[100:103], a[196:199], v[60:63], a[100:103]\n"
    "ds_read_b64_tr_b16 a[222:223], v221 offset:55048\n"
    "v_mfma_f32_16x16x32_bf16 a[104:107], a[192:195], v[64:67], a[104:107]\n"
    "buffer_load_dwordx4 v[16:19], v226, s[36:39], s69 offen offset:0\n"
    "v_mfma_f32_16x16x32_bf16 a[104:107], a[196:199], v[68:71], a[104:107]\n"
    "buffer_load_dwordx4 v[20:23], v226, s[36:39], s69 offen offset:1024\n"
    "v_mfma_f32_16x16x32_bf16 a[108:111], a[192:195], v[72:75], a[108:111]\n"
    "v_mfma_f32_16x16x32_bf16 a[108:111], a[196:199], v[76:79], a[108:111]\n"
    "s_waitcnt lgkmcnt(8)\n"
    "v_mfma_f32_16x16x32_bf16 a[112:115], a[200:203], v[48:51], a[112:115]\n"
    "ds_read_b64_tr_b16 a[224:225], v222 offset:41216\n"
    "v_mfma_f32_16x16x32_bf16 a[112:115], a[204:207], v[52:55], a[112:115]\n"
    "ds_read_b64_tr_b16 a[226:227], v222 offset:42752\n"
    "v_mfma_f32_16x16x32_bf16 a[116:119], a[200:203], v[56:59], a[116:119]\n"
    "ds_read_b64_tr_b16 a[228:229], v222 offset:53504\n"
    "v_mfma_f32_16x16x32_bf16 a[116:119], a[204:207], v[60:63], a[116:119]\n"
    "ds_read_b64_tr_b16 a[230:231], v222 offset:55040\n"
    "v_mfma_f32_16x16x32_bf16 a[120:123], a[200:203], v[64:67], a[120:123]\n"
    "buffer_load_dwordx4 v[24:27], v226, s[36:39], s70 offen offset:0\n"
    "v_mfma_f32_16x16x32_bf16 a[120:123], a[204:207], v[68:71], a[120:123]\n"
    "v_mfma_f32_16x16x32_bf16 a[124:127], a[200:203], v[72:75], a[124:127]\n"
    "s_waitcnt lgkmcnt(8)\n"
    "v_mfma_f32_16x16x32_bf16 a[124:127], a[204:207], v[76:79], a[124:127]\n"
    "buffer_load_dwordx4 v[28:31], v226, s[36:39], s70 offen offset:1024\n"
    "v_mfma_f32_16x16x32_bf16 a[128:131], a[208:211], v[48:51], a[128:131]\n"
    "ds_read_b64_tr_b16 a[232:233], v222 offset:41224\n"
    "v_mfma_f32_16x16x32_bf16 a[128:131], a[212:215], v[52:55], a[128:131]\n"
    "ds_read_b64_tr_b16 a[234:235], v222 offset:42760\n"
    "v_mfma_f32_16x16x32_bf16 a[132:135], a[208:211], v[56:59], a[132:135]\n"
    "ds_read_b64_tr_b16 a[236:237], v222 offset:53512\n"
    "v_mfma_f32_16x16x32_bf16 a[132:135], a[212:215], v[60:63], a[132:135]\n"
    "ds_read_b64_tr_b16 a[238:239], v222 offset:55048\n"
    "v_mfma_f32_16x16x32_bf16 a[136:139], a[208:211], v[64:67], a[136:139]\n"
    "buffer_load_dwordx4 v[32:35], v226, s[36:39], s71 offen offset:0\n"
    "v_mfma_f32_16x16x32_bf16 a[136:139], a[212:215], v[68:71], a[136:139]\n"
    "v_mfma_f32_16x16x32_bf16 a[140:143], a[208:211], v[72:75], a[140:143]\n"
    "v_mfma_f32_16x16x32_bf16 a[140:143], a[212:215], v[76:79], a[140:143]\n"
    "s_waitcnt lgkmcnt(8)\n"
    "v_mfma_f32_16x16x32_bf16 a[144:147], a[216:219], v[48:51], a[144:147]\n"
    "ds_read_b64_tr_b16 a[192:193], v2 offset:40960\n"
    "v_mfma_f32_16x16x32_bf16 a[144:147], a[220:223], v[52:55], a[144:147]\n"
    "ds_read_b64_tr_b16 a[194:195], v2 offset:41472\n"
    "v_mfma_f32_16x16x32_bf16 a[148:151], a[216:219], v[56:59], a[148:151]\n"
    "ds_read_b64_tr_b16 a[196:197], v2 offset:45056\n"
    "v_mfma_f32_16x16x32_bf16 a[148:151], a[220:223], v[60:63], a[148:151]\n"
    "ds_read_b64_tr_b16 a[198:199], v2 offset:45568\n"
    "v_mfma_f32_16x16x32_bf16 a[152:155], a[216:219], v[64:67], a[152:155]\n"
    "buffer_load_dwordx4 v[36:39], v226, s[36:39], s71 offen offset:1024\n"
    "v_mfma_f32_16x16x32_bf16 a[152:155], a[220:223], v[68:71], a[152:155]\n"
    "buffer_load_dwordx4 v[40:43], v226, s[36:39], s72 offen offset:0\n"
    "v_mfma_f32_16x16x32_bf16 a[156:159], a[216:219], v[72:75], a[156:159]\n"
    "v_mfma_f32_16x16x32_bf16 a[156:159], a[220:223], v[76:79], a[156:159]\n"
    "s_waitcnt lgkmcnt(8)\n"
    "v_mfma_f32_16x16x32_bf16 a[160:163], a[224:227], v[48:51], a[160:163]\n"
    "ds_read_b64_tr_b16 a[200:201], v2 offset:49152\n"
    "v_mfma_f32_16x16x32_bf16 a[160:163], a[228:231], v[52:55], a[160:163]\n"
    "ds_read_b64_tr_b16 a[202:203], v2 offset:49664\n"
    "v_mfma_f32_16x16x32_bf16 a[164:167], a[224:227], v[56:59], a[164:167]\n"
    "ds_read_b64_tr_b16 a[204:205], v2 offset:53248\n"
    "v_mfma_f32_16x16x32_bf16 a[164:167], a[228:231], v[60:63], a[164:167]\n"
    "ds_read_b64_tr_b16 a[206:207], v2 offset:53760\n"
    "v_mfma_f32_16x16x32_bf16 a[168:171], a[224:227], v[64:67], a[168:171]\n"
    "buffer_load_dwordx4 v[44:47], v226, s[36:39], s72 offen offset:1024\n"
    "v_mfma_f32_16x16x32_bf16 a[168:171], a[228:231], v[68:71], a[168:171]\n"
    "v_mfma_f32_16x16x32_bf16 a[172:175], a[224:227], v[72:75], a[172:175]\n"
    "v_mfma_f32_16x16x32_bf16 a[172:175], a[228:231], v[76:79], a[172:175]\n"
    "s_waitcnt lgkmcnt(8)\n"
    "s_add_u32 s69, s69, 2048\n"
    "v_mfma_f32_16x16x32_bf16 a[176:179], a[232:235], v[48:51], a[176:179]\n"
    "ds_read_b64_tr_b16 a[208:209], v3 offset:40960\n"
    "s_add_u32 s70, s70, 2048\n"
    "v_mfma_f32_16x16x32_bf16 a[176:179], a[236:239], v[52:55], a[176:179]\n"
    "ds_read_b64_tr_b16 a[210:211], v3 offset:41472\n"
    "s_add_u32 s71, s71, 2048\n"
    "v_mfma_f32_16x16x32_bf16 a[180:183], a[232:235], v[56:59], a[180:183]\n"
    "ds_read_b64_tr_b16 a[212:213], v3 offset:45056\n"
    "s_add_u32 s72, s72, 2048\n"
    "v_mfma_f32_16x16x32_bf16 a[180:183], a[236:239], v[60:63], a[180:183]\n"
    "ds_read_b64_tr_b16 a[214:215], v3 offset:45568\n"
    "buffer_load_dword v15, v10, s[40:43], s96 offen offset:0\n"
    "v_mfma_f32_16x16x32_bf16 a[184:187], a[232:235], v[64:67], a[184:187]\n"
    "buffer_load_dword v15, v11, s[20:23], s97 offen offset:0\n"
    "v_mfma_f32_16x16x32_bf16 a[184:187], a[236:239], v[68:71], a[184:187]\n"
    "v_mfma_f32_16x16x32_bf16 a[188:191], a[232:235], v[72:75], a[188:191]\n"
    "v_mfma_f32_16x16x32_bf16 a[188:191], a[236:239], v[76:79], a[188:191]\n"
    "s_waitcnt lgkmcnt(8)\n"
    "v_mfma_f32_16x16x32_bf16 v[112:115], a[192:195], v[48:51], v[112:115]\n"
    "ds_read_b64_tr_b16 a[216:217], v3 offset:49152\n"
    "v_mfma_f32_16x16x32_bf16 v[112:115], a[196:199], v[52:55], v[112:115]\n"
    "ds_read_b64_tr_b16 a[218:219], v3 offset:49664\n"
    "s_add_u32 s96, s96, 786432\n"
    "v_mfma_f32_16x16x32_bf16 v[116:119], a[192:195], v[56:59], v[116:119]\n"
    "ds_read_b64_tr_b16 a[220:221], v3 offset:53248\n"
    "s_add_u32 s97, s97, 196608\n"
    "v_mfma_f32_16x16x32_bf16 v[116:119], a[196:199], v[60:63], v[116:119]\n"
    "ds_read_b64_tr_b16 a[222:223], v3 offset:53760\n"
    "v_mfma_f32_16x16x32_bf16 v[120:123], a[192:195], v[64:67], v[120:123]\n"
    "v_mfma_f32_16x16x32_bf16 v[120:123], a[196:199], v[68:71], v[120:123]\n"
    "v_mfma_f32_16x16x32_bf16 v[124:127], a[192:195], v[72:75], v[124:127]\n"
    "v_mfma_f32_16x16x32_bf16 v[124:127], a[196:199], v[76:79], v[124:127]\n"
    "s_waitcnt lgkmcnt(8)\n"
    "v_mfma_f32_16x16x32_bf16 v[128:131], a[200:203], v[48:51], v[128:131]\n"
    "ds_read_b64_tr_b16 a[224:225], v4 offset:40960\n"
    "v_mfma_f32_16x16x32_bf16 v[128:131], a[204:207], v[52:55], v[128:131]\n"
    "ds_read_b64_tr_b16 a[226:227], v4 offset:41472\n"
    "v_mfma_f32_16x16x32_bf16 v[132:135], a[200:203], v[56:59], v[132:135]\n"
    "ds_read_b64_tr_b16 a[228:229], v4 offset:45056\n"
    "v_mfma_f32_16x16x32_bf16 v[132:135], a[204:207], v[60:63], v[132:135]\n"
    "ds_read_b64_tr_b16 a[230:231], v4 offset:45568\n"
    "v_mfma_f32_16x16x32_bf16 v[136:139], a[200:203], v[64:67], v[136:139]\n"
    "v_mfma_f32_16x16x32_bf16 v[136:139], a[204:207], v[68:71], v[136:139]\n"
    "v_mfma_f32_16x16x32_bf16 v[140:143], a[200:203], v[72:75], v[140:143]\n"
    "v_mfma_f32_16x16x32_bf16 v[140:143], a[204:207], v[76:79], v[140:143]\n"
    "s_waitcnt lgkmcnt(8)\n"
    "v_mfma_f32_16x16x32_bf16 v[144:147], a[208:211], v[48:51], v[144:147]\n"
    "ds_read_b64_tr_b16 a[232:233], v4 offset:49152\n"
    "v_mfma_f32_16x16x32_bf16 v[144:147], a[212:215], v[52:55], v[144:147]\n"
    "ds_read_b64_tr_b16 a[234:235], v4 offset:49664\n"
    "v_mfma_f32_16x16x32_bf16 v[148:151], a[208:211], v[56:59], v[148:151]\n"
    "ds_read_b64_tr_b16 a[236:237], v4 offset:53248\n"
    "v_mfma_f32_16x16x32_bf16 v[148:151], a[212:215], v[60:63], v[148:151]\n"
    "ds_read_b64_tr_b16 a[238:239], v4 offset:53760\n"
    "v_mfma_f32_16x16x32_bf16 v[152:155], a[208:211], v[64:67], v[152:155]\n"
    "v_mfma_f32_16x16x32_bf16 v[152:155], a[212:215], v[68:71], v[152:155]\n"
    "v_mfma_f32_16x16x32_bf16 v[156:159], a[208:211], v[72:75], v[156:159]\n"
    "v_mfma_f32_16x16x32_bf16 v[156:159], a[212:215], v[76:79], v[156:159]\n"
    "s_waitcnt lgkmcnt(8)\n"
    "v_mfma_f32_16x16x32_bf16 v[160:163], a[216:219], v[48:51], v[160:163]\n"
    "v_mfma_f32_16x16x32_bf16 v[160:163], a[220:223], v[52:55], v[160:163]\n"
    "v_mfma_f32_16x16x32_bf16 v[164:167], a[216:219], v[56:59], v[164:167]\n"
    "v_mfma_f32_16x16x32_bf16 v[164:167], a[220:223], v[60:63], v[164:167]\n"
    "v_mfma_f32_16x16x32_bf16 v[168:171], a[216:219], v[64:67], v[168:171]\n"
    "v_mfma_f32_16x16x32_bf16 v[168:171], a[220:223], v[68:71], v[168:171]\n"
    "v_mfma_f32_16x16x32_bf16 v[172:175], a[216:219], v[72:75], v[172:175]\n"
    "v_mfma_f32_16x16x32_bf16 v[172:175], a[220:223], v[76:79], v[172:175]\n"
    "s_waitcnt vmcnt(10)\n"
    "s_waitcnt lgkmcnt(0)\n"
    "s_barrier\n"
    "v_mfma_f32_16x16x32_bf16 v[176:179], a[224:227], v[48:51], v[176:179]\n"
    "ds_read_b64_tr_b16 a[192:193], v221 offset:0\n"
    "v_mfma_f32_16x16x32_bf16 v[176:179], a[228:231], v[52:55], v[176:179]\n"
    "ds_read_b64_tr_b16 a[194:195], v221 offset:1536\n"
    "v_mfma_f32_16x16x32_bf16 v[180:183], a[224:227], v[56:59], v[180:183]\n"
    "ds_read_b64_tr_b16 a[196:197], v221 offset:12288\n"
    "v_mfma_f32_16x16x32_bf16 v[180:183], a[228:231], v[60:63], v[180:183]\n"
    "ds_read_b64_tr_b16 a[198:199], v221 offset:13824\n"
    "v_mfma_f32_16x16x32_bf16 v[184:187], a[224:227], v[64:67], v[184:187]\n"
    "ds_read_b64_tr_b16 a[200:201], v221 offset:8\n"
    "v_mfma_f32_16x16x32_bf16 v[184:187], a[228:231], v[68:71], v[184:187]\n"
    "ds_read_b64_tr_b16 a[202:203], v221 offset:1544\n"
    "v_mfma_f32_16x16x32_bf16 v[188:191], a[224:227], v[72:75], v[188:191]\n"
    "ds_read_b64_tr_b16 a[204:205], v221 offset:12296\n"
    "v_mfma_f32_16x16x32_bf16 v[188:191], a[228:231], v[76:79], v[188:191]\n"
    "ds_read_b64_tr_b16 a[206:207], v221 offset:13832\n"
    "v_mfma_f32_16x16x32_bf16 v[192:195], a[232:235], v[48:51], v[192:195]\n"
    "v_mfma_f32_16x16x32_bf16 v[192:195], a[236:239], v[52:55], v[192:195]\n"
    "v_mfma_f32_16x16x32_bf16 v[196:199], a[232:235], v[56:59], v[196:199]\n"
    "v_mfma_f32_16x16x32_bf16 v[196:199], a[236:239], v[60:63], v[196:199]\n"
    "v_mfma_f32_16x16x32_bf16 v[200:203], a[232:235], v[64:67], v[200:203]\n"
    "v_mfma_f32_16x16x32_bf16 v[200:203], a[236:239], v[68:71], v[200:203]\n"
    "v_mfma_f32_16x16x32_bf16 v[204:207], a[232:235], v[72:75], v[204:207]\n"
    "v_mfma_f32_16x16x32_bf16 v[204:207], a[236:239], v[76:79], v[204:207]\n"
    "s_waitcnt lgkmcnt(4)\n"
    "ds_read_b64_tr_b16 a[208:209], v222 offset:0\n"
    "s_waitcnt vmcnt(2)\n"
    "v_mfma_f32_16x16x32_bf16 a[0:3], a[192:195], v[16:19], a[0:3]\n"
    "ds_read_b64_tr_b16 a[210:211], v222 offset:1536\n"
    "s_add_u32 m0, s65, 114688\n"
    "v_mfma_f32_16x16x32_bf16 a[0:3], a[196:199], v[20:23], a[0:3]\n"
    "ds_read_b64_tr_b16 a[212:213], v222 offset:12288\n"
    "buffer_load_dwordx4 v223, s[40:43], s73 offen offset:0 lds\n"
    "v_mfma_f32_16x16x32_bf16 a[4:7], a[192:195], v[24:27], a[4:7]\n"
    "ds_read_b64_tr_b16 a[214:215], v222 offset:13824\n"
    "v_mfma_f32_16x16x32_bf16 a[4:7], a[196:199], v[28:31], a[4:7]\n"
    "ds_read_b64_tr_b16 a[216:217], v222 offset:8\n"
    "v_mfma_f32_16x16x32_bf16 a[8:11], a[192:195], v[32:35], a[8:11]\n"
    "ds_read_b64_tr_b16 a[218:219], v222 offset:1544\n"
    "s_add_u32 m0, s65, 115712\n"
    "v_mfma_f32_16x16x32_bf16 a[8:11], a[196:199], v[36:39], a[8:11]\n"
    "ds_read_b64_tr_b16 a[220:221], v222 offset:12296\n"
    "buffer_load_dwordx4 v224, s[40:43], s73 offen offset:0 lds\n"
    "v_mfma_f32_16x16x32_bf16 a[12:15], a[192:195], v[40:43], a[12:15]\n"
    "ds_read_b64_tr_b16 a[222:223], v222 offset:13832\n"
    "v_mfma_f32_16x16x32_bf16 a[12:15], a[196:199], v[44:47], a[12:15]\n"
    "s_waitcnt lgkmcnt(8)\n"
    "v_mfma_f32_16x16x32_bf16 a[16:19], a[200:203], v[16:19], a[16:19]\n"
    "ds_read_b64_tr_b16 a[224:225], v221 offset:128\n"
    "v_mfma_f32_16x16x32_bf16 a[16:19], a[204:207], v[20:23], a[16:19]\n"
    "ds_read_b64_tr_b16 a[226:227], v221 offset:1664\n"
    "s_add_u32 m0, s65, 116736\n"
    "v_mfma_f32_16x16x32_bf16 a[20:23], a[200:203], v[24:27], a[20:23]\n"
    "ds_read_b64_tr_b16 a[228:229], v221 offset:12416\n"
    "v_mfma_f32_16x16x32_bf16 a[20:23], a[204:207], v[28:31], a[20:23]\n"
    "ds_read_b64_tr_b16 a[230:231], v221 offset:13952\n"
    "v_mfma_f32_16x16x32_bf16 a[24:27], a[200:203], v[32:35], a[24:27]\n"
    "buffer_load_dwordx4 v225, s[40:43], s73 offen offset:0 lds\n"
    "v_mfma_f32_16x16x32_bf16 a[24:27], a[204:207], v[36:39], a[24:27]\n"
    "v_mfma_f32_16x16x32_bf16 a[28:31], a[200:203], v[40:43], a[28:31]\n"
    "s_add_u32 m0, s65, 117760\n"
    "s_nop 0\n"
    "buffer_load_dwordx4 v223, s[40:43], s74 offen offset:0 lds\n"
    "v_mfma_f32_16x16x32_bf16 a[28:31], a[204:207], v[44:47], a[28:31]\n"
    "s_waitcnt lgkmcnt(8)\n"
    "v_mfma_f32_16x16x32_bf16 a[32:35], a[208:211], v[16:19], a[32:35]\n"
    "ds_read_b64_tr_b16 a[232:233], v221 offset:136\n"
    "v_mfma_f32_16x16x32_bf16 a[32:35], a[212:215], v[20:23], a[32:35]\n"
    "ds_read_b64_tr_b16 a[234:235], v221 offset:1672\n"
    "v_mfma_f32_16x16x32_bf16 a[36:39], a[208:211], v[24:27], a[36:39]\n"
    "ds_read_b64_tr_b16 a[236:237], v221 offset:12424\n"
    "v_mfma_f32_16x16x32_bf16 a[36:39], a[212:215], v[28:31], a[36:39]\n"
    "ds_read_b64_tr_b16 a[238:239], v221 offset:13960\n"
    "s_add_u32 m0, s65, 118784\n"
    "v_mfma_f32_16x16x32_bf16 a[40:43], a[208:211], v[32:35], a[40:43]\n"
    "buffer_load_dwordx4 v224, s[40:43], s74 offen offset:0 lds\n"
    "v_mfma_f32_16x16x32_bf16 a[40:43], a[212:215], v[36:39], a[40:43]\n"
    "v_mfma_f32_16x16x32_bf16 a[44:47], a[208:211], v[40:43], a[44:47]\n"
    "v_mfma_f32_16x16x32_bf16 a[44:47], a[212:215], v[44:47], a[44:47]\n"
    "s_waitcnt lgkmcnt(8)\n"
    "v_mfma_f32_16x16x32_bf16 a[48:51], a[216:219], v[16:19], a[48:51]\n"
    "ds_read_b64_tr_b16 a[192:193], v222 offset:128\n"
    "s_add_u32 m0, s65, 119808\n"
    "v_mfma_f32_16x16x32_bf16 a[48:51], a[220:223], v[20:23], a[48:51]\n"
    "ds_read_b64_tr_b16 a[194:195], v222 offset:1664\n"
    "v_mfma_f32_16x16x32_bf16 a[52:55], a[216:219], v[24:27], a[52:55]\n"
    "ds_read_b64_tr_b16 a[196:197], v222 offset:12416\n"
    "v_mfma_f32_16x16x32_bf16 a[52:55], a[220:223], v[28:31], a[52:55]\n"
    "ds_read_b64_tr_b16 a[198:199], v222 offset:13952\n"
    "v_mfma_f32_16x16x32_bf16 a[56:59], a[216:219], v[32:35], a[56:59]\n"
    "buffer_load_dwordx4 v225, s[40:43], s74 offen offset:0 lds\n"
    "v_mfma_f32_16x16x32_bf16 a[56:59], a[220:223], v[36:39], a[56:59]\n"
    "s_add_u32 m0, s29, 139264\n"
    "v_mfma_f32_16x16x32_bf16 a[60:63], a[216:219], v[40:43], a[60:63]\n"
    "buffer_load_dwordx4 v5, s[20:23], s28 offen offset:0 lds\n"
    "v_mfma_f32_16x16x32_bf16 a[60:63], a[220:223], v[44:47], a[60:63]\n"
    "s_waitcnt lgkmcnt(8)\n"
    "v_mfma_f32_16x16x32_bf16 a[64:67], a[224:227], v[16:19], a[64:67]\n"
    "ds_read_b64_tr_b16 a[200:201], v222 offset:136\n"
    "v_mfma_f32_16x16x32_bf16 a[64:67], a[228:231], v[20:23], a[64:67]\n"
    "ds_read_b64_tr_b16 a[202:203], v222 offset:1672\n"
    "v_mfma_f32_16x16x32_bf16 a[68:71], a[224:227], v[24:27], a[68:71]\n"
    "ds_read_b64_tr_b16 a[204:205], v222 offset:12424\n"
    "v_mfma_f32_16x16x32_bf16 a[68:71], a[228:231], v[28:31], a[68:71]\n"
    "ds_read_b64_tr_b16 a[206:207], v222 offset:13960\n"
    "v_mfma_f32_16x16x32_bf16 a[72:75], a[224:227], v[32:35], a[72:75]\n"
    "buffer_load_dwordx4 v6, s[20:23], s28 offen offset:1024 lds\n"
    "v_mfma_f32_16x16x32_bf16 a[72:75], a[228:231], v[36:39], a[72:75]\n"
    "v_mfma_f32_16x16x32_bf16 a[76:79], a[224:227], v[40:43], a[76:79]\n"
    "buffer_load_dwordx4 v7, s[20:23], s28 offen offset:2048 lds\n"
    "v_mfma_f32_16x16x32_bf16 a[76:79], a[228:231], v[44:47], a[76:79]\n"
    "s_waitcnt lgkmcnt(8)\n"
    "v_mfma_f32_16x16x32_bf16 a[80:83], a[232:235], v[16:19], a[80:83]\n"
    "ds_read_b64_tr_b16 a[208:209], v221 offset:256\n"
    "v_mfma_f32_16x16x32_bf16 a[80:83], a[236:239], v[20:23], a[80:83]\n"
    "ds_read_b64_tr_b16 a[210:211], v221 offset:1792\n"
    "v_mfma_f32_16x16x32_bf16 a[84:87], a[232:235], v[24:27], a[84:87]\n"
    "ds_read_b64_tr_b16 a[212:213], v221 offset:12544\n"
    "v_mfma_f32_16x16x32_bf16 a[84:87], a[236:239], v[28:31], a[84:87]\n"
    "ds_read_b64_tr_b16 a[214:215], v221 offset:14080\n"
    "v_mfma_f32_16x16x32_bf16 a[88:91], a[232:235], v[32:35], a[88:91]\n"
    "buffer_load_dwordx4 v8, s[20:23], s28 offen offset:3072 lds\n"
    "v_mfma_f32_16x16x32_bf16 a[88:91], a[236:239], v[36:39], a[88:91]\n"
    "v_mfma_f32_16x16x32_bf16 a[92:95], a[232:235], v[40:43], a[92:95]\n"
    "v_mfma_f32_16x16x32_bf16 a[92:95], a[236:239], v[44:47], a[92:95]\n"
    "s_waitcnt lgkmcnt(8)\n"
    "v_mfma_f32_16x16x32_bf16 a[96:99], a[192:195], v[16:19], a[96:99]\n"
    "ds_read_b64_tr_b16 a[216:217], v221 offset:264\n"
    "s_add_u32 s73, s73, 786432\n"
    "v_mfma_f32_16x16x32_bf16 a[96:99], a[196:199], v[20:23], a[96:99]\n"
    "ds_read_b64_tr_b16 a[218:219], v221 offset:1800\n"
    "s_add_u32 s74, s74, 786432\n"
    "v_mfma_f32_16x16x32_bf16 a[100:103], a[192:195], v[24:27], a[100:103]\n"
    "ds_read_b64_tr_b16 a[220:221], v221 offset:12552\n"
    "s_add_u32 s28, s28, 196608\n"
    "v_mfma_f32_16x16x32_bf16 a[100:103], a[196:199], v[28:31], a[100:103]\n"
    "ds_read_b64_tr_b16 a[222:223], v221 offset:14088\n"
    "v_mfma_f32_16x16x32_bf16 a[104:107], a[192:195], v[32:35], a[104:107]\n"
    "buffer_load_dwordx4 v[48:51], v226, s[36:39], s69 offen offset:0\n"
    "v_mfma_f32_16x16x32_bf16 a[104:107], a[196:199], v[36:39], a[104:107]\n"
    "buffer_load_dwordx4 v[52:55], v226, s[36:39], s69 offen offset:1024\n"
    "v_mfma_f32_16x16x32_bf16 a[108:111], a[192:195], v[40:43], a[108:111]\n"
    "v_mfma_f32_16x16x32_bf16 a[108:111], a[196:199], v[44:47], a[108:111]\n"
    "s_waitcnt lgkmcnt(8)\n"
    "v_mfma_f32_16x16x32_bf16 a[112:115], a[200:203], v[16:19], a[112:115]\n"
    "ds_read_b64_tr_b16 a[224:225], v222 offset:256\n"
    "v_mfma_f32_16x16x32_bf16 a[112:115], a[204:207], v[20:23], a[112:115]\n"
    "ds_read_b64_tr_b16 a[226:227], v222 offset:1792\n"
    "v_mfma_f32_16x16x32_bf16 a[116:119], a[200:203], v[24:27], a[116:119]\n"
    "ds_read_b64_tr_b16 a[228:229], v222 offset:12544\n"
    "v_mfma_f32_16x16x32_bf16 a[116:119], a[204:207], v[28:31], a[116:119]\n"
    "ds_read_b64_tr_b16 a[230:231], v222 offset:14080\n"
    "v_mfma_f32_16x16x32_bf16 a[120:123], a[200:203], v[32:35], a[120:123]\n"
    "buffer_load_dwordx4 v[56:59], v226, s[36:39], s70 offen offset:0\n"
    "v_mfma_f32_16x16x32_bf16 a[120:123], a[204:207], v[36:39], a[120:123]\n"
    "v_mfma_f32_16x16x32_bf16 a[124:127], a[200:203], v[40:43], a[124:127]\n"
    "s_waitcnt lgkmcnt(8)\n"
    "v_mfma_f32_16x16x32_bf16 a[124:127], a[204:207], v[44:47], a[124:127]\n"
    "buffer_load_dwordx4 v[60:63], v226, s[36:39], s70 offen offset:1024\n"
    "v_mfma_f32_16x16x32_bf16 a[128:131], a[208:211], v[16:19], a[128:131]\n"
    "ds_read_b64_tr_b16 a[232:233], v222 offset:264\n"
    "v_mfma_f32_16x16x32_bf16 a[128:131], a[212:215], v[20:23], a[128:131]\n"
    "ds_read_b64_tr_b16 a[234:235], v222 offset:1800\n"
    "v_mfma_f32_16x16x32_bf16 a[132:135], a[208:211], v[24:27], a[132:135]\n"
    "ds_read_b64_tr_b16 a[236:237], v222 offset:12552\n"
    "v_mfma_f32_16x16x32_bf16 a[132:135], a[212:215], v[28:31], a[132:135]\n"
    "ds_read_b64_tr_b16 a[238:239], v222 offset:14088\n"
    "v_mfma_f32_16x16x32_bf16 a[136:139], a[208:211], v[32:35], a[136:139]\n"
    "buffer_load_dwordx4 v[64:67], v226, s[36:39], s71 offen offset:0\n"
    "v_mfma_f32_16x16x32_bf16 a[136:139], a[212:215], v[36:39], a[136:139]\n"
    "v_mfma_f32_16x16x32_bf16 a[140:143], a[208:211], v[40:43], a[140:143]\n"
    "v_mfma_f32_16x16x32_bf16 a[140:143], a[212:215], v[44:47], a[140:143]\n"
    "s_waitcnt lgkmcnt(8)\n"
    "v_mfma_f32_16x16x32_bf16 a[144:147], a[216:219], v[16:19], a[144:147]\n"
    "ds_read_b64_tr_b16 a[192:193], v2 offset:0\n"
    "v_mfma_f32_16x16x32_bf16 a[144:147], a[220:223], v[20:23], a[144:147]\n"
    "ds_read_b64_tr_b16 a[194:195], v2 offset:512\n"
    "v_mfma_f32_16x16x32_bf16 a[148:151], a[216:219], v[24:27], a[148:151]\n"
    "ds_read_b64_tr_b16 a[196:197], v2 offset:4096\n"
    "v_mfma_f32_16x16x32_bf16 a[148:151], a[220:223], v[28:31], a[148:151]\n"
    "ds_read_b64_tr_b16 a[198:199], v2 offset:4608\n"
    "v_mfma_f32_16x16x32_bf16 a[152:155], a[216:219], v[32:35], a[152:155]\n"
    "buffer_load_dwordx4 v[68:71], v226, s[36:39], s71 offen offset:1024\n"
    "v_mfma_f32_16x16x32_bf16 a[152:155], a[220:223], v[36:39], a[152:155]\n"
    "buffer_load_dwordx4 v[72:75], v226, s[36:39], s72 offen offset:0\n"
    "v_mfma_f32_16x16x32_bf16 a[156:159], a[216:219], v[40:43], a[156:159]\n"
    "v_mfma_f32_16x16x32_bf16 a[156:159], a[220:223], v[44:47], a[156:159]\n"
    "s_waitcnt lgkmcnt(8)\n"
    "v_mfma_f32_16x16x32_bf16 a[160:163], a[224:227], v[16:19], a[160:163]\n"
    "ds_read_b64_tr_b16 a[200:201], v2 offset:8192\n"
    "v_mfma_f32_16x16x32_bf16 a[160:163], a[228:231], v[20:23], a[160:163]\n"
    "ds_read_b64_tr_b16 a[202:203], v2 offset:8704\n"
    "v_mfma_f32_16x16x32_bf16 a[164:167], a[224:227], v[24:27], a[164:167]\n"
    "ds_read_b64_tr_b16 a[204:205], v2 offset:12288\n"
    "v_mfma_f32_16x16x32_bf16 a[164:167], a[228:231], v[28:31], a[164:167]\n"
    "ds_read_b64_tr_b16 a[206:207], v2 offset:12800\n"
    "v_mfma_f32_16x16x32_bf16 a[168:171], a[224:227], v[32:35], a[168:171]\n"
    "buffer_load_dwordx4 v[76:79], v226, s[36:39], s72 offen offset:1024\n"
    "v_mfma_f32_16x16x32_bf16 a[168:171], a[228:231], v[36:39], a[168:171]\n"
    "v_mfma_f32_16x16x32_bf16 a[172:175], a[224:227], v[40:43], a[172:175]\n"
    "v_mfma_f32_16x16x32_bf16 a[172:175], a[228:231], v[44:47], a[172:175]\n"
    "s_waitcnt lgkmcnt(8)\n"
    "s_add_u32 s69, s69, 2048\n"
    "v_mfma_f32_16x16x32_bf16 a[176:179], a[232:235], v[16:19], a[176:179]\n"
    "ds_read_b64_tr_b16 a[208:209], v3 offset:0\n"
    "s_add_u32 s70, s70, 2048\n"
    "v_mfma_f32_16x16x32_bf16 a[176:179], a[236:239], v[20:23], a[176:179]\n"
    "ds_read_b64_tr_b16 a[210:211], v3 offset:512\n"
    "s_add_u32 s71, s71, 2048\n"
    "v_mfma_f32_16x16x32_bf16 a[180:183], a[232:235], v[24:27], a[180:183]\n"
    "ds_read_b64_tr_b16 a[212:213], v3 offset:4096\n"
    "s_add_u32 s72, s72, 2048\n"
    "v_mfma_f32_16x16x32_bf16 a[180:183], a[236:239], v[28:31], a[180:183]\n"
    "ds_read_b64_tr_b16 a[214:215], v3 offset:4608\n"
    "buffer_load_dword v15, v10, s[40:43], s96 offen offset:0\n"
    "v_mfma_f32_16x16x32_bf16 a[184:187], a[232:235], v[32:35], a[184:187]\n"
    "buffer_load_dword v15, v11, s[20:23], s97 offen offset:0\n"
    "v_mfma_f32_16x16x32_bf16 a[184:187], a[236:239], v[36:39], a[184:187]\n"
    "v_mfma_f32_16x16x32_bf16 a[188:191], a[232:235], v[40:43], a[188:191]\n"
    "v_mfma_f32_16x16x32_bf16 a[188:191], a[236:239], v[44:47], a[188:191]\n"
    "s_waitcnt lgkmcnt(8)\n"
    "v_mfma_f32_16x16x32_bf16 v[112:115], a[192:195], v[16:19], v[112:115]\n"
    "ds_read_b64_tr_b16 a[216:217], v3 offset:8192\n"
    "v_mfma_f32_16x16x32_bf16 v[112:115], a[196:199], v[20:23], v[112:115]\n"
    "ds_read_b64_tr_b16 a[218:219], v3 offset:8704\n"
    "s_add_u32 s96, s96, 786432\n"
    "v_mfma_f32_16x16x32_bf16 v[116:119], a[192:195], v[24:27], v[116:119]\n"
    "ds_read_b64_tr_b16 a[220:221], v3 offset:12288\n"
    "s_add_u32 s97, s97, 196608\n"
    "v_mfma_f32_16x16x32_bf16 v[116:119], a[196:199], v[28:31], v[116:119]\n"
    "ds_read_b64_tr_b16 a[222:223], v3 offset:12800\n"
    "v_mfma_f32_16x16x32_bf16 v[120:123], a[192:195], v[32:35], v[120:123]\n"
    "v_mfma_f32_16x16x32_bf16 v[120:123], a[196:199], v[36:39], v[120:123]\n"
    "v_mfma_f32_16x16x32_bf16 v[124:127], a[192:195], v[40:43], v[124:127]\n"
    "v_mfma_f32_16x16x32_bf16 v[124:127], a[196:199], v[44:47], v[124:127]\n"
    "s_waitcnt lgkmcnt(8)\n"
    "v_mfma_f32_16x16x32_bf16 v[128:131], a[200:203], v[16:19], v[128:131]\n"
    "ds_read_b64_tr_b16 a[224:225], v4 offset:0\n"
    "v_mfma_f32_16x16x32_bf16 v[128:131], a[204:207], v[20:23], v[128:131]\n"
    "ds_read_b64_tr_b16 a[226:227], v4 offset:512\n"
    "v_mfma_f32_16x16x32_bf16 v[132:135], a[200:203], v[24:27], v[132:135]\n"
    "ds_read_b64_tr_b16 a[228:229], v4 offset:4096\n"
    "v_mfma_f32_16x16x32_bf16 v[132:135], a[204:207], v[28:31], v[132:135]\n"
    "ds_read_b64_tr_b16 a[230:231], v4 offset:4608\n"
    "v_mfma_f32_16x16x32_bf16 v[136:139], a[200:203], v[32:35], v[136:139]\n"
    "v_mfma_f32_16x16x32_bf16 v[136:139], a[204:207], v[36:39], v[136:139]\n"
    "v_mfma_f32_16x16x32_bf16 v[140:143], a[200:203], v[40:43], v[140:143]\n"
    "v_mfma_f32_16x16x32_bf16 v[140:143], a[204:207], v[44:47], v[140:143]\n"
    "s_waitcnt lgkmcnt(8)\n"
    "v_mfma_f32_16x16x32_bf16 v[144:147], a[208:211], v[16:19], v[144:147]\n"
    "ds_read_b64_tr_b16 a[232:233], v4 offset:8192\n"
    "v_mfma_f32_16x16x32_bf16 v[144:147], a[212:215], v[20:23], v[144:147]\n"
    "ds_read_b64_tr_b16 a[234:235], v4 offset:8704\n"
    "v_mfma_f32_16x16x32_bf16 v[148:151], a[208:211], v[24:27], v[148:151]\n"
    "ds_read_b64_tr_b16 a[236:237], v4 offset:12288\n"
    "v_mfma_f32_16x16x32_bf16 v[148:151], a[212:215], v[28:31], v[148:151]\n"
    "ds_read_b64_tr_b16 a[238:239], v4 offset:12800\n"
    "v_mfma_f32_16x16x32_bf16 v[152:155], a[208:211], v[32:35], v[152:155]\n"
    "v_mfma_f32_16x16x32_bf16 v[152:155], a[212:215], v[36:39], v[152:155]\n"
    "v_mfma_f32_16x16x32_bf16 v[156:159], a[208:211], v[40:43], v[156:159]\n"
    "v_mfma_f32_16x16x32_bf16 v[156:159], a[212:215], v[44:47], v[156:159]\n"
    "s_waitcnt lgkmcnt(8)\n"
    "v_mfma_f32_16x16x32_bf16 v[160:163], a[216:219], v[16:19], v[160:163]\n"
    "v_mfma_f32_16x16x32_bf16 v[160:163], a[220:223], v[20:23], v[160:163]\n"
    "v_mfma_f32_16x16x32_bf16 v[164:167], a[216:219], v[24:27], v[164:167]\n"
    "v_mfma_f32_16x16x32_bf16 v[164:167], a[220:223], v[28:31], v[164:167]\n"
    "v_mfma_f32_16x16x32_bf16 v[168:171], a[216:219], v[32:35], v[168:171]\n"
    "v_mfma_f32_16x16x32_bf16 v[168:171], a[220:223], v[36:39], v[168:171]\n"
    "v_mfma_f32_16x16x32_bf16 v[172:175], a[216:219], v[40:43], v[172:175]\n"
    "v_mfma_f32_16x16x32_bf16 v[172:175], a[220:223], v[44:47], v[172:175]\n"
    "s_waitcnt vmcnt(10)\n"
    "s_waitcnt lgkmcnt(0)\n"
    "s_barrier\n"
    "v_mfma_f32_16x16x32_bf16 v[176:179], a[224:227], v[16:19], v[176:179]\n"
    "ds_read_b64_tr_b16 a[192:193], v221 offset:40960\n"
    "v_mfma_f32_16x16x32_bf16 v[176:179], a[228:231], v[20:23], v[176:179]\n"
    "ds_read_b64_tr_b16 a[194:195], v221 offset:42496\n"
    "v_mfma_f32_16x16x32_bf16 v[180:183], a[224:227], v[24:27], v[180:183]\n"
    "ds_read_b64_tr_b16 a[196:197], v221 offset:53248\n"
    "v_mfma_f32_16x16x32_bf16 v[180:183], a[228:231], v[28:31], v[180:183]\n"
    "ds_read_b64_tr_b16 a[198:199], v221 offset:54784\n"
    "v_mfma_f32_16x16x32_bf16 v[184:187], a[224:227], v[32:35], v[184:187]\n"
    "ds_read_b64_tr_b16 a[200:201], v221 offset:40968\n"
    "v_mfma_f32_16x16x32_bf16 v[184:187], a[228:231], v[36:39], v[184:187]\n"
    "ds_read_b64_tr_b16 a[202:203], v221 offset:42504\n"
    "v_mfma_f32_16x16x32_bf16 v[188:191], a[224:227], v[40:43], v[188:191]\n"
    "ds_read_b64_tr_b16 a[204:205], v221 offset:53256\n"
    "v_mfma_f32_16x16x32_bf16 v[188:191], a[228:231], v[44:47], v[188:191]\n"
    "ds_read_b64_tr_b16 a[206:207], v221 offset:54792\n"
    "v_mfma_f32_16x16x32_bf16 v[192:195], a[232:235], v[16:19], v[192:195]\n"
    "v_mfma_f32_16x16x32_bf16 v[192:195], a[236:239], v[20:23], v[192:195]\n"
    "v_mfma_f32_16x16x32_bf16 v[196:199], a[232:235], v[24:27], v[196:199]\n"
    "v_mfma_f32_16x16x32_bf16 v[196:199], a[236:239], v[28:31], v[196:199]\n"
    "v_mfma_f32_16x16x32_bf16 v[200:203], a[232:235], v[32:35], v[200:203]\n"
    "v_mfma_f32_16x16x32_bf16 v[200:203], a[236:239], v[36:39], v[200:203]\n"
    "v_mfma_f32_16x16x32_bf16 v[204:207], a[232:235], v[40:43], v[204:207]\n"
    "v_mfma_f32_16x16x32_bf16 v[204:207], a[236:239], v[44:47], v[204:207]\n"
    "s_sub_u32 s68, s68, 1\n"
    "s_cbranch_scc0 L_qloop\n"
    "; Q chunk 7\n"
    "s_waitcnt lgkmcnt(4)\n"
    "ds_read_b64_tr_b16 a[208:209], v222 offset:40960\n"
    "s_waitcnt vmcnt(2)\n"
    "v_mfma_f32_16x16x32_bf16 a[0:3], a[192:195], v[48:51], a[0:3]\n"
    "ds_read_b64_tr_b16 a[210:211], v222 offset:42496\n"
    "s_add_u32 m0, s65, 73728\n"
    "v_mfma_f32_16x16x32_bf16 a[0:3], a[196:199], v[52:55], a[0:3]\n"
    "ds_read_b64_tr_b16 a[212:213], v222 offset:53248\n"
    "buffer_load_dwordx4 v223, s[40:43], s73 offen offset:0 lds\n"
    "v_mfma_f32_16x16x32_bf16 a[4:7], a[192:195], v[56:59], a[4:7]\n"
    "ds_read_b64_tr_b16 a[214:215], v222 offset:54784\n"
    "v_mfma_f32_16x16x32_bf16 a[4:7], a[196:199], v[60:63], a[4:7]\n"
    "ds_read_b64_tr_b16 a[216:217], v222 offset:40968\n"
    "v_mfma_f32_16x16x32_bf16 a[8:11], a[192:195], v[64:67], a[8:11]\n"
    "ds_read_b64_tr_b16 a[218:219], v222 offset:42504\n"
    "s_add_u32 m0, s65, 74752\n"
    "v_mfma_f32_16x16x32_bf16 a[8:11], a[196:199], v[68:71], a[8:11]\n"
    "ds_read_b64_tr_b16 a[220:221], v222 offset:53256\n"
    "buffer_load_dwordx4 v224, s[40:43], s73 offen offset:0 lds\n"
    "v_mfma_f32_16x16x32_bf16 a[12:15], a[192:195], v[72:75], a[12:15]\n"
    "ds_read_b64_tr_b16 a[222:223], v222 offset:54792\n"
    "v_mfma_f32_16x16x32_bf16 a[12:15], a[196:199], v[76:79], a[12:15]\n"
    "s_waitcnt lgkmcnt(8)\n"
    "v_mfma_f32_16x16x32_bf16 a[16:19], a[200:203], v[48:51], a[16:19]\n"
    "ds_read_b64_tr_b16 a[224:225], v221 offset:41088\n"
    "s_add_u32 m0, s65, 75776\n"
    "v_mfma_f32_16x16x32_bf16 a[16:19], a[204:207], v[52:55], a[16:19]\n"
    "ds_read_b64_tr_b16 a[226:227], v221 offset:42624\n"
    "v_mfma_f32_16x16x32_bf16 a[20:23], a[200:203], v[56:59], a[20:23]\n"
    "ds_read_b64_tr_b16 a[228:229], v221 offset:53376\n"
    "v_mfma_f32_16x16x32_bf16 a[20:23], a[204:207], v[60:63], a[20:23]\n"
    "ds_read_b64_tr_b16 a[230:231], v221 offset:54912\n"
    "v_mfma_f32_16x16x32_bf16 a[24:27], a[200:203], v[64:67], a[24:27]\n"
    "buffer_load_dwordx4 v225, s[40:43], s73 offen offset:0 lds\n"
    "v_mfma_f32_16x16x32_bf16 a[24:27], a[204:207], v[68:71], a[24:27]\n"
    "s_add_u32 m0, s65, 76800\n"
    "v_mfma_f32_16x16x32_bf16 a[28:31], a[200:203], v[72:75], a[28:31]\n"
    "buffer_load_dwordx4 v223, s[40:43], s74 offen offset:0 lds\n"
    "v_mfma_f32_16x16x32_bf16 a[28:31], a[204:207], v[76:79], a[28:31]\n"
    "s_waitcnt lgkmcnt(8)\n"
    "v_mfma_f32_16x16x32_bf16 a[32:35], a[208:211], v[48:51], a[32:35]\n"
    "ds_read_b64_tr_b16 a[232:233], v221 offset:41096\n"
    "s_add_u32 m0, s65, 77824\n"
    "v_mfma_f32_16x16x32_bf16 a[32:35], a[212:215], v[52:55], a[32:35]\n"
    "ds_read_b64_tr_b16 a[234:235], v221 offset:42632\n"
    "v_mfma_f32_16x16x32_bf16 a[36:39], a[208:211], v[56:59], a[36:39]\n"
    "ds_read_b64_tr_b16 a[236:237], v221 offset:53384\n"
    "v_mfma_f32_16x16x32_bf16 a[36:39], a[212:215], v[60:63], a[36:39]\n"
    "ds_read_b64_tr_b16 a[238:239], v221 offset:54920\n"
    "v_mfma_f32_16x16x32_bf16 a[40:43], a[208:211], v[64:67], a[40:43]\n"
    "buffer_load_dwordx4 v224, s[40:43], s74 offen offset:0 lds\n"
    "v_mfma_f32_16x16x32_bf16 a[40:43], a[212:215], v[68:71], a[40:43]\n"
    "s_add_u32 m0, s65, 78848\n"
    "v_mfma_f32_16x16x32_bf16 a[44:47], a[208:211], v[72:75], a[44:47]\n"
    "buffer_load_dwordx4 v225, s[40:43], s74 offen offset:0 lds\n"
    "v_mfma_f32_16x16x32_bf16 a[44:47], a[212:215], v[76:79], a[44:47]\n"
    "s_waitcnt lgkmcnt(8)\n"
    "v_mfma_f32_16x16x32_bf16 a[48:51], a[216:219], v[48:51], a[48:51]\n"
    "ds_read_b64_tr_b16 a[192:193], v222 offset:41088\n"
    "s_add_u32 m0, s29, 98304\n"
    "v_mfma_f32_16x16x32_bf16 a[48:51], a[220:223], v[52:55], a[48:51]\n"
    "ds_read_b64_tr_b16 a[194:195], v222 offset:42624\n"
    "v_mfma_f32_16x16x32_bf16 a[52:55], a[216:219], v[56:59], a[52:55]\n"
    "ds_read_b64_tr_b16 a[196:197], v222 offset:53376\n"
    "v_mfma_f32_16x16x32_bf16 a[52:55], a[220:223], v[60:63], a[52:55]\n"
    "ds_read_b64_tr_b16 a[198:199], v222 offset:54912\n"
    "v_mfma_f32_16x16x32_bf16 a[56:59], a[216:219], v[64:67], a[56:59]\n"
    "buffer_load_dwordx4 v5, s[20:23], s28 offen offset:0 lds\n"
    "v_mfma_f32_16x16x32_bf16 a[56:59], a[220:223], v[68:71], a[56:59]\n"
    "buffer_load_dwordx4 v6, s[20:23], s28 offen offset:1024 lds\n"
    "v_mfma_f32_16x16x32_bf16 a[60:63], a[216:219], v[72:75], a[60:63]\n"
    "v_mfma_f32_16x16x32_bf16 a[60:63], a[220:223], v[76:79], a[60:63]\n"
    "s_waitcnt lgkmcnt(8)\n"
    "v_mfma_f32_16x16x32_bf16 a[64:67], a[224:227], v[48:51], a[64:67]\n"
    "ds_read_b64_tr_b16 a[200:201], v222 offset:41096\n"
    "v_mfma_f32_16x16x32_bf16 a[64:67], a[228:231], v[52:55], a[64:67]\n"
    "ds_read_b64_tr_b16 a[202:203], v222 offset:42632\n"
    "v_mfma_f32_16x16x32_bf16 a[68:71], a[224:227], v[56:59], a[68:71]\n"
    "ds_read_b64_tr_b16 a[204:205], v222 offset:53384\n"
    "v_mfma_f32_16x16x32_bf16 a[68:71], a[228:231], v[60:63], a[68:71]\n"
    "ds_read_b64_tr_b16 a[206:207], v222 offset:54920\n"
    "v_mfma_f32_16x16x32_bf16 a[72:75], a[224:227], v[64:67], a[72:75]\n"
    "buffer_load_dwordx4 v7, s[20:23], s28 offen offset:2048 lds\n"
    "v_mfma_f32_16x16x32_bf16 a[72:75], a[228:231], v[68:71], a[72:75]\n"
    "buffer_load_dwordx4 v8, s[20:23], s28 offen offset:3072 lds\n"
    "v_mfma_f32_16x16x32_bf16 a[76:79], a[224:227], v[72:75], a[76:79]\n"
    "v_mfma_f32_16x16x32_bf16 a[76:79], a[228:231], v[76:79], a[76:79]\n"
    "s_waitcnt lgkmcnt(8)\n"
    "v_mfma_f32_16x16x32_bf16 a[80:83], a[232:235], v[48:51], a[80:83]\n"
    "ds_read_b64_tr_b16 a[208:209], v221 offset:41216\n"
    "s_add_u32 s73, s73, 786432\n"
    "v_mfma_f32_16x16x32_bf16 a[80:83], a[236:239], v[52:55], a[80:83]\n"
    "ds_read_b64_tr_b16 a[210:211], v221 offset:42752\n"
    "s_add_u32 s74, s74, 786432\n"
    "v_mfma_f32_16x16x32_bf16 a[84:87], a[232:235], v[56:59], a[84:87]\n"
    "ds_read_b64_tr_b16 a[212:213], v221 offset:53504\n"
    "s_add_u32 s28, s28, 196608\n"
    "v_mfma_f32_16x16x32_bf16 a[84:87], a[236:239], v[60:63], a[84:87]\n"
    "ds_read_b64_tr_b16 a[214:215], v221 offset:55040\n"
    "v_mfma_f32_16x16x32_bf16 a[88:91], a[232:235], v[64:67], a[88:91]\n"
    "buffer_load_dwordx4 v[16:19], v226, s[36:39], s69 offen offset:0\n"
    "v_mfma_f32_16x16x32_bf16 a[88:91], a[236:239], v[68:71], a[88:91]\n"
    "buffer_load_dwordx4 v[20:23], v226, s[36:39], s69 offen offset:1024\n"
    "v_mfma_f32_16x16x32_bf16 a[92:95], a[232:235], v[72:75], a[92:95]\n"
    "v_mfma_f32_16x16x32_bf16 a[92:95], a[236:239], v[76:79], a[92:95]\n"
    "s_waitcnt lgkmcnt(8)\n"
    "v_mfma_f32_16x16x32_bf16 a[96:99], a[192:195], v[48:51], a[96:99]\n"
    "ds_read_b64_tr_b16 a[216:217], v221 offset:41224\n"
    "v_mfma_f32_16x16x32_bf16 a[96:99], a[196:199], v[52:55], a[96:99]\n"
    "ds_read_b64_tr_b16 a[218:219], v221 offset:42760\n"
    "v_mfma_f32_16x16x32_bf16 a[100:103], a[192:195], v[56:59], a[100:103]\n"
    "ds_read_b64_tr_b16 a[220:221], v221 offset:53512\n"
    "v_mfma_f32_16x16x32_bf16 a[100:103], a[196:199], v[60:63], a[100:103]\n"
    "ds_read_b64_tr_b16 a[222:223], v221 offset:55048\n"
    "v_mfma_f32_16x16x32_bf16 a[104:107], a[192:195], v[64:67], a[104:107]\n"
    "buffer_load_dwordx4 v[24:27], v226, s[36:39], s70 offen offset:0\n"
    "v_mfma_f32_16x16x32_bf16 a[104:107], a[196:199], v[68:71], a[104:107]\n"
    "buffer_load_dwordx4 v[28:31], v226, s[36:39], s70 offen offset:1024\n"
    "v_mfma_f32_16x16x32_bf16 a[108:111], a[192:195], v[72:75], a[108:111]\n"
    "v_mfma_f32_16x16x32_bf16 a[108:111], a[196:199], v[76:79], a[108:111]\n"
    "s_waitcnt lgkmcnt(8)\n"
    "v_mfma_f32_16x16x32_bf16 a[112:115], a[200:203], v[48:51], a[112:115]\n"
    "ds_read_b64_tr_b16 a[224:225], v222 offset:41216\n"
    "v_mfma_f32_16x16x32_bf16 a[112:115], a[204:207], v[52:55], a[112:115]\n"
    "ds_read_b64_tr_b16 a[226:227], v222 offset:42752\n"
    "v_mfma_f32_16x16x32_bf16 a[116:119], a[200:203], v[56:59], a[116:119]\n"
    "ds_read_b64_tr_b16 a[228:229], v222 offset:53504\n"
    "v_mfma_f32_16x16x32_bf16 a[116:119], a[204:207], v[60:63], a[116:119]\n"
    "ds_read_b64_tr_b16 a[230:231], v222 offset:55040\n"
    "v_mfma_f32_16x16x32_bf16 a[120:123], a[200:203], v[64:67], a[120:123]\n"
    "buffer_load_dwordx4 v[32:35], v226, s[36:39], s71 offen offset:0\n"
    "v_mfma_f32_16x16x32_bf16 a[120:123], a[204:207], v[68:71], a[120:123]\n"
    "buffer_load_dwordx4 v[36:39], v226, s[36:39], s71 offen offset:1024\n"
    "v_mfma_f32_16x16x32_bf16 a[124:127], a[200:203], v[72:75], a[124:127]\n"
    "v_mfma_f32_16x16x32_bf16 a[124:127], a[204:207], v[76:79], a[124:127]\n"
    "s_waitcnt lgkmcnt(8)\n"
    "v_mfma_f32_16x16x32_bf16 a[128:131], a[208:211], v[48:51], a[128:131]\n"
    "ds_read_b64_tr_b16 a[232:233], v222 offset:41224\n"
    "v_mfma_f32_16x16x32_bf16 a[128:131], a[212:215], v[52:55], a[128:131]\n"
    "ds_read_b64_tr_b16 a[234:235], v222 offset:42760\n"
    "v_mfma_f32_16x16x32_bf16 a[132:135], a[208:211], v[56:59], a[132:135]\n"
    "ds_read_b64_tr_b16 a[236:237], v222 offset:53512\n"
    "v_mfma_f32_16x16x32_bf16 a[132:135], a[212:215], v[60:63], a[132:135]\n"
    "ds_read_b64_tr_b16 a[238:239], v222 offset:55048\n"
    "v_mfma_f32_16x16x32_bf16 a[136:139], a[208:211], v[64:67], a[136:139]\n"
    "buffer_load_dwordx4 v[40:43], v226, s[36:39], s72 offen offset:0\n"
    "v_mfma_f32_16x16x32_bf16 a[136:139], a[212:215], v[68:71], a[136:139]\n"
    "buffer_load_dwordx4 v[44:47], v226, s[36:39], s72 offen offset:1024\n"
    "v_mfma_f32_16x16x32_bf16 a[140:143], a[208:211], v[72:75], a[140:143]\n"
    "v_mfma_f32_16x16x32_bf16 a[140:143], a[212:215], v[76:79], a[140:143]\n"
    "s_waitcnt lgkmcnt(8)\n"
    "v_mfma_f32_16x16x32_bf16 a[144:147], a[216:219], v[48:51], a[144:147]\n"
    "ds_read_b64_tr_b16 a[192:193], v2 offset:40960\n"
    "s_add_u32 s69, s69, 2048\n"
    "v_mfma_f32_16x16x32_bf16 a[144:147], a[220:223], v[52:55], a[144:147]\n"
    "ds_read_b64_tr_b16 a[194:195], v2 offset:41472\n"
    "s_add_u32 s70, s70, 2048\n"
    "v_mfma_f32_16x16x32_bf16 a[148:151], a[216:219], v[56:59], a[148:151]\n"
    "ds_read_b64_tr_b16 a[196:197], v2 offset:45056\n"
    "s_add_u32 s71, s71, 2048\n"
    "v_mfma_f32_16x16x32_bf16 a[148:151], a[220:223], v[60:63], a[148:151]\n"
    "ds_read_b64_tr_b16 a[198:199], v2 offset:45568\n"
    "s_add_u32 s72, s72, 2048\n"
    "v_mfma_f32_16x16x32_bf16 a[152:155], a[216:219], v[64:67], a[152:155]\n"
    "s_add_u32 m0, s65, 0\n"
    "s_nop 0\n"
    "buffer_load_dwordx4 v218, s[44:47], s65 offen offset:0 lds\n"
    "v_mfma_f32_16x16x32_bf16 a[152:155], a[220:223], v[68:71], a[152:155]\n"
    "buffer_load_dwordx4 v219, s[44:47], s65 offen offset:1024 lds\n"
    "v_mfma_f32_16x16x32_bf16 a[156:159], a[216:219], v[72:75], a[156:159]\n"
    "v_mfma_f32_16x16x32_bf16 a[156:159], a[220:223], v[76:79], a[156:159]\n"
    "s_waitcnt lgkmcnt(8)\n"
    "v_mfma_f32_16x16x32_bf16 a[160:163], a[224:227], v[48:51], a[160:163]\n"
    "ds_read_b64_tr_b16 a[200:201], v2 offset:49152\n"
    "v_mfma_f32_16x16x32_bf16 a[160:163], a[228:231], v[52:55], a[160:163]\n"
    "ds_read_b64_tr_b16 a[202:203], v2 offset:49664\n"
    "v_mfma_f32_16x16x32_bf16 a[164:167], a[224:227], v[56:59], a[164:167]\n"
    "ds_read_b64_tr_b16 a[204:205], v2 offset:53248\n"
    "v_mfma_f32_16x16x32_bf16 a[164:167], a[228:231], v[60:63], a[164:167]\n"
    "ds_read_b64_tr_b16 a[206:207], v2 offset:53760\n"
    "v_mfma_f32_16x16x32_bf16 a[168:171], a[224:227], v[64:67], a[168:171]\n"
    "buffer_load_dwordx4 v220, s[44:47], s65 offen offset:2048 lds\n"
    "v_mfma_f32_16x16x32_bf16 a[168:171], a[228:231], v[68:71], a[168:171]\n"
    "buffer_load_dwordx4 v218, s[44:47], s65 offen offset:3072 lds\n"
    "v_mfma_f32_16x16x32_bf16 a[172:175], a[224:227], v[72:75], a[172:175]\n"
    "v_mfma_f32_16x16x32_bf16 a[172:175], a[228:231], v[76:79], a[172:175]\n"
    "s_waitcnt lgkmcnt(8)\n"
    "v_mfma_f32_16x16x32_bf16 a[176:179], a[232:235], v[48:51], a[176:179]\n"
    "ds_read_b64_tr_b16 a[208:209], v3 offset:40960\n"
    "s_add_u32 m0, s65, 4096\n"
    "v_mfma_f32_16x16x32_bf16 a[176:179], a[236:239], v[52:55], a[176:179]\n"
    "ds_read_b64_tr_b16 a[210:211], v3 offset:41472\n"
    "v_mfma_f32_16x16x32_bf16 a[180:183], a[232:235], v[56:59], a[180:183]\n"
    "ds_read_b64_tr_b16 a[212:213], v3 offset:45056\n"
    "v_mfma_f32_16x16x32_bf16 a[180:183], a[236:239], v[60:63], a[180:183]\n"
    "ds_read_b64_tr_b16 a[214:215], v3 offset:45568\n"
    "v_mfma_f32_16x16x32_bf16 a[184:187], a[232:235], v[64:67], a[184:187]\n"
    "buffer_load_dwordx4 v219, s[48:51], s65 offen offset:0 lds\n"
    "v_mfma_f32_16x16x32_bf16 a[184:187], a[236:239], v[68:71], a[184:187]\n"
    "buffer_load_dwordx4 v220, s[48:51], s65 offen offset:1024 lds\n"
    "v_mfma_f32_16x16x32_bf16 a[188:191], a[232:235], v[72:75], a[188:191]\n"
    "v_mfma_f32_16x16x32_bf16 a[188:191], a[236:239], v[76:79], a[188:191]\n"
    "s_waitcnt lgkmcnt(8)\n"
    "v_mfma_f32_16x16x32_bf16 v[112:115], a[192:195], v[48:51], v[112:115]\n"
    "ds_read_b64_tr_b16 a[216:217], v3 offset:49152\n"
    "v_mfma_f32_16x16x32_bf16 v[112:115], a[196:199], v[52:55], v[112:115]\n"
    "ds_read_b64_tr_b16 a[218:219], v3 offset:49664\n"
    "v_mfma_f32_16x16x32_bf16 v[116:119], a[192:195], v[56:59], v[116:119]\n"
    "ds_read_b64_tr_b16 a[220:221], v3 offset:53248\n"
    "v_mfma_f32_16x16x32_bf16 v[116:119], a[196:199], v[60:63], v[116:119]\n"
    "ds_read_b64_tr_b16 a[222:223], v3 offset:53760\n"
    "v_mfma_f32_16x16x32_bf16 v[120:123], a[192:195], v[64:67], v[120:123]\n"
    "v_mfma_f32_16x16x32_bf16 v[120:123], a[196:199], v[68:71], v[120:123]\n"
    "v_mfma_f32_16x16x32_bf16 v[124:127], a[192:195], v[72:75], v[124:127]\n"
    "v_mfma_f32_16x16x32_bf16 v[124:127], a[196:199], v[76:79], v[124:127]\n"
    "s_waitcnt lgkmcnt(8)\n"
    "v_mfma_f32_16x16x32_bf16 v[128:131], a[200:203], v[48:51], v[128:131]\n"
    "ds_read_b64_tr_b16 a[224:225], v4 offset:40960\n"
    "v_mfma_f32_16x16x32_bf16 v[128:131], a[204:207], v[52:55], v[128:131]\n"
    "ds_read_b64_tr_b16 a[226:227], v4 offset:41472\n"
    "v_mfma_f32_16x16x32_bf16 v[132:135], a[200:203], v[56:59], v[132:135]\n"
    "ds_read_b64_tr_b16 a[228:229], v4 offset:45056\n"
    "v_mfma_f32_16x16x32_bf16 v[132:135], a[204:207], v[60:63], v[132:135]\n"
    "ds_read_b64_tr_b16 a[230:231], v4 offset:45568\n"
    "v_mfma_f32_16x16x32_bf16 v[136:139], a[200:203], v[64:67], v[136:139]\n"
    "v_mfma_f32_16x16x32_bf16 v[136:139], a[204:207], v[68:71], v[136:139]\n"
    "v_mfma_f32_16x16x32_bf16 v[140:143], a[200:203], v[72:75], v[140:143]\n"
    "v_mfma_f32_16x16x32_bf16 v[140:143], a[204:207], v[76:79], v[140:143]\n"
    "s_waitcnt lgkmcnt(8)\n"
    "v_mfma_f32_16x16x32_bf16 v[144:147], a[208:211], v[48:51], v[144:147]\n"
    "ds_read_b64_tr_b16 a[232:233], v4 offset:49152\n"
    "v_mfma_f32_16x16x32_bf16 v[144:147], a[212:215], v[52:55], v[144:147]\n"
    "ds_read_b64_tr_b16 a[234:235], v4 offset:49664\n"
    "v_mfma_f32_16x16x32_bf16 v[148:151], a[208:211], v[56:59], v[148:151]\n"
    "ds_read_b64_tr_b16 a[236:237], v4 offset:53248\n"
    "v_mfma_f32_16x16x32_bf16 v[148:151], a[212:215], v[60:63], v[148:151]\n"
    "ds_read_b64_tr_b16 a[238:239], v4 offset:53760\n"
    "v_mfma_f32_16x16x32_bf16 v[152:155], a[208:211], v[64:67], v[152:155]\n"
    "v_mfma_f32_16x16x32_bf16 v[152:155], a[212:215], v[68:71], v[152:155]\n"
    "v_mfma_f32_16x16x32_bf16 v[156:159], a[208:211], v[72:75], v[156:159]\n"
    "v_mfma_f32_16x16x32_bf16 v[156:159], a[212:215], v[76:79], v[156:159]\n"
    "s_waitcnt lgkmcnt(8)\n"
    "v_mfma_f32_16x16x32_bf16 v[160:163], a[216:219], v[48:51], v[160:163]\n"
    "v_mfma_f32_16x16x32_bf16 v[160:163], a[220:223], v[52:55], v[160:163]\n"
    "v_mfma_f32_16x16x32_bf16 v[164:167], a[216:219], v[56:59], v[164:167]\n"
    "v_mfma_f32_16x16x32_bf16 v[164:167], a[220:223], v[60:63], v[164:167]\n"
    "v_mfma_f32_16x16x32_bf16 v[168:171], a[216:219], v[64:67], v[168:171]\n"
    "v_mfma_f32_16x16x32_bf16 v[168:171], a[220:223], v[68:71], v[168:171]\n"
    "v_mfma_f32_16x16x32_bf16 v[172:175], a[216:219], v[72:75], v[172:175]\n"
    "v_mfma_f32_16x16x32_bf16 v[172:175], a[220:223], v[76:79], v[172:175]\n"
    "s_waitcnt vmcnt(14)\n"
    "s_waitcnt lgkmcnt(0)\n"
    "s_barrier\n"
    "v_mfma_f32_16x16x32_bf16 v[176:179], a[224:227], v[48:51], v[176:179]\n"
    "ds_read_b64_tr_b16 a[192:193], v221 offset:0\n"
    "v_mfma_f32_16x16x32_bf16 v[176:179], a[228:231], v[52:55], v[176:179]\n"
    "ds_read_b64_tr_b16 a[194:195], v221 offset:1536\n"
    "v_mfma_f32_16x16x32_bf16 v[180:183], a[224:227], v[56:59], v[180:183]\n"
    "ds_read_b64_tr_b16 a[196:197], v221 offset:12288\n"
    "v_mfma_f32_16x16x32_bf16 v[180:183], a[228:231], v[60:63], v[180:183]\n"
    "ds_read_b64_tr_b16 a[198:199], v221 offset:13824\n"
    "v_mfma_f32_16x16x32_bf16 v[184:187], a[224:227], v[64:67], v[184:187]\n"
    "ds_read_b64_tr_b16 a[200:201], v221 offset:8\n"
    "v_mfma_f32_16x16x32_bf16 v[184:187], a[228:231], v[68:71], v[184:187]\n"
    "ds_read_b64_tr_b16 a[202:203], v221 offset:1544\n"
    "v_mfma_f32_16x16x32_bf16 v[188:191], a[224:227], v[72:75], v[188:191]\n"
    "ds_read_b64_tr_b16 a[204:205], v221 offset:12296\n"
    "v_mfma_f32_16x16x32_bf16 v[188:191], a[228:231], v[76:79], v[188:191]\n"
    "ds_read_b64_tr_b16 a[206:207], v221 offset:13832\n"
    "v_mfma_f32_16x16x32_bf16 v[192:195], a[232:235], v[48:51], v[192:195]\n"
    "v_mfma_f32_16x16x32_bf16 v[192:195], a[236:239], v[52:55], v[192:195]\n"
    "v_mfma_f32_16x16x32_bf16 v[196:199], a[232:235], v[56:59], v[196:199]\n"
    "v_mfma_f32_16x16x32_bf16 v[196:199], a[236:239], v[60:63], v[196:199]\n"
    "v_mfma_f32_16x16x32_bf16 v[200:203], a[232:235], v[64:67], v[200:203]\n"
    "v_mfma_f32_16x16x32_bf16 v[200:203], a[236:239], v[68:71], v[200:203]\n"
    "v_mfma_f32_16x16x32_bf16 v[204:207], a[232:235], v[72:75], v[204:207]\n"
    "v_mfma_f32_16x16x32_bf16 v[204:207], a[236:239], v[76:79], v[204:207]\n"
    "; Q chunk 8\n"
    "s_waitcnt lgkmcnt(4)\n"
    "ds_read_b64_tr_b16 a[208:209], v222 offset:0\n"
    "s_waitcnt vmcnt(6)\n"
    "v_mfma_f32_16x16x32_bf16 a[0:3], a[192:195], v[16:19], a[0:3]\n"
    "ds_read_b64_tr_b16 a[210:211], v222 offset:1536\n"
    "s_add_u32 m0, s65, 114688\n"
    "v_mfma_f32_16x16x32_bf16 a[0:3], a[196:199], v[20:23], a[0:3]\n"
    "ds_read_b64_tr_b16 a[212:213], v222 offset:12288\n"
    "buffer_load_dwordx4 v223, s[40:43], s73 offen offset:0 lds\n"
    "v_mfma_f32_16x16x32_bf16 a[4:7], a[192:195], v[24:27], a[4:7]\n"
    "ds_read_b64_tr_b16 a[214:215], v222 offset:13824\n"
    "v_mfma_f32_16x16x32_bf16 a[4:7], a[196:199], v[28:31], a[4:7]\n"
    "ds_read_b64_tr_b16 a[216:217], v222 offset:8\n"
    "v_mfma_f32_16x16x32_bf16 a[8:11], a[192:195], v[32:35], a[8:11]\n"
    "ds_read_b64_tr_b16 a[218:219], v222 offset:1544\n"
    "s_add_u32 m0, s65, 115712\n"
    "v_mfma_f32_16x16x32_bf16 a[8:11], a[196:199], v[36:39], a[8:11]\n"
    "ds_read_b64_tr_b16 a[220:221], v222 offset:12296\n"
    "buffer_load_dwordx4 v224, s[40:43], s73 offen offset:0 lds\n"
    "v_mfma_f32_16x16x32_bf16 a[12:15], a[192:195], v[40:43], a[12:15]\n"
    "ds_read_b64_tr_b16 a[222:223], v222 offset:13832\n"
    "v_mfma_f32_16x16x32_bf16 a[12:15], a[196:199], v[44:47], a[12:15]\n"
    "s_waitcnt lgkmcnt(8)\n"
    "v_mfma_f32_16x16x32_bf16 a[16:19], a[200:203], v[16:19], a[16:19]\n"
    "ds_read_b64_tr_b16 a[224:225], v221 offset:128\n"
    "s_add_u32 m0, s65, 116736\n"
    "v_mfma_f32_16x16x32_bf16 a[16:19], a[204:207], v[20:23], a[16:19]\n"
    "ds_read_b64_tr_b16 a[226:227], v221 offset:1664\n"
    "v_mfma_f32_16x16x32_bf16 a[20:23], a[200:203], v[24:27], a[20:23]\n"
    "ds_read_b64_tr_b16 a[228:229], v221 offset:12416\n"
    "v_mfma_f32_16x16x32_bf16 a[20:23], a[204:207], v[28:31], a[20:23]\n"
    "ds_read_b64_tr_b16 a[230:231], v221 offset:13952\n"
    "v_mfma_f32_16x16x32_bf16 a[24:27], a[200:203], v[32:35], a[24:27]\n"
    "buffer_load_dwordx4 v225, s[40:43], s73 offen offset:0 lds\n"
    "v_mfma_f32_16x16x32_bf16 a[24:27], a[204:207], v[36:39], a[24:27]\n"
    "s_add_u32 m0, s65, 117760\n"
    "v_mfma_f32_16x16x32_bf16 a[28:31], a[200:203], v[40:43], a[28:31]\n"
    "buffer_load_dwordx4 v223, s[40:43], s74 offen offset:0 lds\n"
    "v_mfma_f32_16x16x32_bf16 a[28:31], a[204:207], v[44:47], a[28:31]\n"
    "s_waitcnt lgkmcnt(8)\n"
    "v_mfma_f32_16x16x32_bf16 a[32:35], a[208:211], v[16:19], a[32:35]\n"
    "ds_read_b64_tr_b16 a[232:233], v221 offset:136\n"
    "s_add_u32 m0, s65, 118784\n"
    "v_mfma_f32_16x16x32_bf16 a[32:35], a[212:215], v[20:23], a[32:35]\n"
    "ds_read_b64_tr_b16 a[234:235], v221 offset:1672\n"
    "v_mfma_f32_16x16x32_bf16 a[36:39], a[208:211], v[24:27], a[36:39]\n"
    "ds_read_b64_tr_b16 a[236:237], v221 offset:12424\n"
    "v_mfma_f32_16x16x32_bf16 a[36:39], a[212:215], v[28:31], a[36:39]\n"
    "ds_read_b64_tr_b16 a[238:239], v221 offset:13960\n"
    "v_mfma_f32_16x16x32_bf16 a[40:43], a[208:211], v[32:35], a[40:43]\n"
    "buffer_load_dwordx4 v224, s[40:43], s74 offen offset:0 lds\n"
    "v_mfma_f32_16x16x32_bf16 a[40:43], a[212:215], v[36:39], a[40:43]\n"
    "s_add_u32 m0, s65, 119808\n"
    "v_mfma_f32_16x16x32_bf16 a[44:47], a[208:211], v[40:43], a[44:47]\n"
    "buffer_load_dwordx4 v225, s[40:43], s74 offen offset:0 lds\n"
    "v_mfma_f32_16x16x32_bf16 a[44:47], a[212:215], v[44:47], a[44:47]\n"
    "s_waitcnt lgkmcnt(8)\n"
    "v_mfma_f32_16x16x32_bf16 a[48:51], a[216:219], v[16:19], a[48:51]\n"
    "ds_read_b64_tr_b16 a[192:193], v222 offset:128\n"
    "s_add_u32 m0, s29, 139264\n"
    "v_mfma_f32_16x16x32_bf16 a[48:51], a[220:223], v[20:23], a[48:51]\n"
    "ds_read_b64_tr_b16 a[194:195], v222 offset:1664\n"
    "v_mfma_f32_16x16x32_bf16 a[52:55], a[216:219], v[24:27], a[52:55]\n"
    "ds_read_b64_tr_b16 a[196:197], v222 offset:12416\n"
    "v_mfma_f32_16x16x32_bf16 a[52:55], a[220:223], v[28:31], a[52:55]\n"
    "ds_read_b64_tr_b16 a[198:199], v222 offset:13952\n"
    "v_mfma_f32_16x16x32_bf16 a[56:59], a[216:219], v[32:35], a[56:59]\n"
    "buffer_load_dwordx4 v5, s[20:23], s28 offen offset:0 lds\n"
    "v_mfma_f32_16x16x32_bf16 a[56:59], a[220:223], v[36:39], a[56:59]\n"
    "buffer_load_dwordx4 v6, s[20:23], s28 offen offset:1024 lds\n"
    "v_mfma_f32_16x16x32_bf16 a[60:63], a[216:219], v[40:43], a[60:63]\n"
    "v_mfma_f32_16x16x32_bf16 a[60:63], a[220:223], v[44:47], a[60:63]\n"
    "s_waitcnt lgkmcnt(8)\n"
    "v_mfma_f32_16x16x32_bf16 a[64:67], a[224:227], v[16:19], a[64:67]\n"
    "ds_read_b64_tr_b16 a[200:201], v222 offset:136\n"
    "v_mfma_f32_16x16x32_bf16 a[64:67], a[228:231], v[20:23], a[64:67]\n"
    "ds_read_b64_tr_b16 a[202:203], v222 offset:1672\n"
    "v_mfma_f32_16x16x32_bf16 a[68:71], a[224:227], v[24:27], a[68:71]\n"
    "ds_read_b64_tr_b16 a[204:205], v222 offset:12424\n"
    "v_mfma_f32_16x16x32_bf16 a[68:71], a[228:231], v[28:31], a[68:71]\n"
    "ds_read_b64_tr_b16 a[206:207], v222 offset:13960\n"
    "v_mfma_f32_16x16x32_bf16 a[72:75], a[224:227], v[32:35], a[72:75]\n"
    "buffer_load_dwordx4 v7, s[20:23], s28 offen offset:2048 lds\n"
    "v_mfma_f32_16x16x32_bf16 a[72:75], a[228:231], v[36:39], a[72:75]\n"
    "buffer_load_dwordx4 v8, s[20:23], s28 offen offset:3072 lds\n"
    "v_mfma_f32_16x16x32_bf16 a[76:79], a[224:227], v[40:43], a[76:79]\n"
    "v_mfma_f32_16x16x32_bf16 a[76:79], a[228:231], v[44:47], a[76:79]\n"
    "s_waitcnt lgkmcnt(8)\n"
    "v_mfma_f32_16x16x32_bf16 a[80:83], a[232:235], v[16:19], a[80:83]\n"
    "ds_read_b64_tr_b16 a[208:209], v221 offset:256\n"
    "s_add_u32 s73, s73, 786432\n"
    "v_mfma_f32_16x16x32_bf16 a[80:83], a[236:239], v[20:23], a[80:83]\n"
    "ds_read_b64_tr_b16 a[210:211], v221 offset:1792\n"
    "s_add_u32 s74, s74, 786432\n"
    "v_mfma_f32_16x16x32_bf16 a[84:87], a[232:235], v[24:27], a[84:87]\n"
    "ds_read_b64_tr_b16 a[212:213], v221 offset:12544\n"
    "s_add_u32 s28, s28, 196608\n"
    "v_mfma_f32_16x16x32_bf16 a[84:87], a[236:239], v[28:31], a[84:87]\n"
    "ds_read_b64_tr_b16 a[214:215], v221 offset:14080\n"
    "v_mfma_f32_16x16x32_bf16 a[88:91], a[232:235], v[32:35], a[88:91]\n"
    "buffer_load_dwordx4 v[48:51], v226, s[36:39], s69 offen offset:0\n"
    "v_mfma_f32_16x16x32_bf16 a[88:91], a[236:239], v[36:39], a[88:91]\n"
    "buffer_load_dwordx4 v[52:55], v226, s[36:39], s69 offen offset:1024\n"
    "v_mfma_f32_16x16x32_bf16 a[92:95], a[232:235], v[40:43], a[92:95]\n"
    "v_mfma_f32_16x16x32_bf16 a[92:95], a[236:239], v[44:47], a[92:95]\n"
    "s_waitcnt lgkmcnt(8)\n"
    "v_mfma_f32_16x16x32_bf16 a[96:99], a[192:195], v[16:19], a[96:99]\n"
    "ds_read_b64_tr_b16 a[216:217], v221 offset:264\n"
    "v_mfma_f32_16x16x32_bf16 a[96:99], a[196:199], v[20:23], a[96:99]\n"
    "ds_read_b64_tr_b16 a[218:219], v221 offset:1800\n"
    "v_mfma_f32_16x16x32_bf16 a[100:103], a[192:195], v[24:27], a[100:103]\n"
    "ds_read_b64_tr_b16 a[220:221], v221 offset:12552\n"
    "v_mfma_f32_16x16x32_bf16 a[100:103], a[196:199], v[28:31], a[100:103]\n"
    "ds_read_b64_tr_b16 a[222:223], v221 offset:14088\n"
    "v_mfma_f32_16x16x32_bf16 a[104:107], a[192:195], v[32:35], a[104:107]\n"
    "buffer_load_dwordx4 v[56:59], v226, s[36:39], s70 offen offset:0\n"
    "v_mfma_f32_16x16x32_bf16 a[104:107], a[196:199], v[36:39], a[104:107]\n"
    "buffer_load_dwordx4 v[60:63], v226, s[36:39], s70 offen offset:1024\n"
    "v_mfma_f32_16x16x32_bf16 a[108:111], a[192:195], v[40:43], a[108:111]\n"
    "v_mfma_f32_16x16x32_bf16 a[108:111], a[196:199], v[44:47], a[108:111]\n"
    "s_waitcnt lgkmcnt(8)\n"
    "v_mfma_f32_16x16x32_bf16 a[112:115], a[200:203], v[16:19], a[112:115]\n"
    "ds_read_b64_tr_b16 a[224:225], v222 offset:256\n"
    "v_mfma_f32_16x16x32_bf16 a[112:115], a[204:207], v[20:23], a[112:115]\n"
    "ds_read_b64_tr_b16 a[226:227], v222 offset:1792\n"
    "v_mfma_f32_16x16x32_bf16 a[116:119], a[200:203], v[24:27], a[116:119]\n"
    "ds_read_b64_tr_b16 a[228:229], v222 offset:12544\n"
    "v_mfma_f32_16x16x32_bf16 a[116:119], a[204:207], v[28:31], a[116:119]\n"
    "ds_read_b64_tr_b16 a[230:231], v222 offset:14080\n"
    "v_mfma_f32_16x16x32_bf16 a[120:123], a[200:203], v[32:35], a[120:123]\n"
    "buffer_load_dwordx4 v[64:67], v226, s[36:39], s71 offen offset:0\n"
    "v_mfma_f32_16x16x32_bf16 a[120:123], a[204:207], v[36:39], a[120:123]\n"
    "buffer_load_dwordx4 v[68:71], v226, s[36:39], s71 offen offset:1024\n"
    "v_mfma_f32_16x16x32_bf16 a[124:127], a[200:203], v[40:43], a[124:127]\n"
    "v_mfma_f32_16x16x32_bf16 a[124:127], a[204:207], v[44:47], a[124:127]\n"
    "s_waitcnt lgkmcnt(8)\n"
    "v_mfma_f32_16x16x32_bf16 a[128:131], a[208:211], v[16:19], a[128:131]\n"
    "ds_read_b64_tr_b16 a[232:233], v222 offset:264\n"
    "v_mfma_f32_16x16x32_bf16 a[128:131], a[212:215], v[20:23], a[128:131]\n"
    "ds_read_b64_tr_b16 a[234:235], v222 offset:1800\n"
    "v_mfma_f32_16x16x32_bf16 a[132:135], a[208:211], v[24:27], a[132:135]\n"
    "ds_read_b64_tr_b16 a[236:237], v222 offset:12552\n"
    "v_mfma_f32_16x16x32_bf16 a[132:135], a[212:215], v[28:31], a[132:135]\n"
    "ds_read_b64_tr_b16 a[238:239], v222 offset:14088\n"
    "v_mfma_f32_16x16x32_bf16 a[136:139], a[208:211], v[32:35], a[136:139]\n"
    "buffer_load_dwordx4 v[72:75], v226, s[36:39], s72 offen offset:0\n"
    "v_mfma_f32_16x16x32_bf16 a[136:139], a[212:215], v[36:39], a[136:139]\n"
    "buffer_load_dwordx4 v[76:79], v226, s[36:39], s72 offen offset:1024\n"
    "v_mfma_f32_16x16x32_bf16 a[140:143], a[208:211], v[40:43], a[140:143]\n"
    "v_mfma_f32_16x16x32_bf16 a[140:143], a[212:215], v[44:47], a[140:143]\n"
    "s_waitcnt lgkmcnt(8)\n"
    "v_mfma_f32_16x16x32_bf16 a[144:147], a[216:219], v[16:19], a[144:147]\n"
    "ds_read_b64_tr_b16 a[192:193], v2 offset:0\n"
    "s_add_u32 s69, s69, 2048\n"
    "v_mfma_f32_16x16x32_bf16 a[144:147], a[220:223], v[20:23], a[144:147]\n"
    "ds_read_b64_tr_b16 a[194:195], v2 offset:512\n"
    "s_add_u32 s70, s70, 2048\n"
    "v_mfma_f32_16x16x32_bf16 a[148:151], a[216:219], v[24:27], a[148:151]\n"
    "ds_read_b64_tr_b16 a[196:197], v2 offset:4096\n"
    "s_add_u32 s71, s71, 2048\n"
    "v_mfma_f32_16x16x32_bf16 a[148:151], a[220:223], v[28:31], a[148:151]\n"
    "ds_read_b64_tr_b16 a[198:199], v2 offset:4608\n"
    "s_add_u32 s72, s72, 2048\n"
    "v_mfma_f32_16x16x32_bf16 a[152:155], a[216:219], v[32:35], a[152:155]\n"
    "s_add_u32 m0, s65, 49152\n"
    "s_nop 0\n"
    "buffer_load_dwordx4 v218, s[52:55], s65 offen offset:0 lds\n"
    "v_mfma_f32_16x16x32_bf16 a[152:155], a[220:223], v[36:39], a[152:155]\n"
    "buffer_load_dwordx4 v219, s[52:55], s65 offen offset:1024 lds\n"
    "v_mfma_f32_16x16x32_bf16 a[156:159], a[216:219], v[40:43], a[156:159]\n"
    "v_mfma_f32_16x16x32_bf16 a[156:159], a[220:223], v[44:47], a[156:159]\n"
    "s_waitcnt lgkmcnt(8)\n"
    "v_mfma_f32_16x16x32_bf16 a[160:163], a[224:227], v[16:19], a[160:163]\n"
    "ds_read_b64_tr_b16 a[200:201], v2 offset:8192\n"
    "v_mfma_f32_16x16x32_bf16 a[160:163], a[228:231], v[20:23], a[160:163]\n"
    "ds_read_b64_tr_b16 a[202:203], v2 offset:8704\n"
    "v_mfma_f32_16x16x32_bf16 a[164:167], a[224:227], v[24:27], a[164:167]\n"
    "ds_read_b64_tr_b16 a[204:205], v2 offset:12288\n"
    "v_mfma_f32_16x16x32_bf16 a[164:167], a[228:231], v[28:31], a[164:167]\n"
    "ds_read_b64_tr_b16 a[206:207], v2 offset:12800\n"
    "v_mfma_f32_16x16x32_bf16 a[168:171], a[224:227], v[32:35], a[168:171]\n"
    "buffer_load_dwordx4 v220, s[52:55], s65 offen offset:2048 lds\n"
    "v_mfma_f32_16x16x32_bf16 a[168:171], a[228:231], v[36:39], a[168:171]\n"
    "buffer_load_dwordx4 v218, s[52:55], s65 offen offset:3072 lds\n"
    "v_mfma_f32_16x16x32_bf16 a[172:175], a[224:227], v[40:43], a[172:175]\n"
    "v_mfma_f32_16x16x32_bf16 a[172:175], a[228:231], v[44:47], a[172:175]\n"
    "s_waitcnt lgkmcnt(8)\n"
    "v_mfma_f32_16x16x32_bf16 a[176:179], a[232:235], v[16:19], a[176:179]\n"
    "ds_read_b64_tr_b16 a[208:209], v3 offset:0\n"
    "s_add_u32 m0, s65, 53248\n"
    "v_mfma_f32_16x16x32_bf16 a[176:179], a[236:239], v[20:23], a[176:179]\n"
    "ds_read_b64_tr_b16 a[210:211], v3 offset:512\n"
    "v_mfma_f32_16x16x32_bf16 a[180:183], a[232:235], v[24:27], a[180:183]\n"
    "ds_read_b64_tr_b16 a[212:213], v3 offset:4096\n"
    "v_mfma_f32_16x16x32_bf16 a[180:183], a[236:239], v[28:31], a[180:183]\n"
    "ds_read_b64_tr_b16 a[214:215], v3 offset:4608\n"
    "v_mfma_f32_16x16x32_bf16 a[184:187], a[232:235], v[32:35], a[184:187]\n"
    "buffer_load_dwordx4 v219, s[56:59], s65 offen offset:0 lds\n"
    "v_mfma_f32_16x16x32_bf16 a[184:187], a[236:239], v[36:39], a[184:187]\n"
    "buffer_load_dwordx4 v220, s[56:59], s65 offen offset:1024 lds\n"
    "v_mfma_f32_16x16x32_bf16 a[188:191], a[232:235], v[40:43], a[188:191]\n"
    "v_mfma_f32_16x16x32_bf16 a[188:191], a[236:239], v[44:47], a[188:191]\n"
    "s_waitcnt lgkmcnt(8)\n"
    "v_mfma_f32_16x16x32_bf16 v[112:115], a[192:195], v[16:19], v[112:115]\n"
    "ds_read_b64_tr_b16 a[216:217], v3 offset:8192\n"
    "v_mfma_f32_16x16x32_bf16 v[112:115], a[196:199], v[20:23], v[112:115]\n"
    "ds_read_b64_tr_b16 a[218:219], v3 offset:8704\n"
    "v_mfma_f32_16x16x32_bf16 v[116:119], a[192:195], v[24:27], v[116:119]\n"
    "ds_read_b64_tr_b16 a[220:221], v3 offset:12288\n"
    "v_mfma_f32_16x16x32_bf16 v[116:119], a[196:199], v[28:31], v[116:119]\n"
    "ds_read_b64_tr_b16 a[222:223], v3 offset:12800\n"
    "v_mfma_f32_16x16x32_bf16 v[120:123], a[192:195], v[32:35], v[120:123]\n"
    "v_mfma_f32_16x16x32_bf16 v[120:123], a[196:199], v[36:39], v[120:123]\n"
    "v_mfma_f32_16x16x32_bf16 v[124:127], a[192:195], v[40:43], v[124:127]\n"
    "v_mfma_f32_16x16x32_bf16 v[124:127], a[196:199], v[44:47], v[124:127]\n"
    "s_waitcnt lgkmcnt(8)\n"
    "v_mfma_f32_16x16x32_bf16 v[128:131], a[200:203], v[16:19], v[128:131]\n"
    "ds_read_b64_tr_b16 a[224:225], v4 offset:0\n"
    "v_mfma_f32_16x16x32_bf16 v[128:131], a[204:207], v[20:23], v[128:131]\n"
    "ds_read_b64_tr_b16 a[226:227], v4 offset:512\n"
    "v_mfma_f32_16x16x32_bf16 v[132:135], a[200:203], v[24:27], v[132:135]\n"
    "ds_read_b64_tr_b16 a[228:229], v4 offset:4096\n"
    "v_mfma_f32_16x16x32_bf16 v[132:135], a[204:207], v[28:31], v[132:135]\n"
    "ds_read_b64_tr_b16 a[230:231], v4 offset:4608\n"
    "v_mfma_f32_16x16x32_bf16 v[136:139], a[200:203], v[32:35], v[136:139]\n"
    "v_mfma_f32_16x16x32_bf16 v[136:139], a[204:207], v[36:39], v[136:139]\n"
    "v_mfma_f32_16x16x32_bf16 v[140:143], a[200:203], v[40:43], v[140:143]\n"
    "v_mfma_f32_16x16x32_bf16 v[140:143], a[204:207], v[44:47], v[140:143]\n"
    "s_waitcnt lgkmcnt(8)\n"
    "v_mfma_f32_16x16x32_bf16 v[144:147], a[208:211], v[16:19], v[144:147]\n"
    "ds_read_b64_tr_b16 a[232:233], v4 offset:8192\n"
    "v_mfma_f32_16x16x32_bf16 v[144:147], a[212:215], v[20:23], v[144:147]\n"
    "ds_read_b64_tr_b16 a[234:235], v4 offset:8704\n"
    "v_mfma_f32_16x16x32_bf16 v[148:151], a[208:211], v[24:27], v[148:151]\n"
    "ds_read_b64_tr_b16 a[236:237], v4 offset:12288\n"
    "v_mfma_f32_16x16x32_bf16 v[148:151], a[212:215], v[28:31], v[148:151]\n"
    "ds_read_b64_tr_b16 a[238:239], v4 offset:12800\n"
    "v_mfma_f32_16x16x32_bf16 v[152:155], a[208:211], v[32:35], v[152:155]\n"
    "v_mfma_f32_16x16x32_bf16 v[152:155], a[212:215], v[36:39], v[152:155]\n"
    "v_mfma_f32_16x16x32_bf16 v[156:159], a[208:211], v[40:43], v[156:159]\n"
    "v_mfma_f32_16x16x32_bf16 v[156:159], a[212:215], v[44:47], v[156:159]\n"
    "s_waitcnt lgkmcnt(8)\n"
    "v_mfma_f32_16x16x32_bf16 v[160:163], a[216:219], v[16:19], v[160:163]\n"
    "v_mfma_f32_16x16x32_bf16 v[160:163], a[220:223], v[20:23], v[160:163]\n"
    "v_mfma_f32_16x16x32_bf16 v[164:167], a[216:219], v[24:27], v[164:167]\n"
    "v_mfma_f32_16x16x32_bf16 v[164:167], a[220:223], v[28:31], v[164:167]\n"
    "v_mfma_f32_16x16x32_bf16 v[168:171], a[216:219], v[32:35], v[168:171]\n"
    "v_mfma_f32_16x16x32_bf16 v[168:171], a[220:223], v[36:39], v[168:171]\n"
    "v_mfma_f32_16x16x32_bf16 v[172:175], a[216:219], v[40:43], v[172:175]\n"
    "v_mfma_f32_16x16x32_bf16 v[172:175], a[220:223], v[44:47], v[172:175]\n"
    "s_waitcnt vmcnt(14)\n"
    "s_waitcnt lgkmcnt(0)\n"
    "s_barrier\n"
    "v_mfma_f32_16x16x32_bf16 v[176:179], a[224:227], v[16:19], v[176:179]\n"
    "ds_read_b64_tr_b16 a[192:193], v2 offset:40960\n"
    "v_mfma_f32_16x16x32_bf16 v[176:179], a[228:231], v[20:23], v[176:179]\n"
    "ds_read_b64_tr_b16 a[194:195], v2 offset:41472\n"
    "v_mfma_f32_16x16x32_bf16 v[180:183], a[224:227], v[24:27], v[180:183]\n"
    "ds_read_b64_tr_b16 a[196:197], v2 offset:45056\n"
    "v_mfma_f32_16x16x32_bf16 v[180:183], a[228:231], v[28:31], v[180:183]\n"
    "ds_read_b64_tr_b16 a[198:199], v2 offset:45568\n"
    "v_mfma_f32_16x16x32_bf16 v[184:187], a[224:227], v[32:35], v[184:187]\n"
    "ds_read_b64_tr_b16 a[200:201], v2 offset:49152\n"
    "v_mfma_f32_16x16x32_bf16 v[184:187], a[228:231], v[36:39], v[184:187]\n"
    "ds_read_b64_tr_b16 a[202:203], v2 offset:49664\n"
    "v_mfma_f32_16x16x32_bf16 v[188:191], a[224:227], v[40:43], v[188:191]\n"
    "ds_read_b64_tr_b16 a[204:205], v2 offset:53248\n"
    "v_mfma_f32_16x16x32_bf16 v[188:191], a[228:231], v[44:47], v[188:191]\n"
    "ds_read_b64_tr_b16 a[206:207], v2 offset:53760\n"
    "v_mfma_f32_16x16x32_bf16 v[192:195], a[232:235], v[16:19], v[192:195]\n"
    "v_mfma_f32_16x16x32_bf16 v[192:195], a[236:239], v[20:23], v[192:195]\n"
    "v_mfma_f32_16x16x32_bf16 v[196:199], a[232:235], v[24:27], v[196:199]\n"
    "v_mfma_f32_16x16x32_bf16 v[196:199], a[236:239], v[28:31], v[196:199]\n"
    "v_mfma_f32_16x16x32_bf16 v[200:203], a[232:235], v[32:35], v[200:203]\n"
    "v_mfma_f32_16x16x32_bf16 v[200:203], a[236:239], v[36:39], v[200:203]\n"
    "v_mfma_f32_16x16x32_bf16 v[204:207], a[232:235], v[40:43], v[204:207]\n"
    "v_mfma_f32_16x16x32_bf16 v[204:207], a[236:239], v[44:47], v[204:207]\n"
    "; Q chunk 9\n"
    "s_waitcnt lgkmcnt(4)\n"
    "ds_read_b64_tr_b16 a[208:209], v3 offset:40960\n"
    "s_waitcnt vmcnt(6)\n"
    "v_mfma_f32_16x16x32_bf16 v[112:115], a[192:195], v[48:51], v[112:115]\n"
    "ds_read_b64_tr_b16 a[210:211], v3 offset:41472\n"
    "s_add_u32 m0, s65, 24576\n"
    "v_mfma_f32_16x16x32_bf16 v[112:115], a[196:199], v[52:55], v[112:115]\n"
    "ds_read_b64_tr_b16 a[212:213], v3 offset:45056\n"
    "buffer_load_dwordx4 v218, s[44:47], s80 offen offset:0 lds\n"
    "v_mfma_f32_16x16x32_bf16 v[116:119], a[192:195], v[56:59], v[116:119]\n"
    "ds_read_b64_tr_b16 a[214:215], v3 offset:45568\n"
    "s_mov_b32 s90, 0xc15db3d7\n"
    "v_mfma_f32_16x16x32_bf16 v[116:119], a[196:199], v[60:63], v[116:119]\n"
    "ds_read_b64_tr_b16 a[216:217], v3 offset:49152\n"
    "s_mov_b32 s91, 0x3c319c33\n"
    "v_mfma_f32_16x16x32_bf16 v[120:123], a[192:195], v[64:67], v[120:123]\n"
    "ds_read_b64_tr_b16 a[218:219], v3 offset:49664\n"
    "s_lshl_b32 s85, s99, 4\n"
    "v_mfma_f32_16x16x32_bf16 v[120:123], a[196:199], v[68:71], v[120:123]\n"
    "ds_read_b64_tr_b16 a[220:221], v3 offset:53248\n"
    "s_lshl_b32 s86, s64, 2\n"
    "v_mfma_f32_16x16x32_bf16 v[124:127], a[192:195], v[72:75], v[124:127]\n"
    "ds_read_b64_tr_b16 a[222:223], v3 offset:53760\n"
    "s_add_u32 s85, s85, s86\n"
    "v_mfma_f32_16x16x32_bf16 v[124:127], a[196:199], v[76:79], v[124:127]\n"
    "s_waitcnt lgkmcnt(8)\n"
    "s_mul_i32 s85, s85, 49152\n"
    "v_mfma_f32_16x16x32_bf16 v[128:131], a[200:203], v[48:51], v[128:131]\n"
    "ds_read_b64_tr_b16 a[224:225], v4 offset:40960\n"
    "s_mul_i32 s86, s79, 3\n"
    "s_add_u32 s83, s86, 0\n"
    "s_and_b32 s84, s83, 1\n"
    "s_lshl_b32 s84, s84, 3\n"
    "s_lshr_b32 s83, s83, 1\n"
    "s_lshl_b32 s83, s83, 10\n"
    "s_add_u32 s83, s83, s84\n"
    "s_add_u32 s87, s83, s85\n"
    "s_add_u32 s83, s86, 1\n"
    "s_and_b32 s84, s83, 1\n"
    "s_lshl_b32 s84, s84, 3\n"
    "s_lshr_b32 s83, s83, 1\n"
    "s_lshl_b32 s83, s83, 10\n"
    "s_add_u32 s83, s83, s84\n"
    "s_add_u32 s88, s83, s85\n"
    "s_add_u32 s83, s86, 2\n"
    "s_and_b32 s84, s83, 1\n"
    "s_lshl_b32 s84, s84, 3\n"
    "s_lshr_b32 s83, s83, 1\n"
    "s_lshl_b32 s83, s83, 10\n"
    "s_add_u32 s83, s83, s84\n"
    "s_add_u32 s89, s83, s85\n"
    "v_mfma_f32_16x16x32_bf16 v[128:131], a[204:207], v[52:55], v[128:131]\n"
    "ds_read_b64_tr_b16 a[226:227], v4 offset:41472\n"
    "v_mfma_f32_16x16x32_bf16 v[132:135], a[200:203], v[56:59], v[132:135]\n"
    "ds_read_b64_tr_b16 a[228:229], v4 offset:45056\n"
    "v_mfma_f32_16x16x32_bf16 v[132:135], a[204:207], v[60:63], v[132:135]\n"
    "ds_read_b64_tr_b16 a[230:231], v4 offset:45568\n"
    "v_mfma_f32_16x16x32_bf16 v[136:139], a[200:203], v[64:67], v[136:139]\n"
    "v_mfma_f32_16x16x32_bf16 v[136:139], a[204:207], v[68:71], v[136:139]\n"
    "v_mfma_f32_16x16x32_bf16 v[140:143], a[200:203], v[72:75], v[140:143]\n"
    "v_mul_f32 v228, s90, v112\n"
    "v_mul_f32 v229, s90, v113\n"
    "v_mfma_f32_16x16x32_bf16 v[140:143], a[204:207], v[76:79], v[140:143]\n"
    "s_waitcnt lgkmcnt(8)\n"
    "v_mul_f32 v230, s90, v114\n"
    "v_mfma_f32_16x16x32_bf16 v[144:147], a[208:211], v[48:51], v[144:147]\n"
    "ds_read_b64_tr_b16 a[232:233], v4 offset:49152\n"
    "v_mul_f32 v231, s90, v115\n"
    "v_mfma_f32_16x16x32_bf16 v[144:147], a[212:215], v[52:55], v[144:147]\n"
    "ds_read_b64_tr_b16 a[234:235], v4 offset:49664\n"
    "v_mfma_f32_16x16x32_bf16 v[148:151], a[208:211], v[56:59], v[148:151]\n"
    "ds_read_b64_tr_b16 a[236:237], v4 offset:53248\n"
    "v_mfma_f32_16x16x32_bf16 v[148:151], a[212:215], v[60:63], v[148:151]\n"
    "ds_read_b64_tr_b16 a[238:239], v4 offset:53760\n"
    "v_mfma_f32_16x16x32_bf16 v[152:155], a[208:211], v[64:67], v[152:155]\n"
    "buffer_load_dwordx4 v219, s[44:47], s80 offen offset:1024 lds\n"
    "v_mfma_f32_16x16x32_bf16 v[152:155], a[212:215], v[68:71], v[152:155]\n"
    "v_exp_f32 v228, v228\n"
    "v_mfma_f32_16x16x32_bf16 v[156:159], a[208:211], v[72:75], v[156:159]\n"
    "v_exp_f32 v229, v229\n"
    "v_mfma_f32_16x16x32_bf16 v[156:159], a[212:215], v[76:79], v[156:159]\n"
    "s_waitcnt lgkmcnt(8)\n"
    "v_mfma_f32_16x16x32_bf16 v[160:163], a[216:219], v[48:51], v[160:163]\n"
    "ds_read_b64_tr_b16 a[192:193], v221 offset:40960\n"
    "v_mfma_f32_16x16x32_bf16 v[160:163], a[220:223], v[52:55], v[160:163]\n"
    "ds_read_b64_tr_b16 a[194:195], v221 offset:42496\n"
    "v_mfma_f32_16x16x32_bf16 v[164:167], a[216:219], v[56:59], v[164:167]\n"
    "ds_read_b64_tr_b16 a[196:197], v221 offset:53248\n"
    "v_mfma_f32_16x16x32_bf16 v[164:167], a[220:223], v[60:63], v[164:167]\n"
    "ds_read_b64_tr_b16 a[198:199], v221 offset:54784\n"
    "v_mfma_f32_16x16x32_bf16 v[168:171], a[216:219], v[64:67], v[168:171]\n"
    "v_exp_f32 v230, v230\n"
    "v_mfma_f32_16x16x32_bf16 v[168:171], a[220:223], v[68:71], v[168:171]\n"
    "v_exp_f32 v231, v231\n"
    "v_mfma_f32_16x16x32_bf16 v[172:175], a[216:219], v[72:75], v[172:175]\n"
    "v_mul_f32 v128, v128, v112\n"
    "v_mul_f32 v129, v129, v113\n"
    "v_mfma_f32_16x16x32_bf16 v[172:175], a[220:223], v[76:79], v[172:175]\n"
    "s_waitcnt lgkmcnt(8)\n"
    "v_mul_f32 v130, v130, v114\n"
    "v_mfma_f32_16x16x32_bf16 v[176:179], a[224:227], v[48:51], v[176:179]\n"
    "ds_read_b64_tr_b16 a[200:201], v221 offset:40968\n"
    "v_mul_f32 v131, v131, v115\n"
    "v_mfma_f32_16x16x32_bf16 v[176:179], a[228:231], v[52:55], v[176:179]\n"
    "ds_read_b64_tr_b16 a[202:203], v221 offset:42504\n"
    "v_fma_f32 v228, v228, s91, s91\n"
    "v_mfma_f32_16x16x32_bf16 v[180:183], a[224:227], v[56:59], v[180:183]\n"
    "ds_read_b64_tr_b16 a[204:205], v221 offset:53256\n"
    "v_fma_f32 v229, v229, s91, s91\n"
    "v_mfma_f32_16x16x32_bf16 v[180:183], a[228:231], v[60:63], v[180:183]\n"
    "ds_read_b64_tr_b16 a[206:207], v221 offset:54792\n"
    "v_fma_f32 v230, v230, s91, s91\n"
    "v_mfma_f32_16x16x32_bf16 v[184:187], a[224:227], v[64:67], v[184:187]\n"
    "v_fma_f32 v231, v231, s91, s91\n"
    "v_mfma_f32_16x16x32_bf16 v[184:187], a[228:231], v[68:71], v[184:187]\n"
    "v_rcp_f32 v228, v228\n"
    "v_mfma_f32_16x16x32_bf16 v[188:191], a[224:227], v[72:75], v[188:191]\n"
    "s_waitcnt lgkmcnt(8)\n"
    "v_mfma_f32_16x16x32_bf16 v[188:191], a[228:231], v[76:79], v[188:191]\n"
    "buffer_load_dwordx4 v220, s[44:47], s80 offen offset:2048 lds\n"
    "v_mfma_f32_16x16x32_bf16 v[192:195], a[232:235], v[48:51], v[192:195]\n"
    "ds_read_b64_tr_b16 a[208:209], v222 offset:40960\n"
    "v_mfma_f32_16x16x32_bf16 v[192:195], a[236:239], v[52:55], v[192:195]\n"
    "ds_read_b64_tr_b16 a[210:211], v222 offset:42496\n"
    "v_mfma_f32_16x16x32_bf16 v[196:199], a[232:235], v[56:59], v[196:199]\n"
    "ds_read_b64_tr_b16 a[212:213], v222 offset:53248\n"
    "v_mfma_f32_16x16x32_bf16 v[196:199], a[236:239], v[60:63], v[196:199]\n"
    "ds_read_b64_tr_b16 a[214:215], v222 offset:54784\n"
    "v_mfma_f32_16x16x32_bf16 v[200:203], a[232:235], v[64:67], v[200:203]\n"
    "v_rcp_f32 v229, v229\n"
    "v_mfma_f32_16x16x32_bf16 v[200:203], a[236:239], v[68:71], v[200:203]\n"
    "v_rcp_f32 v230, v230\n"
    "v_mfma_f32_16x16x32_bf16 v[204:207], a[232:235], v[72:75], v[204:207]\n"
    "v_rcp_f32 v231, v231\n"
    "v_mfma_f32_16x16x32_bf16 v[204:207], a[236:239], v[76:79], v[204:207]\n"
    "s_waitcnt lgkmcnt(8)\n"
    "v_mul_f32 v228, v228, v128\n"
    "v_mfma_f32_16x16x32_bf16 a[0:3], a[192:195], v[48:51], a[0:3]\n"
    "ds_read_b64_tr_b16 a[216:217], v222 offset:40968\n"
    "v_mul_f32 v229, v229, v129\n"
    "v_mfma_f32_16x16x32_bf16 a[0:3], a[196:199], v[52:55], a[0:3]\n"
    "ds_read_b64_tr_b16 a[218:219], v222 offset:42504\n"
    "v_mul_f32 v230, v230, v130\n"
    "v_mfma_f32_16x16x32_bf16 a[4:7], a[192:195], v[56:59], a[4:7]\n"
    "ds_read_b64_tr_b16 a[220:221], v222 offset:53256\n"
    "v_mul_f32 v231, v231, v131\n"
    "v_mfma_f32_16x16x32_bf16 a[4:7], a[196:199], v[60:63], a[4:7]\n"
    "ds_read_b64_tr_b16 a[222:223], v222 offset:54792\n"
    "v_cvt_pk_bf16_f32 v232, v228, v229\n"
    "v_mfma_f32_16x16x32_bf16 a[8:11], a[192:195], v[64:67], a[8:11]\n"
    "v_cvt_pk_bf16_f32 v233, v230, v231\n"
    "v_mfma_f32_16x16x32_bf16 a[8:11], a[196:199], v[68:71], a[8:11]\n"
    "buffer_store_dwordx2 v[232:233], v226, s[24:27], s87 offen offset:0 sc1\n"
    "v_mfma_f32_16x16x32_bf16 a[12:15], a[192:195], v[72:75], a[12:15]\n"
    "s_add_u32 s87, s87, 49152\n"
    "v_mul_f32 v234, s90, v116\n"
    "v_mfma_f32_16x16x32_bf16 a[12:15], a[196:199], v[76:79], a[12:15]\n"
    "s_waitcnt lgkmcnt(8)\n"
    "v_mul_f32 v235, s90, v117\n"
    "v_mfma_f32_16x16x32_bf16 a[16:19], a[200:203], v[48:51], a[16:19]\n"
    "ds_read_b64_tr_b16 a[224:225], v221 offset:41088\n"
    "v_mul_f32 v236, s90, v118\n"
    "v_mfma_f32_16x16x32_bf16 a[16:19], a[204:207], v[52:55], a[16:19]\n"
    "ds_read_b64_tr_b16 a[226:227], v221 offset:42624\n"
    "v_mul_f32 v237, s90, v119\n"
    "v_mfma_f32_16x16x32_bf16 a[20:23], a[200:203], v[56:59], a[20:23]\n"
    "ds_read_b64_tr_b16 a[228:229], v221 offset:53376\n"
    "v_mfma_f32_16x16x32_bf16 a[20:23], a[204:207], v[60:63], a[20:23]\n"
    "ds_read_b64_tr_b16 a[230:231], v221 offset:54912\n"
    "v_mfma_f32_16x16x32_bf16 a[24:27], a[200:203], v[64:67], a[24:27]\n"
    "buffer_load_dwordx4 v218, s[44:47], s80 offen offset:3072 lds\n"
    "v_mfma_f32_16x16x32_bf16 a[24:27], a[204:207], v[68:71], a[24:27]\n"
    "v_exp_f32 v234, v234\n"
    "v_mfma_f32_16x16x32_bf16 a[28:31], a[200:203], v[72:75], a[28:31]\n"
    "v_exp_f32 v235, v235\n"
    "v_mfma_f32_16x16x32_bf16 a[28:31], a[204:207], v[76:79], a[28:31]\n"
    "s_waitcnt lgkmcnt(8)\n"
    "v_mfma_f32_16x16x32_bf16 a[32:35], a[208:211], v[48:51], a[32:35]\n"
    "ds_read_b64_tr_b16 a[232:233], v221 offset:41096\n"
    "v_mfma_f32_16x16x32_bf16 a[32:35], a[212:215], v[52:55], a[32:35]\n"
    "ds_read_b64_tr_b16 a[234:235], v221 offset:42632\n"
    "v_mfma_f32_16x16x32_bf16 a[36:39], a[208:211], v[56:59], a[36:39]\n"
    "ds_read_b64_tr_b16 a[236:237], v221 offset:53384\n"
    "v_mfma_f32_16x16x32_bf16 a[36:39], a[212:215], v[60:63], a[36:39]\n"
    "ds_read_b64_tr_b16 a[238:239], v221 offset:54920\n"
    "v_mfma_f32_16x16x32_bf16 a[40:43], a[208:211], v[64:67], a[40:43]\n"
    "v_exp_f32 v236, v236\n"
    "v_mfma_f32_16x16x32_bf16 a[40:43], a[212:215], v[68:71], a[40:43]\n"
    "v_exp_f32 v237, v237\n"
    "v_mfma_f32_16x16x32_bf16 a[44:47], a[208:211], v[72:75], a[44:47]\n"
    "v_mul_f32 v132, v132, v116\n"
    "v_mul_f32 v133, v133, v117\n"
    "v_mfma_f32_16x16x32_bf16 a[44:47], a[212:215], v[76:79], a[44:47]\n"
    "s_waitcnt lgkmcnt(8)\n"
    "v_mul_f32 v134, v134, v118\n"
    "v_mfma_f32_16x16x32_bf16 a[48:51], a[216:219], v[48:51], a[48:51]\n"
    "ds_read_b64_tr_b16 a[192:193], v222 offset:41088\n"
    "v_mul_f32 v135, v135, v119\n"
    "v_mfma_f32_16x16x32_bf16 a[48:51], a[220:223], v[52:55], a[48:51]\n"
    "ds_read_b64_tr_b16 a[194:195], v222 offset:42624\n"
    "v_fma_f32 v234, v234, s91, s91\n"
    "v_mfma_f32_16x16x32_bf16 a[52:55], a[216:219], v[56:59], a[52:55]\n"
    "ds_read_b64_tr_b16 a[196:197], v222 offset:53376\n"
    "v_fma_f32 v235, v235, s91, s91\n"
    "v_mfma_f32_16x16x32_bf16 a[52:55], a[220:223], v[60:63], a[52:55]\n"
    "ds_read_b64_tr_b16 a[198:199], v222 offset:54912\n"
    "v_fma_f32 v236, v236, s91, s91\n"
    "v_mfma_f32_16x16x32_bf16 a[56:59], a[216:219], v[64:67], a[56:59]\n"
    "v_fma_f32 v237, v237, s91, s91\n"
    "v_mfma_f32_16x16x32_bf16 a[56:59], a[220:223], v[68:71], a[56:59]\n"
    "v_rcp_f32 v234, v234\n"
    "v_mfma_f32_16x16x32_bf16 a[60:63], a[216:219], v[72:75], a[60:63]\n"
    "s_add_u32 m0, s65, 28672\n"
    "s_nop 0\n"
    "buffer_load_dwordx4 v219, s[48:51], s80 offen offset:0 lds\n"
    "v_mfma_f32_16x16x32_bf16 a[60:63], a[220:223], v[76:79], a[60:63]\n"
    "s_waitcnt lgkmcnt(8)\n"
    "v_mfma_f32_16x16x32_bf16 a[64:67], a[224:227], v[48:51], a[64:67]\n"
    "ds_read_b64_tr_b16 a[200:201], v222 offset:41096\n"
    "v_mfma_f32_16x16x32_bf16 a[64:67], a[228:231], v[52:55], a[64:67]\n"
    "ds_read_b64_tr_b16 a[202:203], v222 offset:42632\n"
    "v_mfma_f32_16x16x32_bf16 a[68:71], a[224:227], v[56:59], a[68:71]\n"
    "ds_read_b64_tr_b16 a[204:205], v222 offset:53384\n"
    "v_mfma_f32_16x16x32_bf16 a[68:71], a[228:231], v[60:63], a[68:71]\n"
    "ds_read_b64_tr_b16 a[206:207], v222 offset:54920\n"
    "v_mfma_f32_16x16x32_bf16 a[72:75], a[224:227], v[64:67], a[72:75]\n"
    "v_rcp_f32 v235, v235\n"
    "v_mfma_f32_16x16x32_bf16 a[72:75], a[228:231], v[68:71], a[72:75]\n"
    "v_rcp_f32 v236, v236\n"
    "v_mfma_f32_16x16x32_bf16 a[76:79], a[224:227], v[72:75], a[76:79]\n"
    "v_rcp_f32 v237, v237\n"
    "v_mfma_f32_16x16x32_bf16 a[76:79], a[228:231], v[76:79], a[76:79]\n"
    "s_waitcnt lgkmcnt(8)\n"
    "v_mul_f32 v234, v234, v132\n"
    "v_mfma_f32_16x16x32_bf16 a[80:83], a[232:235], v[48:51], a[80:83]\n"
    "ds_read_b64_tr_b16 a[208:209], v221 offset:41216\n"
    "v_mul_f32 v235, v235, v133\n"
    "v_mfma_f32_16x16x32_bf16 a[80:83], a[236:239], v[52:55], a[80:83]\n"
    "ds_read_b64_tr_b16 a[210:211], v221 offset:42752\n"
    "v_mul_f32 v236, v236, v134\n"
    "v_mfma_f32_16x16x32_bf16 a[84:87], a[232:235], v[56:59], a[84:87]\n"
    "ds_read_b64_tr_b16 a[212:213], v221 offset:53504\n"
    "v_mul_f32 v237, v237, v135\n"
    "v_mfma_f32_16x16x32_bf16 a[84:87], a[236:239], v[60:63], a[84:87]\n"
    "ds_read_b64_tr_b16 a[214:215], v221 offset:55040\n"
    "v_cvt_pk_bf16_f32 v238, v234, v235\n"
    "v_mfma_f32_16x16x32_bf16 a[88:91], a[232:235], v[64:67], a[88:91]\n"
    "v_cvt_pk_bf16_f32 v239, v236, v237\n"
    "v_mfma_f32_16x16x32_bf16 a[88:91], a[236:239], v[68:71], a[88:91]\n"
    "buffer_store_dwordx2 v[238:239], v226, s[24:27], s87 offen offset:0 sc1\n"
    "v_mfma_f32_16x16x32_bf16 a[92:95], a[232:235], v[72:75], a[92:95]\n"
    "s_add_u32 s87, s87, 49152\n"
    "v_mul_f32 v240, s90, v120\n"
    "v_mfma_f32_16x16x32_bf16 a[92:95], a[236:239], v[76:79], a[92:95]\n"
    "s_waitcnt lgkmcnt(8)\n"
    "v_mul_f32 v241, s90, v121\n"
    "v_mfma_f32_16x16x32_bf16 a[96:99], a[192:195], v[48:51], a[96:99]\n"
    "ds_read_b64_tr_b16 a[216:217], v221 offset:41224\n"
    "v_mul_f32 v242, s90, v122\n"
    "v_mfma_f32_16x16x32_bf16 a[96:99], a[196:199], v[52:55], a[96:99]\n"
    "ds_read_b64_tr_b16 a[218:219], v221 offset:42760\n"
    "v_mul_f32 v243, s90, v123\n"
    "v_mfma_f32_16x16x32_bf16 a[100:103], a[192:195], v[56:59], a[100:103]\n"
    "ds_read_b64_tr_b16 a[220:221], v221 offset:53512\n"
    "v_mfma_f32_16x16x32_bf16 a[100:103], a[196:199], v[60:63], a[100:103]\n"
    "ds_read_b64_tr_b16 a[222:223], v221 offset:55048\n"
    "v_mfma_f32_16x16x32_bf16 a[104:107], a[192:195], v[64:67], a[104:107]\n"
    "buffer_load_dwordx4 v220, s[48:51], s80 offen offset:1024 lds\n"
    "v_mfma_f32_16x16x32_bf16 a[104:107], a[196:199], v[68:71], a[104:107]\n"
    "v_exp_f32 v240, v240\n"
    "v_mfma_f32_16x16x32_bf16 a[108:111], a[192:195], v[72:75], a[108:111]\n"
    "v_exp_f32 v241, v241\n"
    "v_mfma_f32_16x16x32_bf16 a[108:111], a[196:199], v[76:79], a[108:111]\n"
    "s_waitcnt lgkmcnt(8)\n"
    "v_mfma_f32_16x16x32_bf16 a[112:115], a[200:203], v[48:51], a[112:115]\n"
    "ds_read_b64_tr_b16 a[224:225], v222 offset:41216\n"
    "v_mfma_f32_16x16x32_bf16 a[112:115], a[204:207], v[52:55], a[112:115]\n"
    "ds_read_b64_tr_b16 a[226:227], v222 offset:42752\n"
    "v_mfma_f32_16x16x32_bf16 a[116:119], a[200:203], v[56:59], a[116:119]\n"
    "ds_read_b64_tr_b16 a[228:229], v222 offset:53504\n"
    "v_mfma_f32_16x16x32_bf16 a[116:119], a[204:207], v[60:63], a[116:119]\n"
    "ds_read_b64_tr_b16 a[230:231], v222 offset:55040\n"
    "v_mfma_f32_16x16x32_bf16 a[120:123], a[200:203], v[64:67], a[120:123]\n"
    "v_exp_f32 v242, v242\n"
    "v_mfma_f32_16x16x32_bf16 a[120:123], a[204:207], v[68:71], a[120:123]\n"
    "v_exp_f32 v243, v243\n"
    "v_mfma_f32_16x16x32_bf16 a[124:127], a[200:203], v[72:75], a[124:127]\n"
    "v_mul_f32 v136, v136, v120\n"
    "v_mul_f32 v137, v137, v121\n"
    "v_mfma_f32_16x16x32_bf16 a[124:127], a[204:207], v[76:79], a[124:127]\n"
    "s_waitcnt lgkmcnt(8)\n"
    "v_mul_f32 v138, v138, v122\n"
    "v_mfma_f32_16x16x32_bf16 a[128:131], a[208:211], v[48:51], a[128:131]\n"
    "ds_read_b64_tr_b16 a[232:233], v222 offset:41224\n"
    "v_mul_f32 v139, v139, v123\n"
    "v_mfma_f32_16x16x32_bf16 a[128:131], a[212:215], v[52:55], a[128:131]\n"
    "ds_read_b64_tr_b16 a[234:235], v222 offset:42760\n"
    "v_fma_f32 v240, v240, s91, s91\n"
    "v_mfma_f32_16x16x32_bf16 a[132:135], a[208:211], v[56:59], a[132:135]\n"
    "ds_read_b64_tr_b16 a[236:237], v222 offset:53512\n"
    "v_fma_f32 v241, v241, s91, s91\n"
    "v_mfma_f32_16x16x32_bf16 a[132:135], a[212:215], v[60:63], a[132:135]\n"
    "ds_read_b64_tr_b16 a[238:239], v222 offset:55048\n"
    "v_fma_f32 v242, v242, s91, s91\n"
    "v_mfma_f32_16x16x32_bf16 a[136:139], a[208:211], v[64:67], a[136:139]\n"
    "v_fma_f32 v243, v243, s91, s91\n"
    "v_mfma_f32_16x16x32_bf16 a[136:139], a[212:215], v[68:71], a[136:139]\n"
    "v_rcp_f32 v240, v240\n"
    "v_mfma_f32_16x16x32_bf16 a[140:143], a[208:211], v[72:75], a[140:143]\n"
    "buffer_load_dword v15, v9, s[12:15], s3 offen offset:0\n"
    "v_mfma_f32_16x16x32_bf16 a[140:143], a[212:215], v[76:79], a[140:143]\n"
    "s_waitcnt lgkmcnt(8)\n"
    "v_mfma_f32_16x16x32_bf16 a[144:147], a[216:219], v[48:51], a[144:147]\n"
    "v_rcp_f32 v241, v241\n"
    "v_mfma_f32_16x16x32_bf16 a[144:147], a[220:223], v[52:55], a[144:147]\n"
    "v_rcp_f32 v242, v242\n"
    "v_mfma_f32_16x16x32_bf16 a[148:151], a[216:219], v[56:59], a[148:151]\n"
    "v_rcp_f32 v243, v243\n"
    "v_mfma_f32_16x16x32_bf16 a[148:151], a[220:223], v[60:63], a[148:151]\n"
    "v_mul_f32 v240, v240, v136\n"
    "v_mul_f32 v241, v241, v137\n"
    "v_mfma_f32_16x16x32_bf16 a[152:155], a[216:219], v[64:67], a[152:155]\n"
    "v_mul_f32 v242, v242, v138\n"
    "v_mul_f32 v243, v243, v139\n"
    "v_mfma_f32_16x16x32_bf16 a[152:155], a[220:223], v[68:71], a[152:155]\n"
    "v_cvt_pk_bf16_f32 v244, v240, v241\n"
    "v_cvt_pk_bf16_f32 v245, v242, v243\n"
    "v_mfma_f32_16x16x32_bf16 a[156:159], a[216:219], v[72:75], a[156:159]\n"
    "buffer_store_dwordx2 v[244:245], v226, s[24:27], s87 offen offset:0 sc1\n"
    "v_mfma_f32_16x16x32_bf16 a[156:159], a[220:223], v[76:79], a[156:159]\n"
    "s_waitcnt lgkmcnt(4)\n"
    "s_add_u32 s87, s87, 49152\n"
    "v_mfma_f32_16x16x32_bf16 a[160:163], a[224:227], v[48:51], a[160:163]\n"
    "v_mul_f32 v228, s90, v124\n"
    "v_mul_f32 v229, s90, v125\n"
    "v_mfma_f32_16x16x32_bf16 a[160:163], a[228:231], v[52:55], a[160:163]\n"
    "v_mul_f32 v230, s90, v126\n"
    "v_mul_f32 v231, s90, v127\n"
    "v_mfma_f32_16x16x32_bf16 a[164:167], a[224:227], v[56:59], a[164:167]\n"
    "v_exp_f32 v228, v228\n"
    "v_mfma_f32_16x16x32_bf16 a[164:167], a[228:231], v[60:63], a[164:167]\n"
    "v_exp_f32 v229, v229\n"
    "v_mfma_f32_16x16x32_bf16 a[168:171], a[224:227], v[64:67], a[168:171]\n"
    "v_exp_f32 v230, v230\n"
    "v_mfma_f32_16x16x32_bf16 a[168:171], a[228:231], v[68:71], a[168:171]\n"
    "v_exp_f32 v231, v231\n"
    "v_mfma_f32_16x16x32_bf16 a[172:175], a[224:227], v[72:75], a[172:175]\n"
    "v_mul_f32 v140, v140, v124\n"
    "v_mul_f32 v141, v141, v125\n"
    "v_mfma_f32_16x16x32_bf16 a[172:175], a[228:231], v[76:79], a[172:175]\n"
    "s_waitcnt lgkmcnt(0)\n"
    "v_mul_f32 v142, v142, v126\n"
    "v_mfma_f32_16x16x32_bf16 a[176:179], a[232:235], v[48:51], a[176:179]\n"
    "v_mul_f32 v143, v143, v127\n"
    "v_fma_f32 v228, v228, s91, s91\n"
    "v_mfma_f32_16x16x32_bf16 a[176:179], a[236:239], v[52:55], a[176:179]\n"
    "v_fma_f32 v229, v229, s91, s91\n"
    "v_fma_f32 v230, v230, s91, s91\n"
    "v_mfma_f32_16x16x32_bf16 a[180:183], a[232:235], v[56:59], a[180:183]\n"
    "s_add_u32 s3, s3, 49152\n"
    "v_fma_f32 v231, v231, s91, s91\n"
    "v_mfma_f32_16x16x32_bf16 a[180:183], a[236:239], v[60:63], a[180:183]\n"
    "v_rcp_f32 v228, v228\n"
    "v_mfma_f32_16x16x32_bf16 a[184:187], a[232:235], v[64:67], a[184:187]\n"
    "v_rcp_f32 v229, v229\n"
    "v_mfma_f32_16x16x32_bf16 a[184:187], a[236:239], v[68:71], a[184:187]\n"
    "v_rcp_f32 v230, v230\n"
    "v_mfma_f32_16x16x32_bf16 a[188:191], a[232:235], v[72:75], a[188:191]\n"
    "v_rcp_f32 v231, v231\n"
    "v_mfma_f32_16x16x32_bf16 a[188:191], a[236:239], v[76:79], a[188:191]\n"
    "v_mul_f32 v228, v228, v140\n"
    "v_mul_f32 v229, v229, v141\n"
    "v_mul_f32 v230, v230, v142\n"
    "v_mul_f32 v231, v231, v143\n"
    "v_cvt_pk_bf16_f32 v232, v228, v229\n"
    "v_cvt_pk_bf16_f32 v233, v230, v231\n"
    "buffer_store_dwordx2 v[232:233], v226, s[24:27], s87 offen offset:0 sc1\n"
    "v_mul_f32 v234, s90, v144\n"
    "v_mul_f32 v235, s90, v145\n"
    "v_mul_f32 v236, s90, v146\n"
    "v_mul_f32 v237, s90, v147\n"
    "v_exp_f32 v234, v234\n"
    "v_exp_f32 v235, v235\n"
    "v_exp_f32 v236, v236\n"
    "v_exp_f32 v237, v237\n"
    "v_mul_f32 v160, v160, v144\n"
    "v_mul_f32 v161, v161, v145\n"
    "v_mul_f32 v162, v162, v146\n"
    "v_mul_f32 v163, v163, v147\n"
    "v_fma_f32 v234, v234, s91, s91\n"
    "v_fma_f32 v235, v235, s91, s91\n"
    "v_fma_f32 v236, v236, s91, s91\n"
    "v_fma_f32 v237, v237, s91, s91\n"
    "v_rcp_f32 v234, v234\n"
    "v_rcp_f32 v235, v235\n"
    "v_rcp_f32 v236, v236\n"
    "v_rcp_f32 v237, v237\n"
    "v_mul_f32 v234, v234, v160\n"
    "v_mul_f32 v235, v235, v161\n"
    "v_mul_f32 v236, v236, v162\n"
    "v_mul_f32 v237, v237, v163\n"
    "v_cvt_pk_bf16_f32 v238, v234, v235\n"
    "v_cvt_pk_bf16_f32 v239, v236, v237\n"
    "buffer_store_dwordx2 v[238:239], v226, s[24:27], s88 offen offset:0 sc1\n"
    "s_add_u32 s88, s88, 49152\n"
    "v_mul_f32 v240, s90, v148\n"
    "v_mul_f32 v241, s90, v149\n"
    "v_mul_f32 v242, s90, v150\n"
    "v_mul_f32 v243, s90, v151\n"
    "v_exp_f32 v240, v240\n"
    "v_exp_f32 v241, v241\n"
    "v_exp_f32 v242, v242\n"
    "v_exp_f32 v243, v243\n"
    "v_mul_f32 v164, v164, v148\n"
    "v_mul_f32 v165, v165, v149\n"
    "v_mul_f32 v166, v166, v150\n"
    "v_mul_f32 v167, v167, v151\n"
    "v_fma_f32 v240, v240, s91, s91\n"
    "v_fma_f32 v241, v241, s91, s91\n"
    "v_fma_f32 v242, v242, s91, s91\n"
    "v_fma_f32 v243, v243, s91, s91\n"
    "v_rcp_f32 v240, v240\n"
    "v_rcp_f32 v241, v241\n"
    "v_rcp_f32 v242, v242\n"
    "v_rcp_f32 v243, v243\n"
    "v_mul_f32 v240, v240, v164\n"
    "v_mul_f32 v241, v241, v165\n"
    "v_mul_f32 v242, v242, v166\n"
    "v_mul_f32 v243, v243, v167\n"
    "v_cvt_pk_bf16_f32 v244, v240, v241\n"
    "v_cvt_pk_bf16_f32 v245, v242, v243\n"
    "buffer_store_dwordx2 v[244:245], v226, s[24:27], s88 offen offset:0 sc1\n"
    "s_add_u32 s88, s88, 49152\n"
    "v_mul_f32 v228, s90, v152\n"
    "v_mul_f32 v229, s90, v153\n"
    "v_mul_f32 v230, s90, v154\n"
    "v_mul_f32 v231, s90, v155\n"
    "v_exp_f32 v228, v228\n"
    "v_exp_f32 v229, v229\n"
    "v_exp_f32 v230, v230\n"
    "v_exp_f32 v231, v231\n"
    "v_mul_f32 v168, v168, v152\n"
    "v_mul_f32 v169, v169, v153\n"
    "v_mul_f32 v170, v170, v154\n"
    "v_mul_f32 v171, v171, v155\n"
    "v_fma_f32 v228, v228, s91, s91\n"
    "v_fma_f32 v229, v229, s91, s91\n"
    "v_fma_f32 v230, v230, s91, s91\n"
    "v_fma_f32 v231, v231, s91, s91\n"
    "v_rcp_f32 v228, v228\n"
    "v_rcp_f32 v229, v229\n"
    "v_rcp_f32 v230, v230\n"
    "v_rcp_f32 v231, v231\n"
    "v_mul_f32 v228, v228, v168\n"
    "v_mul_f32 v229, v229, v169\n"
    "v_mul_f32 v230, v230, v170\n"
    "v_mul_f32 v231, v231, v171\n"
    "v_cvt_pk_bf16_f32 v232, v228, v229\n"
    "v_cvt_pk_bf16_f32 v233, v230, v231\n"
    "buffer_store_dwordx2 v[232:233], v226, s[24:27], s88 offen offset:0 sc1\n"
    "s_add_u32 s88, s88, 49152\n"
    "v_mul_f32 v234, s90, v156\n"
    "v_mul_f32 v235, s90, v157\n"
    "v_mul_f32 v236, s90, v158\n"
    "v_mul_f32 v237, s90, v159\n"
    "v_exp_f32 v234, v234\n"
    "v_exp_f32 v235, v235\n"
    "v_exp_f32 v236, v236\n"
    "v_exp_f32 v237, v237\n"
    "v_mul_f32 v172, v172, v156\n"
    "v_mul_f32 v173, v173, v157\n"
    "v_mul_f32 v174, v174, v158\n"
    "v_mul_f32 v175, v175, v159\n"
    "v_fma_f32 v234, v234, s91, s91\n"
    "v_fma_f32 v235, v235, s91, s91\n"
    "v_fma_f32 v236, v236, s91, s91\n"
    "v_fma_f32 v237, v237, s91, s91\n"
    "v_rcp_f32 v234, v234\n"
    "v_rcp_f32 v235, v235\n"
    "v_rcp_f32 v236, v236\n"
    "v_rcp_f32 v237, v237\n"
    "v_mul_f32 v234, v234, v172\n"
    "v_mul_f32 v235, v235, v173\n"
    "v_mul_f32 v236, v236, v174\n"
    "v_mul_f32 v237, v237, v175\n"
    "v_cvt_pk_bf16_f32 v238, v234, v235\n"
    "v_cvt_pk_bf16_f32 v239, v236, v237\n"
    "buffer_store_dwordx2 v[238:239], v226, s[24:27], s88 offen offset:0 sc1\n"
    "v_mul_f32 v240, s90, v176\n"
    "v_mul_f32 v241, s90, v177\n"
    "v_mul_f32 v242, s90, v178\n"
    "v_mul_f32 v243, s90, v179\n"
    "v_exp_f32 v240, v240\n"
    "v_exp_f32 v241, v241\n"
    "v_exp_f32 v242, v242\n"
    "v_exp_f32 v243, v243\n"
    "v_mul_f32 v192, v192, v176\n"
    "v_mul_f32 v193, v193, v177\n"
    "v_mul_f32 v194, v194, v178\n"
    "v_mul_f32 v195, v195, v179\n"
    "v_fma_f32 v240, v240, s91, s91\n"
    "v_fma_f32 v241, v241, s91, s91\n"
    "v_fma_f32 v242, v242, s91, s91\n"
    "v_fma_f32 v243, v243, s91, s91\n"
    "v_rcp_f32 v240, v240\n"
    "v_rcp_f32 v241, v241\n"
    "v_rcp_f32 v242, v242\n"
    "v_rcp_f32 v243, v243\n"
    "v_mul_f32 v240, v240, v192\n"
    "v_mul_f32 v241, v241, v193\n"
    "v_mul_f32 v242, v242, v194\n"
    "v_mul_f32 v243, v243, v195\n"
    "v_cvt_pk_bf16_f32 v244, v240, v241\n"
    "v_cvt_pk_bf16_f32 v245, v242, v243\n"
    "buffer_store_dwordx2 v[244:245], v226, s[24:27], s89 offen offset:0 sc1\n"
    "s_add_u32 s89, s89, 49152\n"
    "v_mul_f32 v228, s90, v180\n"
    "v_mul_f32 v229, s90, v181\n"
    "v_mul_f32 v230, s90, v182\n"
    "v_mul_f32 v231, s90, v183\n"
    "v_exp_f32 v228, v228\n"
    "v_exp_f32 v229, v229\n"
    "v_exp_f32 v230, v230\n"
    "v_exp_f32 v231, v231\n"
    "v_mul_f32 v196, v196, v180\n"
    "v_mul_f32 v197, v197, v181\n"
    "v_mul_f32 v198, v198, v182\n"
    "v_mul_f32 v199, v199, v183\n"
    "v_fma_f32 v228, v228, s91, s91\n"
    "v_fma_f32 v229, v229, s91, s91\n"
    "v_fma_f32 v230, v230, s91, s91\n"
    "v_fma_f32 v231, v231, s91, s91\n"
    "v_rcp_f32 v228, v228\n"
    "v_rcp_f32 v229, v229\n"
    "v_rcp_f32 v230, v230\n"
    "v_rcp_f32 v231, v231\n"
    "v_mul_f32 v228, v228, v196\n"
    "v_mul_f32 v229, v229, v197\n"
    "v_mul_f32 v230, v230, v198\n"
    "v_mul_f32 v231, v231, v199\n"
    "v_cvt_pk_bf16_f32 v232, v228, v229\n"
    "v_cvt_pk_bf16_f32 v233, v230, v231\n"
    "buffer_store_dwordx2 v[232:233], v226, s[24:27], s89 offen offset:0 sc1\n"
    "s_add_u32 s89, s89, 49152\n"
    "v_mul_f32 v234, s90, v184\n"
    "v_mul_f32 v235, s90, v185\n"
    "v_mul_f32 v236, s90, v186\n"
    "v_mul_f32 v237, s90, v187\n"
    "v_exp_f32 v234, v234\n"
    "v_exp_f32 v235, v235\n"
    "v_exp_f32 v236, v236\n"
    "v_exp_f32 v237, v237\n"
    "v_mul_f32 v200, v200, v184\n"
    "v_mul_f32 v201, v201, v185\n"
    "v_mul_f32 v202, v202, v186\n"
    "v_mul_f32 v203, v203, v187\n"
    "v_fma_f32 v234, v234, s91, s91\n"
    "v_fma_f32 v235, v235, s91, s91\n"
    "v_fma_f32 v236, v236, s91, s91\n"
    "v_fma_f32 v237, v237, s91, s91\n"
    "v_rcp_f32 v234, v234\n"
    "v_rcp_f32 v235, v235\n"
    "v_rcp_f32 v236, v236\n"
    "v_rcp_f32 v237, v237\n"
    "v_mul_f32 v234, v234, v200\n"
    "v_mul_f32 v235, v235, v201\n"
    "v_mul_f32 v236, v236, v202\n"
    "v_mul_f32 v237, v237, v203\n"
    "v_cvt_pk_bf16_f32 v238, v234, v235\n"
    "v_cvt_pk_bf16_f32 v239, v236, v237\n"
    "buffer_store_dwordx2 v[238:239], v226, s[24:27], s89 offen offset:0 sc1\n"
    "s_add_u32 s89, s89, 49152\n"
    "v_mul_f32 v240, s90, v188\n"
    "v_mul_f32 v241, s90, v189\n"
    "v_mul_f32 v242, s90, v190\n"
    "v_mul_f32 v243, s90, v191\n"
    "v_exp_f32 v240, v240\n"
    "v_exp_f32 v241, v241\n"
    "v_exp_f32 v242, v242\n"
    "v_exp_f32 v243, v243\n"
    "v_mul_f32 v204, v204, v188\n"
    "v_mul_f32 v205, v205, v189\n"
    "v_mul_f32 v206, v206, v190\n"
    "v_mul_f32 v207, v207, v191\n"
    "v_fma_f32 v240, v240, s91, s91\n"
    "v_fma_f32 v241, v241, s91, s91\n"
    "v_fma_f32 v242, v242, s91, s91\n"
    "v_fma_f32 v243, v243, s91, s91\n"
    "v_rcp_f32 v240, v240\n"
    "v_rcp_f32 v241, v241\n"
    "v_rcp_f32 v242, v242\n"
    "v_rcp_f32 v243, v243\n"
    "v_mul_f32 v240, v240, v204\n"
    "v_mul_f32 v241, v241, v205\n"
    "v_mul_f32 v242, v242, v206\n"
    "v_mul_f32 v243, v243, v207\n"
    "v_cvt_pk_bf16_f32 v244, v240, v241\n"
    "v_cvt_pk_bf16_f32 v245, v242, v243\n"
    "buffer_store_dwordx2 v[244:245], v226, s[24:27], s89 offen offset:0 sc1\n"
    "; Q epilogue + shift\n"
    "s_nop 7\n"
    "s_barrier\n"
    "ds_read_b128 a[192:195], v212 offset:0\n"
    "ds_read_b128 a[196:199], v213 offset:0\n"
    "ds_read_b128 a[200:203], v212 offset:128\n"
    "ds_read_b128 a[204:207], v213 offset:128\n"
    "ds_read_b128 a[208:211], v212 offset:256\n"
    "ds_read_b128 a[212:215], v213 offset:256\n"
    "s_nop 3\n"
    "v_accvgpr_read_b32 v228, a0\n"
    "v_accvgpr_read_b32 v229, a1\n"
    "v_accvgpr_read_b32 v230, a2\n"
    "v_accvgpr_read_b32 v231, a3\n"
    "v_accvgpr_read_b32 v232, a16\n"
    "v_accvgpr_read_b32 v233, a17\n"
    "v_accvgpr_read_b32 v234, a18\n"
    "v_accvgpr_read_b32 v235, a19\n"
    "v_cvt_pk_bf16_f32 v16, v228, v229\n"
    "v_cvt_pk_bf16_f32 v17, v230, v231\n"
    "v_cvt_pk_bf16_f32 v18, v232, v233\n"
    "v_cvt_pk_bf16_f32 v19, v234, v235\n"
    "v_accvgpr_read_b32 v236, a32\n"
    "v_accvgpr_read_b32 v237, a33\n"
    "v_accvgpr_read_b32 v238, a34\n"
    "v_accvgpr_read_b32 v239, a35\n"
    "v_accvgpr_read_b32 v240, a48\n"
    "v_accvgpr_read_b32 v241, a49\n"
    "v_accvgpr_read_b32 v242, a50\n"
    "v_accvgpr_read_b32 v243, a51\n"
    "v_cvt_pk_bf16_f32 v20, v236, v237\n"
    "v_cvt_pk_bf16_f32 v21, v238, v239\n"
    "v_cvt_pk_bf16_f32 v22, v240, v241\n"
    "v_cvt_pk_bf16_f32 v23, v242, v243\n"
    "v_accvgpr_read_b32 v228, a64\n"
    "v_accvgpr_read_b32 v229, a65\n"
    "v_accvgpr_read_b32 v230, a66\n"
    "v_accvgpr_read_b32 v231, a67\n"
    "v_accvgpr_read_b32 v232, a80\n"
    "v_accvgpr_read_b32 v233, a81\n"
    "v_accvgpr_read_b32 v234, a82\n"
    "v_accvgpr_read_b32 v235, a83\n"
    "v_cvt_pk_bf16_f32 v24, v228, v229\n"
    "v_cvt_pk_bf16_f32 v25, v230, v231\n"
    "v_cvt_pk_bf16_f32 v26, v232, v233\n"
    "v_cvt_pk_bf16_f32 v27, v234, v235\n"
    "v_accvgpr_read_b32 v236, a96\n"
    "v_accvgpr_read_b32 v237, a97\n"
    "v_accvgpr_read_b32 v238, a98\n"
    "v_accvgpr_read_b32 v239, a99\n"
    "v_accvgpr_read_b32 v240, a112\n"
    "v_accvgpr_read_b32 v241, a113\n"
    "v_accvgpr_read_b32 v242, a114\n"
    "v_accvgpr_read_b32 v243, a115\n"
    "v_cvt_pk_bf16_f32 v28, v236, v237\n"
    "v_cvt_pk_bf16_f32 v29, v238, v239\n"
    "v_cvt_pk_bf16_f32 v30, v240, v241\n"
    "v_cvt_pk_bf16_f32 v31, v242, v243\n"
    "v_accvgpr_read_b32 v228, a128\n"
    "v_accvgpr_read_b32 v229, a129\n"
    "v_accvgpr_read_b32 v230, a130\n"
    "v_accvgpr_read_b32 v231, a131\n"
    "v_accvgpr_read_b32 v232, a144\n"
    "v_accvgpr_read_b32 v233, a145\n"
    "v_accvgpr_read_b32 v234, a146\n"
    "v_accvgpr_read_b32 v235, a147\n"
    "v_cvt_pk_bf16_f32 v32, v228, v229\n"
    "v_cvt_pk_bf16_f32 v33, v230, v231\n"
    "v_cvt_pk_bf16_f32 v34, v232, v233\n"
    "v_cvt_pk_bf16_f32 v35, v234, v235\n"
    "v_accvgpr_read_b32 v236, a160\n"
    "v_accvgpr_read_b32 v237, a161\n"
    "v_accvgpr_read_b32 v238, a162\n"
    "v_accvgpr_read_b32 v239, a163\n"
    "v_accvgpr_read_b32 v240, a176\n"
    "v_accvgpr_read_b32 v241, a177\n"
    "v_accvgpr_read_b32 v242, a178\n"
    "v_accvgpr_read_b32 v243, a179\n"
    "v_cvt_pk_bf16_f32 v36, v236, v237\n"
    "v_cvt_pk_bf16_f32 v37, v238, v239\n"
    "v_cvt_pk_bf16_f32 v38, v240, v241\n"
    "v_cvt_pk_bf16_f32 v39, v242, v243\n"
    "s_waitcnt lgkmcnt(0)\n"
    "v_mfma_f32_16x16x32_bf16 v[176:179], a[192:195], v[16:19], 0\n"
    "v_accvgpr_read_b32 v228, a4\n"
    "v_accvgpr_read_b32 v229, a5\n"
    "v_accvgpr_read_b32 v230, a6\n"
    "v_accvgpr_read_b32 v231, a7\n"
    "v_accvgpr_read_b32 v232, a20\n"
    "v_accvgpr_read_b32 v233, a21\n"
    "v_accvgpr_read_b32 v234, a22\n"
    "v_accvgpr_read_b32 v235, a23\n"
    "v_cvt_pk_bf16_f32 v40, v228, v229\n"
    "v_cvt_pk_bf16_f32 v41, v230, v231\n"
    "v_cvt_pk_bf16_f32 v42, v232, v233\n"
    "v_cvt_pk_bf16_f32 v43, v234, v235\n"
    "v_mfma_f32_16x16x32_bf16 v[176:179], a[196:199], v[20:23], v[176:179]\n"
    "v_accvgpr_read_b32 v236, a36\n"
    "v_accvgpr_read_b32 v237, a37\n"
    "v_accvgpr_read_b32 v238, a38\n"
    "v_accvgpr_read_b32 v239, a39\n"
    "v_accvgpr_read_b32 v240, a52\n"
    "v_accvgpr_read_b32 v241, a53\n"
    "v_accvgpr_read_b32 v242, a54\n"
    "v_accvgpr_read_b32 v243, a55\n"
    "v_cvt_pk_bf16_f32 v44, v236, v237\n"
    "v_cvt_pk_bf16_f32 v45, v238, v239\n"
    "v_cvt_pk_bf16_f32 v46, v240, v241\n"
    "v_cvt_pk_bf16_f32 v47, v242, v243\n"
    "v_mfma_f32_16x16x32_bf16 v[176:179], a[200:203], v[24:27], v[176:179]\n"
    "v_accvgpr_read_b32 v228, a68\n"
    "v_accvgpr_read_b32 v229, a69\n"
    "v_accvgpr_read_b32 v230, a70\n"
    "v_accvgpr_read_b32 v231, a71\n"
    "v_accvgpr_read_b32 v232, a84\n"
    "v_accvgpr_read_b32 v233, a85\n"
    "v_accvgpr_read_b32 v234, a86\n"
    "v_accvgpr_read_b32 v235, a87\n"
    "v_cvt_pk_bf16_f32 v48, v228, v229\n"
    "v_cvt_pk_bf16_f32 v49, v230, v231\n"
    "v_cvt_pk_bf16_f32 v50, v232, v233\n"
    "v_cvt_pk_bf16_f32 v51, v234, v235\n"
    "v_mfma_f32_16x16x32_bf16 v[176:179], a[204:207], v[28:31], v[176:179]\n"
    "v_accvgpr_read_b32 v236, a100\n"
    "v_accvgpr_read_b32 v237, a101\n"
    "v_accvgpr_read_b32 v238, a102\n"
    "v_accvgpr_read_b32 v239, a103\n"
    "v_accvgpr_read_b32 v240, a116\n"
    "v_accvgpr_read_b32 v241, a117\n"
    "v_accvgpr_read_b32 v242, a118\n"
    "v_accvgpr_read_b32 v243, a119\n"
    "v_cvt_pk_bf16_f32 v52, v236, v237\n"
    "v_cvt_pk_bf16_f32 v53, v238, v239\n"
    "v_cvt_pk_bf16_f32 v54, v240, v241\n"
    "v_cvt_pk_bf16_f32 v55, v242, v243\n"
    "v_mfma_f32_16x16x32_bf16 v[176:179], a[208:211], v[32:35], v[176:179]\n"
    "v_accvgpr_read_b32 v228, a132\n"
    "v_accvgpr_read_b32 v229, a133\n"
    "v_accvgpr_read_b32 v230, a134\n"
    "v_accvgpr_read_b32 v231, a135\n"
    "v_accvgpr_read_b32 v232, a148\n"
    "v_accvgpr_read_b32 v233, a149\n"
    "v_accvgpr_read_b32 v234, a150\n"
    "v_accvgpr_read_b32 v235, a151\n"
    "v_cvt_pk_bf16_f32 v56, v228, v229\n"
    "v_cvt_pk_bf16_f32 v57, v230, v231\n"
    "v_cvt_pk_bf16_f32 v58, v232, v233\n"
    "v_cvt_pk_bf16_f32 v59, v234, v235\n"
    "v_mfma_f32_16x16x32_bf16 v[176:179], a[212:215], v[36:39], v[176:179]\n"
    "v_accvgpr_read_b32 v236, a164\n"
    "v_accvgpr_read_b32 v237, a165\n"
    "v_accvgpr_read_b32 v238, a166\n"
    "v_accvgpr_read_b32 v239, a167\n"
    "v_accvgpr_read_b32 v240, a180\n"
    "v_accvgpr_read_b32 v241, a181\n"
    "v_accvgpr_read_b32 v242, a182\n"
    "v_accvgpr_read_b32 v243, a183\n"
    "v_cvt_pk_bf16_f32 v60, v236, v237\n"
    "v_cvt_pk_bf16_f32 v61, v238, v239\n"
    "v_cvt_pk_bf16_f32 v62, v240, v241\n"
    "v_cvt_pk_bf16_f32 v63, v242, v243\n"
    "v_mfma_f32_16x16x32_bf16 v[180:183], a[192:195], v[40:43], 0\n"
    "v_accvgpr_read_b32 v228, a8\n"
    "v_accvgpr_read_b32 v229, a9\n"
    "v_accvgpr_read_b32 v230, a10\n"
    "v_accvgpr_read_b32 v231, a11\n"
    "v_accvgpr_read_b32 v232, a24\n"
    "v_accvgpr_read_b32 v233, a25\n"
    "v_accvgpr_read_b32 v234, a26\n"
    "v_accvgpr_read_b32 v235, a27\n"
    "v_cvt_pk_bf16_f32 v64, v228, v229\n"
    "v_cvt_pk_bf16_f32 v65, v230, v231\n"
    "v_cvt_pk_bf16_f32 v66, v232, v233\n"
    "v_cvt_pk_bf16_f32 v67, v234, v235\n"
    "v_mfma_f32_16x16x32_bf16 v[180:183], a[196:199], v[44:47], v[180:183]\n"
    "v_accvgpr_read_b32 v236, a40\n"
    "v_accvgpr_read_b32 v237, a41\n"
    "v_accvgpr_read_b32 v238, a42\n"
    "v_accvgpr_read_b32 v239, a43\n"
    "v_accvgpr_read_b32 v240, a56\n"
    "v_accvgpr_read_b32 v241, a57\n"
    "v_accvgpr_read_b32 v242, a58\n"
    "v_accvgpr_read_b32 v243, a59\n"
    "v_cvt_pk_bf16_f32 v68, v236, v237\n"
    "v_cvt_pk_bf16_f32 v69, v238, v239\n"
    "v_cvt_pk_bf16_f32 v70, v240, v241\n"
    "v_cvt_pk_bf16_f32 v71, v242, v243\n"
    "v_mfma_f32_16x16x32_bf16 v[180:183], a[200:203], v[48:51], v[180:183]\n"
    "v_accvgpr_read_b32 v228, a72\n"
    "v_accvgpr_read_b32 v229, a73\n"
    "v_accvgpr_read_b32 v230, a74\n"
    "v_accvgpr_read_b32 v231, a75\n"
    "v_accvgpr_read_b32 v232, a88\n"
    "v_accvgpr_read_b32 v233, a89\n"
    "v_accvgpr_read_b32 v234, a90\n"
    "v_accvgpr_read_b32 v235, a91\n"
    "v_cvt_pk_bf16_f32 v72, v228, v229\n"
    "v_cvt_pk_bf16_f32 v73, v230, v231\n"
    "v_cvt_pk_bf16_f32 v74, v232, v233\n"
    "v_cvt_pk_bf16_f32 v75, v234, v235\n"
    "v_mfma_f32_16x16x32_bf16 v[180:183], a[204:207], v[52:55], v[180:183]\n"
    "v_accvgpr_read_b32 v236, a104\n"
    "v_accvgpr_read_b32 v237, a105\n"
    "v_accvgpr_read_b32 v238, a106\n"
    "v_accvgpr_read_b32 v239, a107\n"
    "v_accvgpr_read_b32 v240, a120\n"
    "v_accvgpr_read_b32 v241, a121\n"
    "v_accvgpr_read_b32 v242, a122\n"
    "v_accvgpr_read_b32 v243, a123\n"
    "v_cvt_pk_bf16_f32 v76, v236, v237\n"
    "v_cvt_pk_bf16_f32 v77, v238, v239\n"
    "v_cvt_pk_bf16_f32 v78, v240, v241\n"
    "v_cvt_pk_bf16_f32 v79, v242, v243\n"
    "v_mfma_f32_16x16x32_bf16 v[180:183], a[208:211], v[56:59], v[180:183]\n"
    "v_accvgpr_read_b32 v228, a136\n"
    "v_accvgpr_read_b32 v229, a137\n"
    "v_accvgpr_read_b32 v230, a138\n"
    "v_accvgpr_read_b32 v231, a139\n"
    "v_accvgpr_read_b32 v232, a152\n"
    "v_accvgpr_read_b32 v233, a153\n"
    "v_accvgpr_read_b32 v234, a154\n"
    "v_accvgpr_read_b32 v235, a155\n"
    "v_cvt_pk_bf16_f32 v80, v228, v229\n"
    "v_cvt_pk_bf16_f32 v81, v230, v231\n"
    "v_cvt_pk_bf16_f32 v82, v232, v233\n"
    "v_cvt_pk_bf16_f32 v83, v234, v235\n"
    "v_mfma_f32_16x16x32_bf16 v[180:183], a[212:215], v[60:63], v[180:183]\n"
    "v_accvgpr_read_b32 v236, a168\n"
    "v_accvgpr_read_b32 v237, a169\n"
    "v_accvgpr_read_b32 v238, a170\n"
    "v_accvgpr_read_b32 v239, a171\n"
    "v_accvgpr_read_b32 v240, a184\n"
    "v_accvgpr_read_b32 v241, a185\n"
    "v_accvgpr_read_b32 v242, a186\n"
    "v_accvgpr_read_b32 v243, a187\n"
    "v_cvt_pk_bf16_f32 v84, v236, v237\n"
    "v_cvt_pk_bf16_f32 v85, v238, v239\n"
    "v_cvt_pk_bf16_f32 v86, v240, v241\n"
    "v_cvt_pk_bf16_f32 v87, v242, v243\n"
    "v_max3_f32 v244, v176, v177, v178\n"
    "v_max_f32 v244, v244, v179\n"
    "v_mfma_f32_16x16x32_bf16 v[184:187], a[192:195], v[64:67], 0\n"
    "v_accvgpr_read_b32 v228, a12\n"
    "v_accvgpr_read_b32 v229, a13\n"
    "v_accvgpr_read_b32 v230, a14\n"
    "v_accvgpr_read_b32 v231, a15\n"
    "v_accvgpr_read_b32 v232, a28\n"
    "v_accvgpr_read_b32 v233, a29\n"
    "v_accvgpr_read_b32 v234, a30\n"
    "v_accvgpr_read_b32 v235, a31\n"
    "v_cvt_pk_bf16_f32 v88, v228, v229\n"
    "v_cvt_pk_bf16_f32 v89, v230, v231\n"
    "v_cvt_pk_bf16_f32 v90, v232, v233\n"
    "v_cvt_pk_bf16_f32 v91, v234, v235\n"
    "v_mfma_f32_16x16x32_bf16 v[184:187], a[196:199], v[68:71], v[184:187]\n"
    "v_accvgpr_read_b32 v236, a44\n"
    "v_accvgpr_read_b32 v237, a45\n"
    "v_accvgpr_read_b32 v238, a46\n"
    "v_accvgpr_read_b32 v239, a47\n"
    "v_accvgpr_read_b32 v240, a60\n"
    "v_accvgpr_read_b32 v241, a61\n"
    "v_accvgpr_read_b32 v242, a62\n"
    "v_accvgpr_read_b32 v243, a63\n"
    "v_cvt_pk_bf16_f32 v92, v236, v237\n"
    "v_cvt_pk_bf16_f32 v93, v238, v239\n"
    "v_cvt_pk_bf16_f32 v94, v240, v241\n"
    "v_cvt_pk_bf16_f32 v95, v242, v243\n"
    "v_mfma_f32_16x16x32_bf16 v[184:187], a[200:203], v[72:75], v[184:187]\n"
    "v_accvgpr_read_b32 v228, a76\n"
    "v_accvgpr_read_b32 v229, a77\n"
    "v_accvgpr_read_b32 v230, a78\n"
    "v_accvgpr_read_b32 v231, a79\n"
    "v_accvgpr_read_b32 v232, a92\n"
    "v_accvgpr_read_b32 v233, a93\n"
    "v_accvgpr_read_b32 v234, a94\n"
    "v_accvgpr_read_b32 v235, a95\n"
    "v_cvt_pk_bf16_f32 v96, v228, v229\n"
    "v_cvt_pk_bf16_f32 v97, v230, v231\n"
    "v_cvt_pk_bf16_f32 v98, v232, v233\n"
    "v_cvt_pk_bf16_f32 v99, v234, v235\n"
    "v_mfma_f32_16x16x32_bf16 v[184:187], a[204:207], v[76:79], v[184:187]\n"
    "v_accvgpr_read_b32 v236, a108\n"
    "v_accvgpr_read_b32 v237, a109\n"
    "v_accvgpr_read_b32 v238, a110\n"
    "v_accvgpr_read_b32 v239, a111\n"
    "v_accvgpr_read_b32 v240, a124\n"
    "v_accvgpr_read_b32 v241, a125\n"
    "v_accvgpr_read_b32 v242, a126\n"
    "v_accvgpr_read_b32 v243, a127\n"
    "v_cvt_pk_bf16_f32 v100, v236, v237\n"
    "v_cvt_pk_bf16_f32 v101, v238, v239\n"
    "v_cvt_pk_bf16_f32 v102, v240, v241\n"
    "v_cvt_pk_bf16_f32 v103, v242, v243\n"
    "v_mfma_f32_16x16x32_bf16 v[184:187], a[208:211], v[80:83], v[184:187]\n"
    "v_accvgpr_read_b32 v228, a140\n"
    "v_accvgpr_read_b32 v229, a141\n"
    "v_accvgpr_read_b32 v230, a142\n"
    "v_accvgpr_read_b32 v231, a143\n"
    "v_accvgpr_read_b32 v232, a156\n"
    "v_accvgpr_read_b32 v233, a157\n"
    "v_accvgpr_read_b32 v234, a158\n"
    "v_accvgpr_read_b32 v235, a159\n"
    "v_cvt_pk_bf16_f32 v104, v228, v229\n"
    "v_cvt_pk_bf16_f32 v105, v230, v231\n"
    "v_cvt_pk_bf16_f32 v106, v232, v233\n"
    "v_cvt_pk_bf16_f32 v107, v234, v235\n"
    "v_mfma_f32_16x16x32_bf16 v[184:187], a[212:215], v[84:87], v[184:187]\n"
    "v_accvgpr_read_b32 v236, a172\n"
    "v_accvgpr_read_b32 v237, a173\n"
    "v_accvgpr_read_b32 v238, a174\n"
    "v_accvgpr_read_b32 v239, a175\n"
    "v_accvgpr_read_b32 v240, a188\n"
    "v_accvgpr_read_b32 v241, a189\n"
    "v_accvgpr_read_b32 v242, a190\n"
    "v_accvgpr_read_b32 v243, a191\n"
    "v_cvt_pk_bf16_f32 v108, v236, v237\n"
    "v_cvt_pk_bf16_f32 v109, v238, v239\n"
    "v_cvt_pk_bf16_f32 v110, v240, v241\n"
    "v_cvt_pk_bf16_f32 v111, v242, v243\n"
    "v_max3_f32 v245, v180, v181, v182\n"
    "v_max_f32 v245, v245, v183\n"
    "v_mfma_f32_16x16x32_bf16 v[188:191], a[192:195], v[88:91], 0\n"
    "v_mfma_f32_16x16x32_bf16 v[188:191], a[196:199], v[92:95], v[188:191]\n"
    "v_mfma_f32_16x16x32_bf16 v[188:191], a[200:203], v[96:99], v[188:191]\n"
    "v_mfma_f32_16x16x32_bf16 v[188:191], a[204:207], v[100:103], v[188:191]\n"
    "v_mfma_f32_16x16x32_bf16 v[188:191], a[208:211], v[104:107], v[188:191]\n"
    "v_mfma_f32_16x16x32_bf16 v[188:191], a[212:215], v[108:111], v[188:191]\n"
    "v_max3_f32 v246, v184, v185, v186\n"
    "v_max_f32 v246, v246, v187\n"
    "v_mov_b32 v248, v244\n"
    "s_nop 1\n"
    "v_permlane16_swap_b32 v244, v248\n"
    "v_max_f32 v244, v244, v248\n"
    "v_mov_b32 v248, v244\n"
    "s_nop 1\n"
    "v_permlane32_swap_b32 v244, v248\n"
    "v_max_f32 v244, v244, v248\n"
    "v_sub_f32 v192, 0, v244\n"
    "v_sub_f32 v193, 0, v244\n"
    "v_sub_f32 v194, 0, v244\n"
    "v_sub_f32 v195, 0, v244\n"
    "v_mov_b32 v249, v245\n"
    "s_nop 1\n"
    "v_permlane16_swap_b32 v245, v249\n"
    "v_max_f32 v245, v245, v249\n"
    "v_mov_b32 v249, v245\n"
    "s_nop 1\n"
    "v_permlane32_swap_b32 v245, v249\n"
    "v_max_f32 v245, v245, v249\n"
    "v_sub_f32 v196, 0, v245\n"
    "v_sub_f32 v197, 0, v245\n"
    "v_sub_f32 v198, 0, v245\n"
    "v_sub_f32 v199, 0, v245\n"
    "v_mov_b32 v250, v246\n"
    "s_nop 1\n"
    "v_permlane16_swap_b32 v246, v250\n"
    "v_max_f32 v246, v246, v250\n"
    "v_mov_b32 v250, v246\n"
    "s_nop 1\n"
    "v_permlane32_swap_b32 v246, v250\n"
    "v_max_f32 v246, v246, v250\n"
    "v_sub_f32 v200, 0, v246\n"
    "v_sub_f32 v201, 0, v246\n"
    "v_sub_f32 v202, 0, v246\n"
    "v_sub_f32 v203, 0, v246\n"
    "v_max3_f32 v247, v188, v189, v190\n"
    "v_max_f32 v247, v247, v191\n"
    "v_mov_b32 v251, v247\n"
    "s_nop 1\n"
    "v_permlane16_swap_b32 v247, v251\n"
    "v_max_f32 v247, v247, v251\n"
    "v_mov_b32 v251, v247\n"
    "s_nop 1\n"
    "v_permlane32_swap_b32 v247, v251\n"
    "v_max_f32 v247, v247, v251\n"
    "v_sub_f32 v204, 0, v247\n"
    "v_sub_f32 v205, 0, v247\n"
    "v_sub_f32 v206, 0, v247\n"
    "v_sub_f32 v207, 0, v247\n"
    "L_iter_m1:\n"
    "; iter -1\n"
    "v_mfma_f32_16x16x32_bf16 v[176:179], a[192:195], v[16:19], v[192:195]\n"
    "v_mfma_f32_16x16x32_bf16 v[176:179], a[196:199], v[20:23], v[176:179]\n"
    "v_mfma_f32_16x16x32_bf16 v[176:179], a[200:203], v[24:27], v[176:179]\n"
    "v_mfma_f32_16x16x32_bf16 v[176:179], a[204:207], v[28:31], v[176:179]\n"
    "v_mfma_f32_16x16x32_bf16 v[176:179], a[208:211], v[32:35], v[176:179]\n"
    "v_mfma_f32_16x16x32_bf16 v[176:179], a[212:215], v[36:39], v[176:179]\n"
    "v_mfma_f32_16x16x32_bf16 v[180:183], a[192:195], v[40:43], v[196:199]\n"
    "v_mfma_f32_16x16x32_bf16 v[180:183], a[196:199], v[44:47], v[180:183]\n"
    "v_mfma_f32_16x16x32_bf16 v[180:183], a[200:203], v[48:51], v[180:183]\n"
    "s_nop 4\n"
    "v_exp_f32 v176, v176\n"
    "v_mfma_f32_16x16x32_bf16 v[180:183], a[204:207], v[52:55], v[180:183]\n"
    "s_nop 2\n"
    "v_exp_f32 v177, v177\n"
    "v_mfma_f32_16x16x32_bf16 v[180:183], a[208:211], v[56:59], v[180:183]\n"
    "s_nop 0\n"
    "v_exp_f32 v178, v178\n"
    "v_mfma_f32_16x16x32_bf16 v[180:183], a[212:215], v[60:63], v[180:183]\n"
    "v_exp_f32 v179, v179\n"
    "v_mfma_f32_16x16x32_bf16 v[184:187], a[192:195], v[64:67], v[200:203]\n"
    "v_cvt_pk_bf16_f32 v112, v176, v177\n"
    "v_cvt_pk_bf16_f32 v113, v178, v179\n"
    "v_mfma_f32_16x16x32_bf16 v[184:187], a[196:199], v[68:71], v[184:187]\n"
    "v_mfma_f32_16x16x32_bf16 v[184:187], a[200:203], v[72:75], v[184:187]\n"
    "s_nop 1\n"
    "v_exp_f32 v180, v180\n"
    "v_mfma_f32_16x16x32_bf16 v[184:187], a[204:207], v[76:79], v[184:187]\n"
    "v_exp_f32 v181, v181\n"
    "v_mfma_f32_16x16x32_bf16 v[184:187], a[208:211], v[80:83], v[184:187]\n"
    "v_exp_f32 v182, v182\n"
    "v_mfma_f32_16x16x32_bf16 v[184:187], a[212:215], v[84:87], v[184:187]\n"
    "v_exp_f32 v183, v183\n"
    "v_mfma_f32_16x16x32_bf16 v[188:191], a[192:195], v[88:91], v[204:207]\n"
    "ds_read_b128 a[192:195], v212 offset:6144\n"
    "v_cvt_pk_bf16_f32 v120, v180, v181\n"
    "v_mfma_f32_16x16x32_bf16 v[188:191], a[196:199], v[92:95], v[188:191]\n"
    "ds_read_b128 a[196:199], v213 offset:6144\n"
    "v_cvt_pk_bf16_f32 v121, v182, v183\n"
    "v_mfma_f32_16x16x32_bf16 v[188:191], a[200:203], v[96:99], v[188:191]\n"
    "ds_read_b128 a[200:203], v212 offset:6272\n"
    "v_mfma_f32_16x16x32_bf16 v[188:191], a[204:207], v[100:103], v[188:191]\n"
    "ds_read_b128 a[204:207], v213 offset:6272\n"
    "v_mfma_f32_16x16x32_bf16 v[188:191], a[208:211], v[104:107], v[188:191]\n"
    "ds_read_b128 a[208:211], v212 offset:6400\n"
    "v_mfma_f32_16x16x32_bf16 v[188:191], a[212:215], v[108:111], v[188:191]\n"
    "ds_read_b128 a[212:215], v213 offset:6400\n"
    "s_waitcnt lgkmcnt(3)\n"
    "v_mfma_f32_16x16x32_bf16 v[176:179], a[192:195], v[16:19], v[192:195]\n"
    "v_exp_f32 v184, v184\n"
    "v_mfma_f32_16x16x32_bf16 v[176:179], a[196:199], v[20:23], v[176:179]\n"
    "v_exp_f32 v185, v185\n"
    "v_mfma_f32_16x16x32_bf16 v[176:179], a[200:203], v[24:27], v[176:179]\n"
    "v_exp_f32 v186, v186\n"
    "s_waitcnt lgkmcnt(0)\n"
    "v_cvt_pk_bf16_f32 v128, v184, v185\n"
    "v_mfma_f32_16x16x32_bf16 v[176:179], a[204:207], v[28:31], v[176:179]\n"
    "v_exp_f32 v187, v187\n"
    "s_nop 0\n"
    "v_cvt_pk_bf16_f32 v129, v186, v187\n"
    "v_mfma_f32_16x16x32_bf16 v[176:179], a[208:211], v[32:35], v[176:179]\n"
    "v_exp_f32 v188, v188\n"
    "v_mfma_f32_16x16x32_bf16 v[176:179], a[212:215], v[36:39], v[176:179]\n"
    "v_exp_f32 v189, v189\n"
    "v_mfma_f32_16x16x32_bf16 v[180:183], a[192:195], v[40:43], v[196:199]\n"
    "v_exp_f32 v190, v190\n"
    "v_cvt_pk_bf16_f32 v136, v188, v189\n"
    "v_mfma_f32_16x16x32_bf16 v[180:183], a[196:199], v[44:47], v[180:183]\n"
    "v_exp_f32 v191, v191\n"
    "s_nop 0\n"
    "v_cvt_pk_bf16_f32 v137, v190, v191\n"
    "v_mfma_f32_16x16x32_bf16 v[180:183], a[200:203], v[48:51], v[180:183]\n"
    "v_exp_f32 v176, v176\n"
    "v_mfma_f32_16x16x32_bf16 v[180:183], a[204:207], v[52:55], v[180:183]\n"
    "v_exp_f32 v177, v177\n"
    "v_mfma_f32_16x16x32_bf16 v[180:183], a[208:211], v[56:59], v[180:183]\n"
    "v_exp_f32 v178, v178\n"
    "v_mfma_f32_16x16x32_bf16 v[180:183], a[212:215], v[60:63], v[180:183]\n"
    "v_exp_f32 v179, v179\n"
    "v_mfma_f32_16x16x32_bf16 v[184:187], a[192:195], v[64:67], v[200:203]\n"
    "v_cvt_pk_bf16_f32 v114, v176, v177\n"
    "v_cvt_pk_bf16_f32 v115, v178, v179\n"
    "v_mfma_f32_16x16x32_bf16 v[184:187], a[196:199], v[68:71], v[184:187]\n"
    "v_mfma_f32_16x16x32_bf16 v[184:187], a[200:203], v[72:75], v[184:187]\n"
    "s_nop 1\n"
    "v_exp_f32 v180, v180\n"
    "v_mfma_f32_16x16x32_bf16 v[184:187], a[204:207], v[76:79], v[184:187]\n"
    "v_exp_f32 v181, v181\n"
    "v_mfma_f32_16x16x32_bf16 v[184:187], a[208:211], v[80:83], v[184:187]\n"
    "v_exp_f32 v182, v182\n"
    "v_mfma_f32_16x16x32_bf16 v[184:187], a[212:215], v[84:87], v[184:187]\n"
    "v_exp_f32 v183, v183\n"
    "v_mfma_f32_16x16x32_bf16 v[188:191], a[192:195], v[88:91], v[204:207]\n"
    "ds_read_b128 a[192:195], v212 offset:12288\n"
    "v_cvt_pk_bf16_f32 v122, v180, v181\n"
    "v_mfma_f32_16x16x32_bf16 v[188:191], a[196:199], v[92:95], v[188:191]\n"
    "ds_read_b128 a[196:199], v213 offset:12288\n"
    "v_cvt_pk_bf16_f32 v123, v182, v183\n"
    "v_mfma_f32_16x16x32_bf16 v[188:191], a[200:203], v[96:99], v[188:191]\n"
    "ds_read_b128 a[200:203], v212 offset:12416\n"
    "v_mfma_f32_16x16x32_bf16 v[188:191], a[204:207], v[100:103], v[188:191]\n"
    "ds_read_b128 a[204:207], v213 offset:12416\n"
    "v_mfma_f32_16x16x32_bf16 v[188:191], a[208:211], v[104:107], v[188:191]\n"
    "ds_read_b128 a[208:211], v212 offset:12544\n"
    "v_mfma_f32_16x16x32_bf16 v[188:191], a[212:215], v[108:111], v[188:191]\n"
    "ds_read_b128 a[212:215], v213 offset:12544\n"
    "s_waitcnt lgkmcnt(3)\n"
    "v_mfma_f32_16x16x32_bf16 v[176:179], a[192:195], v[16:19], v[192:195]\n"
    "v_exp_f32 v184, v184\n"
    "v_mfma_f32_16x16x32_bf16 v[176:179], a[196:199], v[20:23], v[176:179]\n"
    "v_exp_f32 v185, v185\n"
    "v_mfma_f32_16x16x32_bf16 v[176:179], a[200:203], v[24:27], v[176:179]\n"
    "v_exp_f32 v186, v186\n"
    "s_waitcnt lgkmcnt(0)\n"
    "v_cvt_pk_bf16_f32 v130, v184, v185\n"
    "v_mfma_f32_16x16x32_bf16 v[176:179], a[204:207], v[28:31], v[176:179]\n"
    "v_exp_f32 v187, v187\n"
    "s_nop 0\n"
    "v_cvt_pk_bf16_f32 v131, v186, v187\n"
    "v_mfma_f32_16x16x32_bf16 v[176:179], a[208:211], v[32:35], v[176:179]\n"
    "v_exp_f32 v188, v188\n"
    "v_mfma_f32_16x16x32_bf16 v[176:179], a[212:215], v[36:39], v[176:179]\n"
    "v_exp_f32 v189, v189\n"
    "v_mfma_f32_16x16x32_bf16 v[180:183], a[192:195], v[40:43], v[196:199]\n"
    "v_exp_f32 v190, v190\n"
    "v_cvt_pk_bf16_f32 v138, v188, v189\n"
    "v_mfma_f32_16x16x32_bf16 v[180:183], a[196:199], v[44:47], v[180:183]\n"
    "v_exp_f32 v191, v191\n"
    "s_nop 0\n"
    "v_cvt_pk_bf16_f32 v139, v190, v191\n"
    "v_mfma_f32_16x16x32_bf16 v[180:183], a[200:203], v[48:51], v[180:183]\n"
    "v_exp_f32 v176, v176\n"
    "v_mfma_f32_16x16x32_bf16 v[180:183], a[204:207], v[52:55], v[180:183]\n"
    "v_exp_f32 v177, v177\n"
    "v_mfma_f32_16x16x32_bf16 v[180:183], a[208:211], v[56:59], v[180:183]\n"
    "v_exp_f32 v178, v178\n"
    "v_mfma_f32_16x16x32_bf16 v[180:183], a[212:215], v[60:63], v[180:183]\n"
    "v_exp_f32 v179, v179\n"
    "v_mfma_f32_16x16x32_bf16 v[184:187], a[192:195], v[64:67], v[200:203]\n"
    "v_cvt_pk_bf16_f32 v116, v176, v177\n"
    "v_cvt_pk_bf16_f32 v117, v178, v179\n"
    "v_mfma_f32_16x16x32_bf16 v[184:187], a[196:199], v[68:71], v[184:187]\n"
    "v_mfma_f32_16x16x32_bf16 v[184:187], a[200:203], v[72:75], v[184:187]\n"
    "s_nop 1\n"
    "v_exp_f32 v180, v180\n"
    "v_mfma_f32_16x16x32_bf16 v[184:187], a[204:207], v[76:79], v[184:187]\n"
    "v_exp_f32 v181, v181\n"
    "v_mfma_f32_16x16x32_bf16 v[184:187], a[208:211], v[80:83], v[184:187]\n"
    "v_exp_f32 v182, v182\n"
    "v_mfma_f32_16x16x32_bf16 v[184:187], a[212:215], v[84:87], v[184:187]\n"
    "v_exp_f32 v183, v183\n"
    "v_mfma_f32_16x16x32_bf16 v[188:191], a[192:195], v[88:91], v[204:207]\n"
    "ds_read_b128 a[192:195], v212 offset:18432\n"
    "v_cvt_pk_bf16_f32 v124, v180, v181\n"
    "v_mfma_f32_16x16x32_bf16 v[188:191], a[196:199], v[92:95], v[188:191]\n"
    "ds_read_b128 a[196:199], v213 offset:18432\n"
    "v_cvt_pk_bf16_f32 v125, v182, v183\n"
    "v_mfma_f32_16x16x32_bf16 v[188:191], a[200:203], v[96:99], v[188:191]\n"
    "ds_read_b128 a[200:203], v212 offset:18560\n"
    "v_mfma_f32_16x16x32_bf16 v[188:191], a[204:207], v[100:103], v[188:191]\n"
    "ds_read_b128 a[204:207], v213 offset:18560\n"
    "v_mfma_f32_16x16x32_bf16 v[188:191], a[208:211], v[104:107], v[188:191]\n"
    "ds_read_b128 a[208:211], v212 offset:18688\n"
    "v_mfma_f32_16x16x32_bf16 v[188:191], a[212:215], v[108:111], v[188:191]\n"
    "ds_read_b128 a[212:215], v213 offset:18688\n"
    "s_waitcnt lgkmcnt(3)\n"
    "v_mfma_f32_16x16x32_bf16 v[176:179], a[192:195], v[16:19], v[192:195]\n"
    "v_exp_f32 v184, v184\n"
    "v_mfma_f32_16x16x32_bf16 v[176:179], a[196:199], v[20:23], v[176:179]\n"
    "v_exp_f32 v185, v185\n"
    "v_mfma_f32_16x16x32_bf16 v[176:179], a[200:203], v[24:27], v[176:179]\n"
    "v_exp_f32 v186, v186\n"
    "s_waitcnt lgkmcnt(0)\n"
    "v_cvt_pk_bf16_f32 v132, v184, v185\n"
    "v_mfma_f32_16x16x32_bf16 v[176:179], a[204:207], v[28:31], v[176:179]\n"
    "v_exp_f32 v187, v187\n"
    "s_nop 0\n"
    "v_cvt_pk_bf16_f32 v133, v186, v187\n"
    "v_mfma_f32_16x16x32_bf16 v[176:179], a[208:211], v[32:35], v[176:179]\n"
    "v_exp_f32 v188, v188\n"
    "v_mfma_f32_16x16x32_bf16 v[176:179], a[212:215], v[36:39], v[176:179]\n"
    "v_exp_f32 v189, v189\n"
    "v_mfma_f32_16x16x32_bf16 v[180:183], a[192:195], v[40:43], v[196:199]\n"
    "v_exp_f32 v190, v190\n"
    "v_cvt_pk_bf16_f32 v140, v188, v189\n"
    "v_mfma_f32_16x16x32_bf16 v[180:183], a[196:199], v[44:47], v[180:183]\n"
    "v_exp_f32 v191, v191\n"
    "s_nop 0\n"
    "v_cvt_pk_bf16_f32 v141, v190, v191\n"
    "v_mfma_f32_16x16x32_bf16 v[180:183], a[200:203], v[48:51], v[180:183]\n"
    "v_exp_f32 v176, v176\n"
    "v_mfma_f32_16x16x32_bf16 v[180:183], a[204:207], v[52:55], v[180:183]\n"
    "v_exp_f32 v177, v177\n"
    "v_mfma_f32_16x16x32_bf16 v[180:183], a[208:211], v[56:59], v[180:183]\n"
    "v_exp_f32 v178, v178\n"
    "v_mfma_f32_16x16x32_bf16 v[180:183], a[212:215], v[60:63], v[180:183]\n"
    "v_exp_f32 v179, v179\n"
    "v_mfma_f32_16x16x32_bf16 v[184:187], a[192:195], v[64:67], v[200:203]\n"
    "v_cvt_pk_bf16_f32 v118, v176, v177\n"
    "v_cvt_pk_bf16_f32 v119, v178, v179\n"
    "v_mfma_f32_16x16x32_bf16 v[184:187], a[196:199], v[68:71], v[184:187]\n"
    "v_mfma_f32_16x16x32_bf16 v[184:187], a[200:203], v[72:75], v[184:187]\n"
    "s_nop 1\n"
    "v_exp_f32 v180, v180\n"
    "v_mfma_f32_16x16x32_bf16 v[184:187], a[204:207], v[76:79], v[184:187]\n"
    "v_exp_f32 v181, v181\n"
    "v_mfma_f32_16x16x32_bf16 v[184:187], a[208:211], v[80:83], v[184:187]\n"
    "v_exp_f32 v182, v182\n"
    "v_exp_f32 v183, v183\n"
    "s_nop 0\n"
    "v_cvt_pk_bf16_f32 v127, v182, v183\n"
    "v_mfma_f32_16x16x32_bf16 v[184:187], a[212:215], v[84:87], v[184:187]\n"
    "s_waitcnt vmcnt(11)\n"
    "s_barrier\n"
    "ds_read_b64_tr_b16 a[216:217], v214 offset:0\n"
    "v_mfma_f32_16x16x32_bf16 v[188:191], a[192:195], v[88:91], v[204:207]\n"
    "ds_read_b128 a[192:195], v212 offset:24576\n"
    "ds_read_b64_tr_b16 a[218:219], v214 offset:6144\n"
    "v_mfma_f32_16x16x32_bf16 v[188:191], a[196:199], v[92:95], v[188:191]\n"
    "ds_read_b128 a[196:199], v213 offset:24576\n"
    "ds_read_b64_tr_b16 a[220:221], v214 offset:12288\n"
    "v_mfma_f32_16x16x32_bf16 v[188:191], a[200:203], v[96:99], v[188:191]\n"
    "ds_read_b128 a[200:203], v212 offset:24704\n"
    "v_exp_f32 v184, v184\n"
    "v_exp_f32 v185, v185\n"
    "v_exp_f32 v186, v186\n"
    "v_exp_f32 v187, v187\n"
    "s_nop 0\n"
    "v_cvt_pk_bf16_f32 v135, v186, v187\n"
    "v_mfma_f32_16x16x32_bf16 v[188:191], a[204:207], v[100:103], v[188:191]\n"
    "ds_read_b128 a[204:207], v213 offset:24704\n"
    "ds_read_b64_tr_b16 a[222:223], v214 offset:18432\n"
    "v_mfma_f32_16x16x32_bf16 v[188:191], a[208:211], v[104:107], v[188:191]\n"
    "ds_read_b128 a[208:211], v212 offset:24832\n"
    "v_cvt_pk_bf16_f32 v126, v180, v181\n"
    "v_mfma_f32_16x16x32_bf16 v[188:191], a[212:215], v[108:111], v[188:191]\n"
    "ds_read_b128 a[212:215], v213 offset:24832\n"
    "v_cvt_pk_bf16_f32 v134, v184, v185\n"
    "s_mov_b64 s[36:37], s[4:5]\n"
    "s_and_b32 s37, s37, 0xffff\n"
    "s_mov_b32 s38, 7864320\n"
    "s_mov_b32 s39, 0x00020000\n"
    "s_lshl_b32 s85, %0, 2\n"
    "s_add_u32 s85, s85, s64\n"
    "s_mul_i32 s96, s85, 7680\n"
    "s_mov_b64 s[40:41], s[6:7]\n"
    "s_and_b32 s41, s41, 0xffff\n"
    "s_mov_b32 s42, 1966080\n"
    "s_mov_b32 s43, 0x00020000\n"
    "s_lshl_b32 s85, %0, 2\n"
    "s_add_u32 s85, s85, s64\n"
    "s_mul_i32 s97, s85, 1920\n"
    "; iter 0\n"
    "s_waitcnt lgkmcnt(4)\n"
    "ds_read_b64_tr_b16 a[224:225], v215 offset:0\n"
    "v_mfma_f32_16x16x32_bf16 v[176:179], a[192:195], v[16:19], v[192:195]\n"
    "v_exp_f32 v188, v188\n"
    "v_mfma_f32_16x16x32_bf16 v[176:179], a[196:199], v[20:23], v[176:179]\n"
    "v_exp_f32 v189, v189\n"
    "v_mfma_f32_16x16x32_bf16 v[176:179], a[200:203], v[24:27], v[176:179]\n"
    "v_exp_f32 v190, v190\n"
    "s_waitcnt lgkmcnt(1)\n"
    "v_mfma_f32_16x16x32_bf16 v[176:179], a[204:207], v[28:31], v[176:179]\n"
    "v_exp_f32 v191, v191\n"
    "v_mfma_f32_16x16x32_bf16 v[176:179], a[208:211], v[32:35], v[176:179]\n"
    "ds_read_b64_tr_b16 a[226:227], v215 offset:6144\n"
    "v_cvt_pk_bf16_f32 v142, v188, v189\n"
    "v_mfma_f32_16x16x32_bf16 v[176:179], a[212:215], v[36:39], v[176:179]\n"
    "ds_read_b64_tr_b16 a[228:229], v215 offset:12288\n"
    "v_mfma_f32_16x16x32_bf16 a[0:3], a[216:219], v[112:115], 0\n"
    "ds_read_b64_tr_b16 a[230:231], v215 offset:18432\n"
    "v_cvt_pk_bf16_f32 v143, v190, v191\n"
    "v_mfma_f32_16x16x32_bf16 a[0:3], a[220:223], v[116:119], a[0:3]\n"
    "ds_read_b64_tr_b16 a[232:233], v216 offset:0\n"
    "s_add_u32 m0, s65, 0\n"
    "v_mfma_f32_16x16x32_bf16 a[4:7], a[216:219], v[120:123], 0\n"
    "ds_read_b64_tr_b16 a[234:235], v216 offset:6144\n"
    "v_mfma_f32_16x16x32_bf16 a[4:7], a[220:223], v[124:127], a[4:7]\n"
    "ds_read_b64_tr_b16 a[236:237], v216 offset:12288\n"
    "v_mfma_f32_16x16x32_bf16 a[8:11], a[216:219], v[128:131], 0\n"
    "ds_read_b64_tr_b16 a[238:239], v216 offset:18432\n"
    "v_mfma_f32_16x16x32_bf16 a[8:11], a[220:223], v[132:135], a[8:11]\n"
    "v_exp_f32 v176, v176\n"
    "v_mfma_f32_16x16x32_bf16 v[180:183], a[192:195], v[40:43], v[196:199]\n"
    "v_exp_f32 v177, v177\n"
    "v_mfma_f32_16x16x32_bf16 v[180:183], a[196:199], v[44:47], v[180:183]\n"
    "v_exp_f32 v178, v178\n"
    "v_mfma_f32_16x16x32_bf16 v[180:183], a[200:203], v[48:51], v[180:183]\n"
    "v_exp_f32 v179, v179\n"
    "v_mfma_f32_16x16x32_bf16 v[180:183], a[204:207], v[52:55], v[180:183]\n"
    "buffer_load_dwordx4 v218, s[44:47], s66 offen offset:0 lds\n"
    "v_mfma_f32_16x16x32_bf16 v[180:183], a[208:211], v[56:59], v[180:183]\n"
    "buffer_load_dwordx4 v219, s[44:47], s66 offen offset:1024 lds\n"
    "v_mfma_f32_16x16x32_bf16 v[180:183], a[212:215], v[60:63], v[180:183]\n"
    "buffer_load_dwordx4 v220, s[44:47], s66 offen offset:2048 lds\n"
    "v_mfma_f32_16x16x32_bf16 a[12:15], a[216:219], v[136:139], 0\n"
    "buffer_load_dwordx4 v218, s[44:47], s66 offen offset:3072 lds\n"
    "v_mfma_f32_16x16x32_bf16 a[12:15], a[220:223], v[140:143], a[12:15]\n"
    "ds_read_b64_tr_b16 a[216:217], v217 offset:0\n"
    "s_waitcnt lgkmcnt(5)\n"
    "v_mfma_f32_16x16x32_bf16 a[16:19], a[224:227], v[112:115], 0\n"
    "ds_read_b64_tr_b16 a[218:219], v217 offset:6144\n"
    "v_cvt_pk_bf16_f32 v144, v176, v177\n"
    "v_mfma_f32_16x16x32_bf16 a[16:19], a[228:231], v[116:119], a[16:19]\n"
    "ds_read_b64_tr_b16 a[220:221], v217 offset:12288\n"
    "v_cvt_pk_bf16_f32 v145, v178, v179\n"
    "v_mfma_f32_16x16x32_bf16 a[20:23], a[224:227], v[120:123], 0\n"
    "ds_read_b64_tr_b16 a[222:223], v217 offset:18432\n"
    "s_add_u32 m0, s65, 4096\n"
    "v_mfma_f32_16x16x32_bf16 a[20:23], a[228:231], v[124:127], a[20:23]\n"
    "v_exp_f32 v180, v180\n"
    "v_mfma_f32_16x16x32_bf16 v[184:187], a[192:195], v[64:67], v[200:203]\n"
    "v_exp_f32 v181, v181\n"
    "v_mfma_f32_16x16x32_bf16 v[184:187], a[196:199], v[68:71], v[184:187]\n"
    "v_exp_f32 v182, v182\n"
    "v_mfma_f32_16x16x32_bf16 v[184:187], a[200:203], v[72:75], v[184:187]\n"
    "v_exp_f32 v183, v183\n"
    "v_mfma_f32_16x16x32_bf16 v[184:187], a[204:207], v[76:79], v[184:187]\n"
    "buffer_load_dwordx4 v219, s[48:51], s66 offen offset:0 lds\n"
    "v_mfma_f32_16x16x32_bf16 v[184:187], a[208:211], v[80:83], v[184:187]\n"
    "v_cvt_pk_bf16_f32 v152, v180, v181\n"
    "v_cvt_pk_bf16_f32 v153, v182, v183\n"
    "v_mfma_f32_16x16x32_bf16 v[184:187], a[212:215], v[84:87], v[184:187]\n"
    "v_mfma_f32_16x16x32_bf16 a[24:27], a[224:227], v[128:131], 0\n"
    "buffer_load_dwordx4 v220, s[48:51], s66 offen offset:1024 lds\n"
    "v_mfma_f32_16x16x32_bf16 a[24:27], a[228:231], v[132:135], a[24:27]\n"
    "v_mfma_f32_16x16x32_bf16 a[28:31], a[224:227], v[136:139], 0\n"
    "v_mfma_f32_16x16x32_bf16 a[28:31], a[228:231], v[140:143], a[28:31]\n"
    "ds_read_b64_tr_b16 a[224:225], v214 offset:128\n"
    "s_waitcnt lgkmcnt(5)\n"
    "v_mfma_f32_16x16x32_bf16 a[32:35], a[232:235], v[112:115], 0\n"
    "ds_read_b64_tr_b16 a[226:227], v214 offset:6272\n"
    "v_mfma_f32_16x16x32_bf16 a[32:35], a[236:239], v[116:119], a[32:35]\n"
    "v_exp_f32 v184, v184\n"
    "v_mfma_f32_16x16x32_bf16 a[240:243], v[208:211], v[112:115], 0\n"
    "v_exp_f32 v185, v185\n"
    "v_mfma_f32_16x16x32_bf16 a[240:243], v[208:211], v[116:119], a[240:243]\n"
    "v_exp_f32 v186, v186\n"
    "v_mfma_f32_16x16x32_bf16 v[188:191], a[192:195], v[88:91], v[204:207]\n"
    "ds_read_b128 a[192:195], v212 offset:30720\n"
    "s_add_u32 m0, s65, 73728\n"
    "v_mfma_f32_16x16x32_bf16 v[188:191], a[196:199], v[92:95], v[188:191]\n"
    "ds_read_b128 a[196:199], v213 offset:30720\n"
    "v_cvt_pk_bf16_f32 v160, v184, v185\n"
    "v_mfma_f32_16x16x32_bf16 v[188:191], a[200:203], v[96:99], v[188:191]\n"
    "ds_read_b128 a[200:203], v212 offset:30848\n"
    "v_mfma_f32_16x16x32_bf16 v[188:191], a[204:207], v[100:103], v[188:191]\n"
    "ds_read_b128 a[204:207], v213 offset:30848\n"
    "v_mfma_f32_16x16x32_bf16 v[188:191], a[208:211], v[104:107], v[188:191]\n"
    "ds_read_b128 a[208:211], v212 offset:30976\n"
    "v_mfma_f32_16x16x32_bf16 v[188:191], a[212:215], v[108:111], v[188:191]\n"
    "ds_read_b128 a[212:215], v213 offset:30976\n"
    "v_mfma_f32_16x16x32_bf16 a[36:39], a[232:235], v[120:123], 0\n"
    "v_exp_f32 v187, v187\n"
    "v_mfma_f32_16x16x32_bf16 a[36:39], a[236:239], v[124:127], a[36:39]\n"
    "ds_read_b64_tr_b16 a[228:229], v214 offset:12416\n"
    "v_cvt_pk_bf16_f32 v161, v186, v187\n"
    "v_mfma_f32_16x16x32_bf16 a[40:43], a[232:235], v[128:131], 0\n"
    "ds_read_b64_tr_b16 a[230:231], v214 offset:18560\n"
    "v_mfma_f32_16x16x32_bf16 a[40:43], a[236:239], v[132:135], a[40:43]\n"
    "buffer_load_dwordx4 v218, s[52:55], s67 offen offset:0 lds\n"
    "v_mfma_f32_16x16x32_bf16 a[44:47], a[232:235], v[136:139], 0\n"
    "s_waitcnt lgkmcnt(5)\n"
    "s_waitcnt lgkmcnt(2)\n"
    "v_mfma_f32_16x16x32_bf16 a[44:47], a[236:239], v[140:143], a[44:47]\n"
    "v_exp_f32 v188, v188\n"
    "v_mfma_f32_16x16x32_bf16 v[176:179], a[192:195], v[16:19], v[192:195]\n"
    "v_exp_f32 v189, v189\n"
    "v_mfma_f32_16x16x32_bf16 v[176:179], a[196:199], v[20:23], v[176:179]\n"
    "v_exp_f32 v190, v190\n"
    "v_mfma_f32_16x16x32_bf16 v[176:179], a[200:203], v[24:27], v[176:179]\n"
    "v_exp_f32 v191, v191\n"
    "v_mfma_f32_16x16x32_bf16 v[176:179], a[204:207], v[28:31], v[176:179]\n"
    "ds_read_b64_tr_b16 a[232:233], v215 offset:128\n"
    "v_cvt_pk_bf16_f32 v168, v188, v189\n"
    "v_mfma_f32_16x16x32_bf16 v[176:179], a[208:211], v[32:35], v[176:179]\n"
    "ds_read_b64_tr_b16 a[234:235], v215 offset:6272\n"
    "v_cvt_pk_bf16_f32 v169, v190, v191\n"
    "v_mfma_f32_16x16x32_bf16 v[176:179], a[212:215], v[36:39], v[176:179]\n"
    "ds_read_b64_tr_b16 a[236:237], v215 offset:12416\n"
    "v_mfma_f32_16x16x32_bf16 a[48:51], a[216:219], v[112:115], 0\n"
    "ds_read_b64_tr_b16 a[238:239], v215 offset:18560\n"
    "v_mfma_f32_16x16x32_bf16 a[48:51], a[220:223], v[116:119], a[48:51]\n"
    "buffer_load_dwordx4 v219, s[52:55], s67 offen offset:1024 lds\n"
    "v_mfma_f32_16x16x32_bf16 a[52:55], a[216:219], v[120:123], 0\n"
    "buffer_load_dwordx4 v220, s[52:55], s67 offen offset:2048 lds\n"
    "v_mfma_f32_16x16x32_bf16 a[52:55], a[220:223], v[124:127], a[52:55]\n"
    "buffer_load_dwordx4 v218, s[52:55], s67 offen offset:3072 lds\n"
    "v_mfma_f32_16x16x32_bf16 a[56:59], a[216:219], v[128:131], 0\n"
    "s_add_u32 m0, s65, 77824\n"
    "v_mfma_f32_16x16x32_bf16 a[56:59], a[220:223], v[132:135], a[56:59]\n"
    "v_exp_f32 v176, v176\n"
    "v_mfma_f32_16x16x32_bf16 v[180:183], a[192:195], v[40:43], v[196:199]\n"
    "v_exp_f32 v177, v177\n"
    "v_mfma_f32_16x16x32_bf16 v[180:183], a[196:199], v[44:47], v[180:183]\n"
    "v_exp_f32 v178, v178\n"
    "v_mfma_f32_16x16x32_bf16 v[180:183], a[200:203], v[48:51], v[180:183]\n"
    "v_exp_f32 v179, v179\n"
    "v_mfma_f32_16x16x32_bf16 v[180:183], a[204:207], v[52:55], v[180:183]\n"
    "buffer_load_dwordx4 v219, s[56:59], s67 offen offset:0 lds\n"
    "v_mfma_f32_16x16x32_bf16 v[180:183], a[208:211], v[56:59], v[180:183]\n"
    "buffer_load_dwordx4 v220, s[56:59], s67 offen offset:1024 lds\n"
    "v_mfma_f32_16x16x32_bf16 v[180:183], a[212:215], v[60:63], v[180:183]\n"
    "v_cvt_pk_bf16_f32 v146, v176, v177\n"
    "v_cvt_pk_bf16_f32 v147, v178, v179\n"
    "v_mfma_f32_16x16x32_bf16 a[60:63], a[216:219], v[136:139], 0\n"
    "v_mfma_f32_16x16x32_bf16 a[60:63], a[220:223], v[140:143], a[60:63]\n"
    "ds_read_b64_tr_b16 a[216:217], v216 offset:128\n"
    "s_waitcnt lgkmcnt(5)\n"
    "v_mfma_f32_16x16x32_bf16 a[64:67], a[224:227], v[112:115], 0\n"
    "ds_read_b64_tr_b16 a[218:219], v216 offset:6272\n"
    "s_add_u32 s66, s66, 24576\n"
    "v_mfma_f32_16x16x32_bf16 a[64:67], a[228:231], v[116:119], a[64:67]\n"
    "ds_read_b64_tr_b16 a[220:221], v216 offset:12416\n"
    "v_mfma_f32_16x16x32_bf16 a[68:71], a[224:227], v[120:123], 0\n"
    "ds_read_b64_tr_b16 a[222:223], v216 offset:18560\n"
    "v_mfma_f32_16x16x32_bf16 a[68:71], a[228:231], v[124:127], a[68:71]\n"
    "v_exp_f32 v180, v180\n"
    "v_mfma_f32_16x16x32_bf16 v[184:187], a[192:195], v[64:67], v[200:203]\n"
    "v_exp_f32 v181, v181\n"
    "v_mfma_f32_16x16x32_bf16 v[184:187], a[196:199], v[68:71], v[184:187]\n"
    "v_exp_f32 v182, v182\n"
    "v_mfma_f32_16x16x32_bf16 v[184:187], a[200:203], v[72:75], v[184:187]\n"
    "v_exp_f32 v183, v183\n"
    "v_mfma_f32_16x16x32_bf16 v[184:187], a[204:207], v[76:79], v[184:187]\n"
    "s_add_u32 s67, s67, 24576\n"
    "v_cvt_pk_bf16_f32 v154, v180, v181\n"
    "v_mfma_f32_16x16x32_bf16 v[184:187], a[208:211], v[80:83], v[184:187]\n"
    "v_cvt_pk_bf16_f32 v155, v182, v183\n"
    "v_mfma_f32_16x16x32_bf16 v[184:187], a[212:215], v[84:87], v[184:187]\n"
    "buffer_load_dword v15, v9, s[12:15], s3 offen offset:0\n"
    "v_mfma_f32_16x16x32_bf16 a[72:75], a[224:227], v[128:131], 0\n"
    "v_mfma_f32_16x16x32_bf16 a[72:75], a[228:231], v[132:135], a[72:75]\n"
    "v_mfma_f32_16x16x32_bf16 a[76:79], a[224:227], v[136:139], 0\n"
    "v_mfma_f32_16x16x32_bf16 a[76:79], a[228:231], v[140:143], a[76:79]\n"
    "ds_read_b64_tr_b16 a[224:225], v217 offset:128\n"
    "s_waitcnt lgkmcnt(5)\n"
    "v_mfma_f32_16x16x32_bf16 a[80:83], a[232:235], v[112:115], 0\n"
    "ds_read_b64_tr_b16 a[226:227], v217 offset:6272\n"
    "s_add_u32 s3, s3, 49152\n"
    "v_mfma_f32_16x16x32_bf16 a[80:83], a[236:239], v[116:119], a[80:83]\n"
    "v_exp_f32 v184, v184\n"
    "v_mfma_f32_16x16x32_bf16 a[244:247], v[208:211], v[120:123], 0\n"
    "v_exp_f32 v185, v185\n"
    "v_mfma_f32_16x16x32_bf16 a[244:247], v[208:211], v[124:127], a[244:247]\n"
    "v_exp_f32 v186, v186\n"
    "v_mfma_f32_16x16x32_bf16 v[188:191], a[192:195], v[88:91], v[204:207]\n"
    "ds_read_b128 a[192:195], v212 offset:36864\n"
    "v_cvt_pk_bf16_f32 v162, v184, v185\n"
    "v_mfma_f32_16x16x32_bf16 v[188:191], a[196:199], v[92:95], v[188:191]\n"
    "ds_read_b128 a[196:199], v213 offset:36864\n"
    "v_mfma_f32_16x16x32_bf16 v[188:191], a[200:203], v[96:99], v[188:191]\n"
    "ds_read_b128 a[200:203], v212 offset:36992\n"
    "v_mfma_f32_16x16x32_bf16 v[188:191], a[204:207], v[100:103], v[188:191]\n"
    "ds_read_b128 a[204:207], v213 offset:36992\n"
    "v_mfma_f32_16x16x32_bf16 v[188:191], a[208:211], v[104:107], v[188:191]\n"
    "ds_read_b128 a[208:211], v212 offset:37120\n"
    "v_mfma_f32_16x16x32_bf16 v[188:191], a[212:215], v[108:111], v[188:191]\n"
    "ds_read_b128 a[212:215], v213 offset:37120\n"
    "v_mfma_f32_16x16x32_bf16 a[84:87], a[232:235], v[120:123], 0\n"
    "v_exp_f32 v187, v187\n"
    "v_mfma_f32_16x16x32_bf16 a[84:87], a[236:239], v[124:127], a[84:87]\n"
    "ds_read_b64_tr_b16 a[228:229], v217 offset:12416\n"
    "v_cvt_pk_bf16_f32 v163, v186, v187\n"
    "v_mfma_f32_16x16x32_bf16 a[88:91], a[232:235], v[128:131], 0\n"
    "ds_read_b64_tr_b16 a[230:231], v217 offset:18560\n"
    "v_mfma_f32_16x16x32_bf16 a[88:91], a[236:239], v[132:135], a[88:91]\n"
    "v_mfma_f32_16x16x32_bf16 a[92:95], a[232:235], v[136:139], 0\n"
    "s_waitcnt lgkmcnt(5)\n"
    "s_waitcnt lgkmcnt(2)\n"
    "v_mfma_f32_16x16x32_bf16 a[92:95], a[236:239], v[140:143], a[92:95]\n"
    "v_exp_f32 v188, v188\n"
    "v_mfma_f32_16x16x32_bf16 v[176:179], a[192:195], v[16:19], v[192:195]\n"
    "v_exp_f32 v189, v189\n"
    "v_mfma_f32_16x16x32_bf16 v[176:179], a[196:199], v[20:23], v[176:179]\n"
    "v_exp_f32 v190, v190\n"
    "v_mfma_f32_16x16x32_bf16 v[176:179], a[200:203], v[24:27], v[176:179]\n"
    "v_exp_f32 v191, v191\n"
    "v_mfma_f32_16x16x32_bf16 v[176:179], a[204:207], v[28:31], v[176:179]\n"
    "ds_read_b64_tr_b16 a[232:233], v214 offset:256\n"
    "v_cvt_pk_bf16_f32 v170, v188, v189\n"
    "v_mfma_f32_16x16x32_bf16 v[176:179], a[208:211], v[32:35], v[176:179]\n"
    "ds_read_b64_tr_b16 a[234:235], v214 offset:6400\n"
    "v_cvt_pk_bf16_f32 v171, v190, v191\n"
    "v_mfma_f32_16x16x32_bf16 v[176:179], a[212:215], v[36:39], v[176:179]\n"
    "ds_read_b64_tr_b16 a[236:237], v214 offset:12544\n"
    "v_mfma_f32_16x16x32_bf16 a[96:99], a[216:219], v[112:115], 0\n"
    "ds_read_b64_tr_b16 a[238:239], v214 offset:18688\n"
    "v_mfma_f32_16x16x32_bf16 a[96:99], a[220:223], v[116:119], a[96:99]\n"
    "v_mfma_f32_16x16x32_bf16 a[100:103], a[216:219], v[120:123], 0\n"
    "v_mfma_f32_16x16x32_bf16 a[100:103], a[220:223], v[124:127], a[100:103]\n"
    "v_mfma_f32_16x16x32_bf16 a[104:107], a[216:219], v[128:131], 0\n"
    "v_mfma_f32_16x16x32_bf16 a[104:107], a[220:223], v[132:135], a[104:107]\n"
    "v_exp_f32 v176, v176\n"
    "v_mfma_f32_16x16x32_bf16 v[180:183], a[192:195], v[40:43], v[196:199]\n"
    "v_exp_f32 v177, v177\n"
    "v_mfma_f32_16x16x32_bf16 v[180:183], a[196:199], v[44:47], v[180:183]\n"
    "v_exp_f32 v178, v178\n"
    "v_mfma_f32_16x16x32_bf16 v[180:183], a[200:203], v[48:51], v[180:183]\n"
    "v_exp_f32 v179, v179\n"
    "v_mfma_f32_16x16x32_bf16 v[180:183], a[204:207], v[52:55], v[180:183]\n"
    "v_cvt_pk_bf16_f32 v148, v176, v177\n"
    "v_cvt_pk_bf16_f32 v149, v178, v179\n"
    "v_mfma_f32_16x16x32_bf16 v[180:183], a[208:211], v[56:59], v[180:183]\n"
    "v_mfma_f32_16x16x32_bf16 v[180:183], a[212:215], v[60:63], v[180:183]\n"
    "v_mfma_f32_16x16x32_bf16 a[108:111], a[216:219], v[136:139], 0\n"
    "v_mfma_f32_16x16x32_bf16 a[108:111], a[220:223], v[140:143], a[108:111]\n"
    "ds_read_b64_tr_b16 a[216:217], v215 offset:256\n"
    "s_waitcnt lgkmcnt(5)\n"
    "v_mfma_f32_16x16x32_bf16 a[112:115], a[224:227], v[112:115], 0\n"
    "ds_read_b64_tr_b16 a[218:219], v215 offset:6400\n"
    "v_mfma_f32_16x16x32_bf16 a[112:115], a[228:231], v[116:119], a[112:115]\n"
    "ds_read_b64_tr_b16 a[220:221], v215 offset:12544\n"
    "v_mfma_f32_16x16x32_bf16 a[116:119], a[224:227], v[120:123], 0\n"
    "ds_read_b64_tr_b16 a[222:223], v215 offset:18688\n"
    "v_mfma_f32_16x16x32_bf16 a[116:119], a[228:231], v[124:127], a[116:119]\n"
    "v_exp_f32 v180, v180\n"
    "v_mfma_f32_16x16x32_bf16 v[184:187], a[192:195], v[64:67], v[200:203]\n"
    "v_exp_f32 v181, v181\n"
    "v_mfma_f32_16x16x32_bf16 v[184:187], a[196:199], v[68:71], v[184:187]\n"
    "v_exp_f32 v182, v182\n"
    "v_mfma_f32_16x16x32_bf16 v[184:187], a[200:203], v[72:75], v[184:187]\n"
    "v_exp_f32 v183, v183\n"
    "v_mfma_f32_16x16x32_bf16 v[184:187], a[204:207], v[76:79], v[184:187]\n"
    "v_cvt_pk_bf16_f32 v156, v180, v181\n"
    "v_cvt_pk_bf16_f32 v157, v182, v183\n"
    "v_mfma_f32_16x16x32_bf16 v[184:187], a[208:211], v[80:83], v[184:187]\n"
    "v_mfma_f32_16x16x32_bf16 v[184:187], a[212:215], v[84:87], v[184:187]\n"
    "v_mfma_f32_16x16x32_bf16 a[120:123], a[224:227], v[128:131], 0\n"
    "v_mfma_f32_16x16x32_bf16 a[120:123], a[228:231], v[132:135], a[120:123]\n"
    "v_mfma_f32_16x16x32_bf16 a[124:127], a[224:227], v[136:139], 0\n"
    "v_mfma_f32_16x16x32_bf16 a[124:127], a[228:231], v[140:143], a[124:127]\n"
    "ds_read_b64_tr_b16 a[224:225], v216 offset:256\n"
    "s_waitcnt lgkmcnt(5)\n"
    "v_mfma_f32_16x16x32_bf16 a[128:131], a[232:235], v[112:115], 0\n"
    "ds_read_b64_tr_b16 a[226:227], v216 offset:6400\n"
    "v_mfma_f32_16x16x32_bf16 a[128:131], a[236:239], v[116:119], a[128:131]\n"
    "v_exp_f32 v184, v184\n"
    "v_mfma_f32_16x16x32_bf16 a[248:251], v[208:211], v[128:131], 0\n"
    "v_exp_f32 v185, v185\n"
    "v_mfma_f32_16x16x32_bf16 a[248:251], v[208:211], v[132:135], a[248:251]\n"
    "v_exp_f32 v186, v186\n"
    "v_mfma_f32_16x16x32_bf16 v[188:191], a[192:195], v[88:91], v[204:207]\n"
    "ds_read_b128 a[192:195], v212 offset:43008\n"
    "v_cvt_pk_bf16_f32 v164, v184, v185\n"
    "v_mfma_f32_16x16x32_bf16 v[188:191], a[196:199], v[92:95], v[188:191]\n"
    "ds_read_b128 a[196:199], v213 offset:43008\n"
    "v_mfma_f32_16x16x32_bf16 v[188:191], a[200:203], v[96:99], v[188:191]\n"
    "ds_read_b128 a[200:203], v212 offset:43136\n"
    "v_mfma_f32_16x16x32_bf16 v[188:191], a[204:207], v[100:103], v[188:191]\n"
    "ds_read_b128 a[204:207], v213 offset:43136\n"
    "v_mfma_f32_16x16x32_bf16 v[188:191], a[208:211], v[104:107], v[188:191]\n"
    "ds_read_b128 a[208:211], v212 offset:43264\n"
    "v_mfma_f32_16x16x32_bf16 v[188:191], a[212:215], v[108:111], v[188:191]\n"
    "ds_read_b128 a[212:215], v213 offset:43264\n"
    "v_mfma_f32_16x16x32_bf16 a[132:135], a[232:235], v[120:123], 0\n"
    "v_exp_f32 v187, v187\n"
    "v_mfma_f32_16x16x32_bf16 a[132:135], a[236:239], v[124:127], a[132:135]\n"
    "ds_read_b64_tr_b16 a[228:229], v216 offset:12544\n"
    "v_cvt_pk_bf16_f32 v165, v186, v187\n"
    "v_mfma_f32_16x16x32_bf16 a[136:139], a[232:235], v[128:131], 0\n"
    "ds_read_b64_tr_b16 a[230:231], v216 offset:18688\n"
    "v_mfma_f32_16x16x32_bf16 a[136:139], a[236:239], v[132:135], a[136:139]\n"
    "v_mfma_f32_16x16x32_bf16 a[140:143], a[232:235], v[136:139], 0\n"
    "s_waitcnt lgkmcnt(5)\n"
    "s_waitcnt lgkmcnt(2)\n"
    "v_mfma_f32_16x16x32_bf16 a[140:143], a[236:239], v[140:143], a[140:143]\n"
    "v_exp_f32 v188, v188\n"
    "v_mfma_f32_16x16x32_bf16 v[176:179], a[192:195], v[16:19], v[192:195]\n"
    "v_exp_f32 v189, v189\n"
    "v_mfma_f32_16x16x32_bf16 v[176:179], a[196:199], v[20:23], v[176:179]\n"
    "v_exp_f32 v190, v190\n"
    "v_mfma_f32_16x16x32_bf16 v[176:179], a[200:203], v[24:27], v[176:179]\n"
    "v_exp_f32 v191, v191\n"
    "v_mfma_f32_16x16x32_bf16 v[176:179], a[204:207], v[28:31], v[176:179]\n"
    "ds_read_b64_tr_b16 a[232:233], v217 offset:256\n"
    "v_cvt_pk_bf16_f32 v172, v188, v189\n"
    "v_mfma_f32_16x16x32_bf16 v[176:179], a[208:211], v[32:35], v[176:179]\n"
    "ds_read_b64_tr_b16 a[234:235], v217 offset:6400\n"
    "v_cvt_pk_bf16_f32 v173, v190, v191\n"
    "v_mfma_f32_16x16x32_bf16 v[176:179], a[212:215], v[36:39], v[176:179]\n"
    "ds_read_b64_tr_b16 a[236:237], v217 offset:12544\n"
    "v_mfma_f32_16x16x32_bf16 a[144:147], a[216:219], v[112:115], 0\n"
    "ds_read_b64_tr_b16 a[238:239], v217 offset:18688\n"
    "v_mfma_f32_16x16x32_bf16 a[144:147], a[220:223], v[116:119], a[144:147]\n"
    "v_mfma_f32_16x16x32_bf16 a[148:151], a[216:219], v[120:123], 0\n"
    "v_mfma_f32_16x16x32_bf16 a[148:151], a[220:223], v[124:127], a[148:151]\n"
    "v_mfma_f32_16x16x32_bf16 a[152:155], a[216:219], v[128:131], 0\n"
    "v_mfma_f32_16x16x32_bf16 a[152:155], a[220:223], v[132:135], a[152:155]\n"
    "v_exp_f32 v176, v176\n"
    "v_mfma_f32_16x16x32_bf16 v[180:183], a[192:195], v[40:43], v[196:199]\n"
    "v_exp_f32 v177, v177\n"
    "v_mfma_f32_16x16x32_bf16 v[180:183], a[196:199], v[44:47], v[180:183]\n"
    "v_exp_f32 v178, v178\n"
    "v_mfma_f32_16x16x32_bf16 v[180:183], a[200:203], v[48:51], v[180:183]\n"
    "v_exp_f32 v179, v179\n"
    "v_mfma_f32_16x16x32_bf16 v[180:183], a[204:207], v[52:55], v[180:183]\n"
    "v_cvt_pk_bf16_f32 v150, v176, v177\n"
    "v_cvt_pk_bf16_f32 v151, v178, v179\n"
    "v_mfma_f32_16x16x32_bf16 v[180:183], a[208:211], v[56:59], v[180:183]\n"
    "v_mfma_f32_16x16x32_bf16 v[180:183], a[212:215], v[60:63], v[180:183]\n"
    "v_mfma_f32_16x16x32_bf16 a[156:159], a[216:219], v[136:139], 0\n"
    "v_mfma_f32_16x16x32_bf16 a[156:159], a[220:223], v[140:143], a[156:159]\n"
    "s_waitcnt lgkmcnt(4)\n"
    "v_mfma_f32_16x16x32_bf16 a[160:163], a[224:227], v[112:115], 0\n"
    "v_mfma_f32_16x16x32_bf16 a[160:163], a[228:231], v[116:119], a[160:163]\n"
    "v_mfma_f32_16x16x32_bf16 a[164:167], a[224:227], v[120:123], 0\n"
    "v_mfma_f32_16x16x32_bf16 a[164:167], a[228:231], v[124:127], a[164:167]\n"
    "s_nop 0\n"
    "v_exp_f32 v180, v180\n"
    "v_mfma_f32_16x16x32_bf16 v[184:187], a[192:195], v[64:67], v[200:203]\n"
    "v_exp_f32 v181, v181\n"
    "v_mfma_f32_16x16x32_bf16 v[184:187], a[196:199], v[68:71], v[184:187]\n"
    "v_exp_f32 v182, v182\n"
    "v_mfma_f32_16x16x32_bf16 v[184:187], a[200:203], v[72:75], v[184:187]\n"
    "v_exp_f32 v183, v183\n"
    "v_mfma_f32_16x16x32_bf16 v[184:187], a[204:207], v[76:79], v[184:187]\n"
    "v_cvt_pk_bf16_f32 v158, v180, v181\n"
    "v_cvt_pk_bf16_f32 v159, v182, v183\n"
    "v_mfma_f32_16x16x32_bf16 v[184:187], a[208:211], v[80:83], v[184:187]\n"
    "v_mfma_f32_16x16x32_bf16 v[184:187], a[212:215], v[84:87], v[184:187]\n"
    "v_mfma_f32_16x16x32_bf16 a[168:171], a[224:227], v[128:131], 0\n"
    "v_mfma_f32_16x16x32_bf16 a[168:171], a[228:231], v[132:135], a[168:171]\n"
    "v_mfma_f32_16x16x32_bf16 a[172:175], a[224:227], v[136:139], 0\n"
    "v_mfma_f32_16x16x32_bf16 a[172:175], a[228:231], v[140:143], a[172:175]\n"
    "s_waitcnt lgkmcnt(0)\n"
    "v_mfma_f32_16x16x32_bf16 a[176:179], a[232:235], v[112:115], 0\n"
    "v_mfma_f32_16x16x32_bf16 a[176:179], a[236:239], v[116:119], a[176:179]\n"
    "s_nop 0\n"
    "v_exp_f32 v184, v184\n"
    "v_mfma_f32_16x16x32_bf16 a[252:255], v[208:211], v[136:139], 0\n"
    "v_exp_f32 v185, v185\n"
    "v_mfma_f32_16x16x32_bf16 a[252:255], v[208:211], v[140:143], a[252:255]\n"
    "s_waitcnt vmcnt(1)\n"
    "s_barrier\n"
    "v_mfma_f32_16x16x32_bf16 v[188:191], a[192:195], v[88:91], v[204:207]\n"
    "ds_read_b128 a[192:195], v212 offset:0\n"
    "v_mfma_f32_16x16x32_bf16 v[188:191], a[196:199], v[92:95], v[188:191]\n"
    "ds_read_b128 a[196:199], v213 offset:0\n"
    "v_mfma_f32_16x16x32_bf16 v[188:191], a[200:203], v[96:99], v[188:191]\n"
    "ds_read_b128 a[200:203], v212 offset:128\n"
    "v_mfma_f32_16x16x32_bf16 v[188:191], a[204:207], v[100:103], v[188:191]\n"
    "ds_read_b128 a[204:207], v213 offset:128\n"
    "v_mfma_f32_16x16x32_bf16 v[188:191], a[208:211], v[104:107], v[188:191]\n"
    "ds_read_b128 a[208:211], v212 offset:256\n"
    "v_mfma_f32_16x16x32_bf16 v[188:191], a[212:215], v[108:111], v[188:191]\n"
    "ds_read_b128 a[212:215], v213 offset:256\n"
    "v_mfma_f32_16x16x32_bf16 a[180:183], a[232:235], v[120:123], 0\n"
    "v_exp_f32 v186, v186\n"
    "v_mfma_f32_16x16x32_bf16 a[180:183], a[236:239], v[124:127], a[180:183]\n"
    "v_exp_f32 v187, v187\n"
    "v_mfma_f32_16x16x32_bf16 a[184:187], a[232:235], v[128:131], 0\n"
    "ds_read_b64_tr_b16 a[216:217], v214 offset:24576\n"
    "v_cvt_pk_bf16_f32 v166, v184, v185\n"
    "v_mfma_f32_16x16x32_bf16 a[184:187], a[236:239], v[132:135], a[184:187]\n"
    "ds_read_b64_tr_b16 a[218:219], v214 offset:30720\n"
    "v_cvt_pk_bf16_f32 v167, v186, v187\n"
    "v_mfma_f32_16x16x32_bf16 a[188:191], a[232:235], v[136:139], 0\n"
    "ds_read_b64_tr_b16 a[220:221], v214 offset:36864\n"
    "v_mfma_f32_16x16x32_bf16 a[188:191], a[236:239], v[140:143], a[188:191]\n"
    "ds_read_b64_tr_b16 a[222:223], v214 offset:43008\n"
    "s_mov_b32 s68, 14\n"
    "L_aloop:\n"
    "s_waitcnt lgkmcnt(7)\n"
    "ds_read_b64_tr_b16 a[224:225], v215 offset:24576\n"
    "v_mfma_f32_16x16x32_bf16 v[176:179], a[192:195], v[16:19], v[192:195]\n"
    "v_exp_f32 v188, v188\n"
    "v_mfma_f32_16x16x32_bf16 v[176:179], a[196:199], v[20:23], v[176:179]\n"
    "v_exp_f32 v189, v189\n"
    "v_mfma_f32_16x16x32_bf16 v[176:179], a[200:203], v[24:27], v[176:179]\n"
    "v_exp_f32 v190, v190\n"
    "s_waitcnt lgkmcnt(5)\n"
    "v_mfma_f32_16x16x32_bf16 v[176:179], a[204:207], v[28:31], v[176:179]\n"
    "v_exp_f32 v191, v191\n"
    "v_mfma_f32_16x16x32_bf16 v[176:179], a[208:211], v[32:35], v[176:179]\n"
    "ds_read_b64_tr_b16 a[226:227], v215 offset:30720\n"
    "v_cvt_pk_bf16_f32 v174, v188, v189\n"
    "v_mfma_f32_16x16x32_bf16 v[176:179], a[212:215], v[36:39], v[176:179]\n"
    "ds_read_b64_tr_b16 a[228:229], v215 offset:36864\n"
    "s_waitcnt lgkmcnt(3)\n"
    "v_mfma_f32_16x16x32_bf16 a[0:3], a[216:219], v[144:147], a[0:3]\n"
    "ds_read_b64_tr_b16 a[230:231], v215 offset:43008\n"
    "v_cvt_pk_bf16_f32 v175, v190, v191\n"
    "v_mfma_f32_16x16x32_bf16 a[0:3], a[220:223], v[148:151], a[0:3]\n"
    "ds_read_b64_tr_b16 a[232:233], v216 offset:24576\n"
    "s_add_u32 m0, s65, 24576\n"
    "v_mfma_f32_16x16x32_bf16 a[4:7], a[216:219], v[152:155], a[4:7]\n"
    "ds_read_b64_tr_b16 a[234:235], v216 offset:30720\n"
    "v_mfma_f32_16x16x32_bf16 a[4:7], a[220:223], v[156:159], a[4:7]\n"
    "ds_read_b64_tr_b16 a[236:237], v216 offset:36864\n"
    "v_mfma_f32_16x16x32_bf16 a[8:11], a[216:219], v[160:163], a[8:11]\n"
    "ds_read_b64_tr_b16 a[238:239], v216 offset:43008\n"
    "v_mfma_f32_16x16x32_bf16 a[8:11], a[220:223], v[164:167], a[8:11]\n"
    "v_exp_f32 v176, v176\n"
    "v_mfma_f32_16x16x32_bf16 v[180:183], a[192:195], v[40:43], v[196:199]\n"
    "v_exp_f32 v177, v177\n"
    "v_mfma_f32_16x16x32_bf16 v[180:183], a[196:199], v[44:47], v[180:183]\n"
    "v_exp_f32 v178, v178\n"
    "v_mfma_f32_16x16x32_bf16 v[180:183], a[200:203], v[48:51], v[180:183]\n"
    "v_exp_f32 v179, v179\n"
    "v_mfma_f32_16x16x32_bf16 v[180:183], a[204:207], v[52:55], v[180:183]\n"
    "buffer_load_dwordx4 v218, s[44:47], s66 offen offset:0 lds\n"
    "v_mfma_f32_16x16x32_bf16 v[180:183], a[208:211], v[56:59], v[180:183]\n"
    "buffer_load_dwordx4 v219, s[44:47], s66 offen offset:1024 lds\n"
    "v_mfma_f32_16x16x32_bf16 v[180:183], a[212:215], v[60:63], v[180:183]\n"
    "buffer_load_dwordx4 v220, s[44:47], s66 offen offset:2048 lds\n"
    "v_mfma_f32_16x16x32_bf16 a[12:15], a[216:219], v[168:171], a[12:15]\n"
    "buffer_load_dwordx4 v218, s[44:47], s66 offen offset:3072 lds\n"
    "v_mfma_f32_16x16x32_bf16 a[12:15], a[220:223], v[172:175], a[12:15]\n"
    "ds_read_b64_tr_b16 a[216:217], v217 offset:24576\n"
    "s_waitcnt lgkmcnt(5)\n"
    "v_mfma_f32_16x16x32_bf16 a[16:19], a[224:227], v[144:147], a[16:19]\n"
    "ds_read_b64_tr_b16 a[218:219], v217 offset:30720\n"
    "v_cvt_pk_bf16_f32 v112, v176, v177\n"
    "v_mfma_f32_16x16x32_bf16 a[16:19], a[228:231], v[148:151], a[16:19]\n"
    "ds_read_b64_tr_b16 a[220:221], v217 offset:36864\n"
    "v_cvt_pk_bf16_f32 v113, v178, v179\n"
    "v_mfma_f32_16x16x32_bf16 a[20:23], a[224:227], v[152:155], a[20:23]\n"
    "ds_read_b64_tr_b16 a[222:223], v217 offset:43008\n"
    "s_add_u32 m0, s65, 28672\n"
    "v_mfma_f32_16x16x32_bf16 a[20:23], a[228:231], v[156:159], a[20:23]\n"
    "v_exp_f32 v180, v180\n"
    "v_mfma_f32_16x16x32_bf16 v[184:187], a[192:195], v[64:67], v[200:203]\n"
    "v_exp_f32 v181, v181\n"
    "v_mfma_f32_16x16x32_bf16 v[184:187], a[196:199], v[68:71], v[184:187]\n"
    "v_exp_f32 v182, v182\n"
    "v_mfma_f32_16x16x32_bf16 v[184:187], a[200:203], v[72:75], v[184:187]\n"
    "v_exp_f32 v183, v183\n"
    "v_mfma_f32_16x16x32_bf16 v[184:187], a[204:207], v[76:79], v[184:187]\n"
    "buffer_load_dwordx4 v219, s[48:51], s66 offen offset:0 lds\n"
    "v_mfma_f32_16x16x32_bf16 v[184:187], a[208:211], v[80:83], v[184:187]\n"
    "v_cvt_pk_bf16_f32 v120, v180, v181\n"
    "v_cvt_pk_bf16_f32 v121, v182, v183\n"
    "v_mfma_f32_16x16x32_bf16 v[184:187], a[212:215], v[84:87], v[184:187]\n"
    "v_mfma_f32_16x16x32_bf16 a[24:27], a[224:227], v[160:163], a[24:27]\n"
    "buffer_load_dwordx4 v220, s[48:51], s66 offen offset:1024 lds\n"
    "v_mfma_f32_16x16x32_bf16 a[24:27], a[228:231], v[164:167], a[24:27]\n"
    "v_mfma_f32_16x16x32_bf16 a[28:31], a[224:227], v[168:171], a[28:31]\n"
    "v_mfma_f32_16x16x32_bf16 a[28:31], a[228:231], v[172:175], a[28:31]\n"
    "ds_read_b64_tr_b16 a[224:225], v214 offset:24704\n"
    "s_waitcnt lgkmcnt(5)\n"
    "v_mfma_f32_16x16x32_bf16 a[32:35], a[232:235], v[144:147], a[32:35]\n"
    "ds_read_b64_tr_b16 a[226:227], v214 offset:30848\n"
    "v_mfma_f32_16x16x32_bf16 a[32:35], a[236:239], v[148:151], a[32:35]\n"
    "v_exp_f32 v184, v184\n"
    "v_mfma_f32_16x16x32_bf16 a[240:243], v[208:211], v[144:147], a[240:243]\n"
    "v_exp_f32 v185, v185\n"
    "v_mfma_f32_16x16x32_bf16 a[240:243], v[208:211], v[148:151], a[240:243]\n"
    "v_exp_f32 v186, v186\n"
    "v_mfma_f32_16x16x32_bf16 v[188:191], a[192:195], v[88:91], v[204:207]\n"
    "ds_read_b128 a[192:195], v212 offset:6144\n"
    "s_add_u32 m0, s65, 49152\n"
    "v_mfma_f32_16x16x32_bf16 v[188:191], a[196:199], v[92:95], v[188:191]\n"
    "ds_read_b128 a[196:199], v213 offset:6144\n"
    "v_cvt_pk_bf16_f32 v128, v184, v185\n"
    "v_mfma_f32_16x16x32_bf16 v[188:191], a[200:203], v[96:99], v[188:191]\n"
    "ds_read_b128 a[200:203], v212 offset:6272\n"
    "v_mfma_f32_16x16x32_bf16 v[188:191], a[204:207], v[100:103], v[188:191]\n"
    "ds_read_b128 a[204:207], v213 offset:6272\n"
    "v_mfma_f32_16x16x32_bf16 v[188:191], a[208:211], v[104:107], v[188:191]\n"
    "ds_read_b128 a[208:211], v212 offset:6400\n"
    "v_mfma_f32_16x16x32_bf16 v[188:191], a[212:215], v[108:111], v[188:191]\n"
    "ds_read_b128 a[212:215], v213 offset:6400\n"
    "v_mfma_f32_16x16x32_bf16 a[36:39], a[232:235], v[152:155], a[36:39]\n"
    "v_exp_f32 v187, v187\n"
    "v_mfma_f32_16x16x32_bf16 a[36:39], a[236:239], v[156:159], a[36:39]\n"
    "ds_read_b64_tr_b16 a[228:229], v214 offset:36992\n"
    "v_cvt_pk_bf16_f32 v129, v186, v187\n"
    "v_mfma_f32_16x16x32_bf16 a[40:43], a[232:235], v[160:163], a[40:43]\n"
    "ds_read_b64_tr_b16 a[230:231], v214 offset:43136\n"
    "v_mfma_f32_16x16x32_bf16 a[40:43], a[236:239], v[164:167], a[40:43]\n"
    "buffer_load_dwordx4 v218, s[52:55], s67 offen offset:0 lds\n"
    "v_mfma_f32_16x16x32_bf16 a[44:47], a[232:235], v[168:171], a[44:47]\n"
    "s_waitcnt lgkmcnt(5)\n"
    "s_waitcnt lgkmcnt(2)\n"
    "v_mfma_f32_16x16x32_bf16 a[44:47], a[236:239], v[172:175], a[44:47]\n"
    "v_exp_f32 v188, v188\n"
    "v_mfma_f32_16x16x32_bf16 v[176:179], a[192:195], v[16:19], v[192:195]\n"
    "v_exp_f32 v189, v189\n"
    "v_mfma_f32_16x16x32_bf16 v[176:179], a[196:199], v[20:23], v[176:179]\n"
    "v_exp_f32 v190, v190\n"
    "v_mfma_f32_16x16x32_bf16 v[176:179], a[200:203], v[24:27], v[176:179]\n"
    "v_exp_f32 v191, v191\n"
    "v_mfma_f32_16x16x32_bf16 v[176:179], a[204:207], v[28:31], v[176:179]\n"
    "ds_read_b64_tr_b16 a[232:233], v215 offset:24704\n"
    "v_cvt_pk_bf16_f32 v136, v188, v189\n"
    "v_mfma_f32_16x16x32_bf16 v[176:179], a[208:211], v[32:35], v[176:179]\n"
    "ds_read_b64_tr_b16 a[234:235], v215 offset:30848\n"
    "v_cvt_pk_bf16_f32 v137, v190, v191\n"
    "v_mfma_f32_16x16x32_bf16 v[176:179], a[212:215], v[36:39], v[176:179]\n"
    "ds_read_b64_tr_b16 a[236:237], v215 offset:36992\n"
    "v_mfma_f32_16x16x32_bf16 a[48:51], a[216:219], v[144:147], a[48:51]\n"
    "ds_read_b64_tr_b16 a[238:239], v215 offset:43136\n"
    "v_mfma_f32_16x16x32_bf16 a[48:51], a[220:223], v[148:151], a[48:51]\n"
    "buffer_load_dwordx4 v219, s[52:55], s67 offen offset:1024 lds\n"
    "v_mfma_f32_16x16x32_bf16 a[52:55], a[216:219], v[152:155], a[52:55]\n"
    "buffer_load_dwordx4 v220, s[52:55], s67 offen offset:2048 lds\n"
    "v_mfma_f32_16x16x32_bf16 a[52:55], a[220:223], v[156:159], a[52:55]\n"
    "buffer_load_dwordx4 v218, s[52:55], s67 offen offset:3072 lds\n"
    "v_mfma_f32_16x16x32_bf16 a[56:59], a[216:219], v[160:163], a[56:59]\n"
    "s_add_u32 m0, s65, 53248\n"
    "v_mfma_f32_16x16x32_bf16 a[56:59], a[220:223], v[164:167], a[56:59]\n"
    "v_exp_f32 v176, v176\n"
    "v_mfma_f32_16x16x32_bf16 v[180:183], a[192:195], v[40:43], v[196:199]\n"
    "v_exp_f32 v177, v177\n"
    "v_mfma_f32_16x16x32_bf16 v[180:183], a[196:199], v[44:47], v[180:183]\n"
    "v_exp_f32 v178, v178\n"
    "v_mfma_f32_16x16x32_bf16 v[180:183], a[200:203], v[48:51], v[180:183]\n"
    "v_exp_f32 v179, v179\n"
    "v_mfma_f32_16x16x32_bf16 v[180:183], a[204:207], v[52:55], v[180:183]\n"
    "buffer_load_dwordx4 v219, s[56:59], s67 offen offset:0 lds\n"
    "v_mfma_f32_16x16x32_bf16 v[180:183], a[208:211], v[56:59], v[180:183]\n"
    "buffer_load_dwordx4 v220, s[56:59], s67 offen offset:1024 lds\n"
    "v_mfma_f32_16x16x32_bf16 v[180:183], a[212:215], v[60:63], v[180:183]\n"
    "v_cvt_pk_bf16_f32 v114, v176, v177\n"
    "v_cvt_pk_bf16_f32 v115, v178, v179\n"
    "v_mfma_f32_16x16x32_bf16 a[60:63], a[216:219], v[168:171], a[60:63]\n"
    "v_mfma_f32_16x16x32_bf16 a[60:63], a[220:223], v[172:175], a[60:63]\n"
    "ds_read_b64_tr_b16 a[216:217], v216 offset:24704\n"
    "s_waitcnt lgkmcnt(5)\n"
    "v_mfma_f32_16x16x32_bf16 a[64:67], a[224:227], v[144:147], a[64:67]\n"
    "ds_read_b64_tr_b16 a[218:219], v216 offset:30848\n"
    "s_add_u32 s66, s66, 24576\n"
    "v_mfma_f32_16x16x32_bf16 a[64:67], a[228:231], v[148:151], a[64:67]\n"
    "ds_read_b64_tr_b16 a[220:221], v216 offset:36992\n"
    "v_mfma_f32_16x16x32_bf16 a[68:71], a[224:227], v[152:155], a[68:71]\n"
    "ds_read_b64_tr_b16 a[222:223], v216 offset:43136\n"
    "v_mfma_f32_16x16x32_bf16 a[68:71], a[228:231], v[156:159], a[68:71]\n"
    "v_exp_f32 v180, v180\n"
    "v_mfma_f32_16x16x32_bf16 v[184:187], a[192:195], v[64:67], v[200:203]\n"
    "v_exp_f32 v181, v181\n"
    "v_mfma_f32_16x16x32_bf16 v[184:187], a[196:199], v[68:71], v[184:187]\n"
    "v_exp_f32 v182, v182\n"
    "v_mfma_f32_16x16x32_bf16 v[184:187], a[200:203], v[72:75], v[184:187]\n"
    "v_exp_f32 v183, v183\n"
    "v_mfma_f32_16x16x32_bf16 v[184:187], a[204:207], v[76:79], v[184:187]\n"
    "s_add_u32 s67, s67, 24576\n"
    "v_cvt_pk_bf16_f32 v122, v180, v181\n"
    "v_mfma_f32_16x16x32_bf16 v[184:187], a[208:211], v[80:83], v[184:187]\n"
    "v_cvt_pk_bf16_f32 v123, v182, v183\n"
    "v_mfma_f32_16x16x32_bf16 v[184:187], a[212:215], v[84:87], v[184:187]\n"
    "buffer_load_dword v15, v13, s[40:43], s97 offen offset:0\n"
    "v_mfma_f32_16x16x32_bf16 a[72:75], a[224:227], v[160:163], a[72:75]\n"
    "v_mfma_f32_16x16x32_bf16 a[72:75], a[228:231], v[164:167], a[72:75]\n"
    "v_mfma_f32_16x16x32_bf16 a[76:79], a[224:227], v[168:171], a[76:79]\n"
    "v_mfma_f32_16x16x32_bf16 a[76:79], a[228:231], v[172:175], a[76:79]\n"
    "ds_read_b64_tr_b16 a[224:225], v217 offset:24704\n"
    "s_waitcnt lgkmcnt(5)\n"
    "v_mfma_f32_16x16x32_bf16 a[80:83], a[232:235], v[144:147], a[80:83]\n"
    "ds_read_b64_tr_b16 a[226:227], v217 offset:30848\n"
    "s_add_u32 s97, s97, 128\n"
    "v_mfma_f32_16x16x32_bf16 a[80:83], a[236:239], v[148:151], a[80:83]\n"
    "v_exp_f32 v184, v184\n"
    "v_mfma_f32_16x16x32_bf16 a[244:247], v[208:211], v[152:155], a[244:247]\n"
    "v_exp_f32 v185, v185\n"
    "v_mfma_f32_16x16x32_bf16 a[244:247], v[208:211], v[156:159], a[244:247]\n"
    "v_exp_f32 v186, v186\n"
    "v_mfma_f32_16x16x32_bf16 v[188:191], a[192:195], v[88:91], v[204:207]\n"
    "ds_read_b128 a[192:195], v212 offset:12288\n"
    "v_cvt_pk_bf16_f32 v130, v184, v185\n"
    "v_mfma_f32_16x16x32_bf16 v[188:191], a[196:199], v[92:95], v[188:191]\n"
    "ds_read_b128 a[196:199], v213 offset:12288\n"
    "v_mfma_f32_16x16x32_bf16 v[188:191], a[200:203], v[96:99], v[188:191]\n"
    "ds_read_b128 a[200:203], v212 offset:12416\n"
    "v_mfma_f32_16x16x32_bf16 v[188:191], a[204:207], v[100:103], v[188:191]\n"
    "ds_read_b128 a[204:207], v213 offset:12416\n"
    "v_mfma_f32_16x16x32_bf16 v[188:191], a[208:211], v[104:107], v[188:191]\n"
    "ds_read_b128 a[208:211], v212 offset:12544\n"
    "v_mfma_f32_16x16x32_bf16 v[188:191], a[212:215], v[108:111], v[188:191]\n"
    "ds_read_b128 a[212:215], v213 offset:12544\n"
    "v_mfma_f32_16x16x32_bf16 a[84:87], a[232:235], v[152:155], a[84:87]\n"
    "v_exp_f32 v187, v187\n"
    "v_mfma_f32_16x16x32_bf16 a[84:87], a[236:239], v[156:159], a[84:87]\n"
    "ds_read_b64_tr_b16 a[228:229], v217 offset:36992\n"
    "v_cvt_pk_bf16_f32 v131, v186, v187\n"
    "v_mfma_f32_16x16x32_bf16 a[88:91], a[232:235], v[160:163], a[88:91]\n"
    "ds_read_b64_tr_b16 a[230:231], v217 offset:43136\n"
    "v_mfma_f32_16x16x32_bf16 a[88:91], a[236:239], v[164:167], a[88:91]\n"
    "v_mfma_f32_16x16x32_bf16 a[92:95], a[232:235], v[168:171], a[92:95]\n"
    "s_waitcnt lgkmcnt(5)\n"
    "s_waitcnt lgkmcnt(2)\n"
    "v_mfma_f32_16x16x32_bf16 a[92:95], a[236:239], v[172:175], a[92:95]\n"
    "v_exp_f32 v188, v188\n"
    "v_mfma_f32_16x16x32_bf16 v[176:179], a[192:195], v[16:19], v[192:195]\n"
    "v_exp_f32 v189, v189\n"
    "v_mfma_f32_16x16x32_bf16 v[176:179], a[196:199], v[20:23], v[176:179]\n"
    "v_exp_f32 v190, v190\n"
    "v_mfma_f32_16x16x32_bf16 v[176:179], a[200:203], v[24:27], v[176:179]\n"
    "v_exp_f32 v191, v191\n"
    "v_mfma_f32_16x16x32_bf16 v[176:179], a[204:207], v[28:31], v[176:179]\n"
    "ds_read_b64_tr_b16 a[232:233], v214 offset:24832\n"
    "v_cvt_pk_bf16_f32 v138, v188, v189\n"
    "v_mfma_f32_16x16x32_bf16 v[176:179], a[208:211], v[32:35], v[176:179]\n"
    "ds_read_b64_tr_b16 a[234:235], v214 offset:30976\n"
    "v_cvt_pk_bf16_f32 v139, v190, v191\n"
    "v_mfma_f32_16x16x32_bf16 v[176:179], a[212:215], v[36:39], v[176:179]\n"
    "ds_read_b64_tr_b16 a[236:237], v214 offset:37120\n"
    "v_mfma_f32_16x16x32_bf16 a[96:99], a[216:219], v[144:147], a[96:99]\n"
    "ds_read_b64_tr_b16 a[238:239], v214 offset:43264\n"
    "v_mfma_f32_16x16x32_bf16 a[96:99], a[220:223], v[148:151], a[96:99]\n"
    "v_mfma_f32_16x16x32_bf16 a[100:103], a[216:219], v[152:155], a[100:103]\n"
    "v_mfma_f32_16x16x32_bf16 a[100:103], a[220:223], v[156:159], a[100:103]\n"
    "v_mfma_f32_16x16x32_bf16 a[104:107], a[216:219], v[160:163], a[104:107]\n"
    "v_mfma_f32_16x16x32_bf16 a[104:107], a[220:223], v[164:167], a[104:107]\n"
    "v_exp_f32 v176, v176\n"
    "v_mfma_f32_16x16x32_bf16 v[180:183], a[192:195], v[40:43], v[196:199]\n"
    "v_exp_f32 v177, v177\n"
    "v_mfma_f32_16x16x32_bf16 v[180:183], a[196:199], v[44:47], v[180:183]\n"
    "v_exp_f32 v178, v178\n"
    "v_mfma_f32_16x16x32_bf16 v[180:183], a[200:203], v[48:51], v[180:183]\n"
    "v_exp_f32 v179, v179\n"
    "v_mfma_f32_16x16x32_bf16 v[180:183], a[204:207], v[52:55], v[180:183]\n"
    "v_cvt_pk_bf16_f32 v116, v176, v177\n"
    "v_cvt_pk_bf16_f32 v117, v178, v179\n"
    "v_mfma_f32_16x16x32_bf16 v[180:183], a[208:211], v[56:59], v[180:183]\n"
    "v_mfma_f32_16x16x32_bf16 v[180:183], a[212:215], v[60:63], v[180:183]\n"
    "v_mfma_f32_16x16x32_bf16 a[108:111], a[216:219], v[168:171], a[108:111]\n"
    "v_mfma_f32_16x16x32_bf16 a[108:111], a[220:223], v[172:175], a[108:111]\n"
    "ds_read_b64_tr_b16 a[216:217], v215 offset:24832\n"
    "s_waitcnt lgkmcnt(5)\n"
    "v_mfma_f32_16x16x32_bf16 a[112:115], a[224:227], v[144:147], a[112:115]\n"
    "ds_read_b64_tr_b16 a[218:219], v215 offset:30976\n"
    "v_mfma_f32_16x16x32_bf16 a[112:115], a[228:231], v[148:151], a[112:115]\n"
    "ds_read_b64_tr_b16 a[220:221], v215 offset:37120\n"
    "v_mfma_f32_16x16x32_bf16 a[116:119], a[224:227], v[152:155], a[116:119]\n"
    "ds_read_b64_tr_b16 a[222:223], v215 offset:43264\n"
    "v_mfma_f32_16x16x32_bf16 a[116:119], a[228:231], v[156:159], a[116:119]\n"
    "v_exp_f32 v180, v180\n"
    "v_mfma_f32_16x16x32_bf16 v[184:187], a[192:195], v[64:67], v[200:203]\n"
    "v_exp_f32 v181, v181\n"
    "v_mfma_f32_16x16x32_bf16 v[184:187], a[196:199], v[68:71], v[184:187]\n"
    "v_exp_f32 v182, v182\n"
    "v_mfma_f32_16x16x32_bf16 v[184:187], a[200:203], v[72:75], v[184:187]\n"
    "v_exp_f32 v183, v183\n"
    "v_mfma_f32_16x16x32_bf16 v[184:187], a[204:207], v[76:79], v[184:187]\n"
    "v_cvt_pk_bf16_f32 v124, v180, v181\n"
    "v_cvt_pk_bf16_f32 v125, v182, v183\n"
    "v_mfma_f32_16x16x32_bf16 v[184:187], a[208:211], v[80:83], v[184:187]\n"
    "v_mfma_f32_16x16x32_bf16 v[184:187], a[212:215], v[84:87], v[184:187]\n"
    "v_mfma_f32_16x16x32_bf16 a[120:123], a[224:227], v[160:163], a[120:123]\n"
    "v_mfma_f32_16x16x32_bf16 a[120:123], a[228:231], v[164:167], a[120:123]\n"
    "v_mfma_f32_16x16x32_bf16 a[124:127], a[224:227], v[168:171], a[124:127]\n"
    "v_mfma_f32_16x16x32_bf16 a[124:127], a[228:231], v[172:175], a[124:127]\n"
    "ds_read_b64_tr_b16 a[224:225], v216 offset:24832\n"
    "s_waitcnt lgkmcnt(5)\n"
    "v_mfma_f32_16x16x32_bf16 a[128:131], a[232:235], v[144:147], a[128:131]\n"
    "ds_read_b64_tr_b16 a[226:227], v216 offset:30976\n"
    "v_mfma_f32_16x16x32_bf16 a[128:131], a[236:239], v[148:151], a[128:131]\n"
    "v_exp_f32 v184, v184\n"
    "v_mfma_f32_16x16x32_bf16 a[248:251], v[208:211], v[160:163], a[248:251]\n"
    "v_exp_f32 v185, v185\n"
    "v_mfma_f32_16x16x32_bf16 a[248:251], v[208:211], v[164:167], a[248:251]\n"
    "v_exp_f32 v186, v186\n"
    "v_mfma_f32_16x16x32_bf16 v[188:191], a[192:195], v[88:91], v[204:207]\n"
    "ds_read_b128 a[192:195], v212 offset:18432\n"
    "v_cvt_pk_bf16_f32 v132, v184, v185\n"
    "v_mfma_f32_16x16x32_bf16 v[188:191], a[196:199], v[92:95], v[188:191]\n"
    "ds_read_b128 a[196:199], v213 offset:18432\n"
    "v_mfma_f32_16x16x32_bf16 v[188:191], a[200:203], v[96:99], v[188:191]\n"
    "ds_read_b128 a[200:203], v212 offset:18560\n"
    "v_mfma_f32_16x16x32_bf16 v[188:191], a[204:207], v[100:103], v[188:191]\n"
    "ds_read_b128 a[204:207], v213 offset:18560\n"
    "v_mfma_f32_16x16x32_bf16 v[188:191], a[208:211], v[104:107], v[188:191]\n"
    "ds_read_b128 a[208:211], v212 offset:18688\n"
    "v_mfma_f32_16x16x32_bf16 v[188:191], a[212:215], v[108:111], v[188:191]\n"
    "ds_read_b128 a[212:215], v213 offset:18688\n"
    "v_mfma_f32_16x16x32_bf16 a[132:135], a[232:235], v[152:155], a[132:135]\n"
    "v_exp_f32 v187, v187\n"
    "v_mfma_f32_16x16x32_bf16 a[132:135], a[236:239], v[156:159], a[132:135]\n"
    "ds_read_b64_tr_b16 a[228:229], v216 offset:37120\n"
    "v_cvt_pk_bf16_f32 v133, v186, v187\n"
    "v_mfma_f32_16x16x32_bf16 a[136:139], a[232:235], v[160:163], a[136:139]\n"
    "ds_read_b64_tr_b16 a[230:231], v216 offset:43264\n"
    "v_mfma_f32_16x16x32_bf16 a[136:139], a[236:239], v[164:167], a[136:139]\n"
    "v_mfma_f32_16x16x32_bf16 a[140:143], a[232:235], v[168:171], a[140:143]\n"
    "s_waitcnt lgkmcnt(5)\n"
    "s_waitcnt lgkmcnt(2)\n"
    "v_mfma_f32_16x16x32_bf16 a[140:143], a[236:239], v[172:175], a[140:143]\n"
    "v_exp_f32 v188, v188\n"
    "v_mfma_f32_16x16x32_bf16 v[176:179], a[192:195], v[16:19], v[192:195]\n"
    "v_exp_f32 v189, v189\n"
    "v_mfma_f32_16x16x32_bf16 v[176:179], a[196:199], v[20:23], v[176:179]\n"
    "v_exp_f32 v190, v190\n"
    "v_mfma_f32_16x16x32_bf16 v[176:179], a[200:203], v[24:27], v[176:179]\n"
    "v_exp_f32 v191, v191\n"
    "v_mfma_f32_16x16x32_bf16 v[176:179], a[204:207], v[28:31], v[176:179]\n"
    "ds_read_b64_tr_b16 a[232:233], v217 offset:24832\n"
    "v_cvt_pk_bf16_f32 v140, v188, v189\n"
    "v_mfma_f32_16x16x32_bf16 v[176:179], a[208:211], v[32:35], v[176:179]\n"
    "ds_read_b64_tr_b16 a[234:235], v217 offset:30976\n"
    "v_cvt_pk_bf16_f32 v141, v190, v191\n"
    "v_mfma_f32_16x16x32_bf16 v[176:179], a[212:215], v[36:39], v[176:179]\n"
    "ds_read_b64_tr_b16 a[236:237], v217 offset:37120\n"
    "v_mfma_f32_16x16x32_bf16 a[144:147], a[216:219], v[144:147], a[144:147]\n"
    "ds_read_b64_tr_b16 a[238:239], v217 offset:43264\n"
    "v_mfma_f32_16x16x32_bf16 a[144:147], a[220:223], v[148:151], a[144:147]\n"
    "v_mfma_f32_16x16x32_bf16 a[148:151], a[216:219], v[152:155], a[148:151]\n"
    "v_mfma_f32_16x16x32_bf16 a[148:151], a[220:223], v[156:159], a[148:151]\n"
    "v_mfma_f32_16x16x32_bf16 a[152:155], a[216:219], v[160:163], a[152:155]\n"
    "v_mfma_f32_16x16x32_bf16 a[152:155], a[220:223], v[164:167], a[152:155]\n"
    "v_exp_f32 v176, v176\n"
    "v_mfma_f32_16x16x32_bf16 v[180:183], a[192:195], v[40:43], v[196:199]\n"
    "v_exp_f32 v177, v177\n"
    "v_mfma_f32_16x16x32_bf16 v[180:183], a[196:199], v[44:47], v[180:183]\n"
    "v_exp_f32 v178, v178\n"
    "v_mfma_f32_16x16x32_bf16 v[180:183], a[200:203], v[48:51], v[180:183]\n"
    "v_exp_f32 v179, v179\n"
    "v_mfma_f32_16x16x32_bf16 v[180:183], a[204:207], v[52:55], v[180:183]\n"
    "v_cvt_pk_bf16_f32 v118, v176, v177\n"
    "v_cvt_pk_bf16_f32 v119, v178, v179\n"
    "v_mfma_f32_16x16x32_bf16 v[180:183], a[208:211], v[56:59], v[180:183]\n"
    "v_mfma_f32_16x16x32_bf16 v[180:183], a[212:215], v[60:63], v[180:183]\n"
    "v_mfma_f32_16x16x32_bf16 a[156:159], a[216:219], v[168:171], a[156:159]\n"
    "v_mfma_f32_16x16x32_bf16 a[156:159], a[220:223], v[172:175], a[156:159]\n"
    "s_waitcnt lgkmcnt(4)\n"
    "v_mfma_f32_16x16x32_bf16 a[160:163], a[224:227], v[144:147], a[160:163]\n"
    "v_mfma_f32_16x16x32_bf16 a[160:163], a[228:231], v[148:151], a[160:163]\n"
    "v_mfma_f32_16x16x32_bf16 a[164:167], a[224:227], v[152:155], a[164:167]\n"
    "v_mfma_f32_16x16x32_bf16 a[164:167], a[228:231], v[156:159], a[164:167]\n"
    "s_nop 0\n"
    "v_exp_f32 v180, v180\n"
    "v_mfma_f32_16x16x32_bf16 v[184:187], a[192:195], v[64:67], v[200:203]\n"
    "v_exp_f32 v181, v181\n"
    "v_mfma_f32_16x16x32_bf16 v[184:187], a[196:199], v[68:71], v[184:187]\n"
    "v_exp_f32 v182, v182\n"
    "v_mfma_f32_16x16x32_bf16 v[184:187], a[200:203], v[72:75], v[184:187]\n"
    "v_exp_f32 v183, v183\n"
    "v_mfma_f32_16x16x32_bf16 v[184:187], a[204:207], v[76:79], v[184:187]\n"
    "v_cvt_pk_bf16_f32 v126, v180, v181\n"
    "v_cvt_pk_bf16_f32 v127, v182, v183\n"
    "v_mfma_f32_16x16x32_bf16 v[184:187], a[208:211], v[80:83], v[184:187]\n"
    "v_mfma_f32_16x16x32_bf16 v[184:187], a[212:215], v[84:87], v[184:187]\n"
    "v_mfma_f32_16x16x32_bf16 a[168:171], a[224:227], v[160:163], a[168:171]\n"
    "v_mfma_f32_16x16x32_bf16 a[168:171], a[228:231], v[164:167], a[168:171]\n"
    "v_mfma_f32_16x16x32_bf16 a[172:175], a[224:227], v[168:171], a[172:175]\n"
    "v_mfma_f32_16x16x32_bf16 a[172:175], a[228:231], v[172:175], a[172:175]\n"
    "s_waitcnt lgkmcnt(0)\n"
    "v_mfma_f32_16x16x32_bf16 a[176:179], a[232:235], v[144:147], a[176:179]\n"
    "v_mfma_f32_16x16x32_bf16 a[176:179], a[236:239], v[148:151], a[176:179]\n"
    "s_nop 0\n"
    "v_exp_f32 v184, v184\n"
    "v_mfma_f32_16x16x32_bf16 a[252:255], v[208:211], v[168:171], a[252:255]\n"
    "v_exp_f32 v185, v185\n"
    "v_mfma_f32_16x16x32_bf16 a[252:255], v[208:211], v[172:175], a[252:255]\n"
    "s_waitcnt vmcnt(1)\n"
    "s_barrier\n"
    "v_mfma_f32_16x16x32_bf16 v[188:191], a[192:195], v[88:91], v[204:207]\n"
    "ds_read_b128 a[192:195], v212 offset:24576\n"
    "v_mfma_f32_16x16x32_bf16 v[188:191], a[196:199], v[92:95], v[188:191]\n"
    "ds_read_b128 a[196:199], v213 offset:24576\n"
    "v_mfma_f32_16x16x32_bf16 v[188:191], a[200:203], v[96:99], v[188:191]\n"
    "ds_read_b128 a[200:203], v212 offset:24704\n"
    "v_mfma_f32_16x16x32_bf16 v[188:191], a[204:207], v[100:103], v[188:191]\n"
    "ds_read_b128 a[204:207], v213 offset:24704\n"
    "v_mfma_f32_16x16x32_bf16 v[188:191], a[208:211], v[104:107], v[188:191]\n"
    "ds_read_b128 a[208:211], v212 offset:24832\n"
    "v_mfma_f32_16x16x32_bf16 v[188:191], a[212:215], v[108:111], v[188:191]\n"
    "ds_read_b128 a[212:215], v213 offset:24832\n"
    "v_mfma_f32_16x16x32_bf16 a[180:183], a[232:235], v[152:155], a[180:183]\n"
    "v_exp_f32 v186, v186\n"
    "v_mfma_f32_16x16x32_bf16 a[180:183], a[236:239], v[156:159], a[180:183]\n"
    "v_exp_f32 v187, v187\n"
    "v_mfma_f32_16x16x32_bf16 a[184:187], a[232:235], v[160:163], a[184:187]\n"
    "ds_read_b64_tr_b16 a[216:217], v214 offset:0\n"
    "v_cvt_pk_bf16_f32 v134, v184, v185\n"
    "v_mfma_f32_16x16x32_bf16 a[184:187], a[236:239], v[164:167], a[184:187]\n"
    "ds_read_b64_tr_b16 a[218:219], v214 offset:6144\n"
    "v_cvt_pk_bf16_f32 v135, v186, v187\n"
    "v_mfma_f32_16x16x32_bf16 a[188:191], a[232:235], v[168:171], a[188:191]\n"
    "ds_read_b64_tr_b16 a[220:221], v214 offset:12288\n"
    "v_mfma_f32_16x16x32_bf16 a[188:191], a[236:239], v[172:175], a[188:191]\n"
    "ds_read_b64_tr_b16 a[222:223], v214 offset:18432\n"
    "s_waitcnt lgkmcnt(7)\n"
    "ds_read_b64_tr_b16 a[224:225], v215 offset:0\n"
    "v_mfma_f32_16x16x32_bf16 v[176:179], a[192:195], v[16:19], v[192:195]\n"
    "v_exp_f32 v188, v188\n"
    "v_mfma_f32_16x16x32_bf16 v[176:179], a[196:199], v[20:23], v[176:179]\n"
    "v_exp_f32 v189, v189\n"
    "v_mfma_f32_16x16x32_bf16 v[176:179], a[200:203], v[24:27], v[176:179]\n"
    "v_exp_f32 v190, v190\n"
    "s_waitcnt lgkmcnt(5)\n"
    "v_mfma_f32_16x16x32_bf16 v[176:179], a[204:207], v[28:31], v[176:179]\n"
    "v_exp_f32 v191, v191\n"
    "v_mfma_f32_16x16x32_bf16 v[176:179], a[208:211], v[32:35], v[176:179]\n"
    "ds_read_b64_tr_b16 a[226:227], v215 offset:6144\n"
    "v_cvt_pk_bf16_f32 v142, v188, v189\n"
    "v_mfma_f32_16x16x32_bf16 v[176:179], a[212:215], v[36:39], v[176:179]\n"
    "ds_read_b64_tr_b16 a[228:229], v215 offset:12288\n"
    "s_waitcnt lgkmcnt(3)\n"
    "v_mfma_f32_16x16x32_bf16 a[0:3], a[216:219], v[112:115], a[0:3]\n"
    "ds_read_b64_tr_b16 a[230:231], v215 offset:18432\n"
    "v_cvt_pk_bf16_f32 v143, v190, v191\n"
    "v_mfma_f32_16x16x32_bf16 a[0:3], a[220:223], v[116:119], a[0:3]\n"
    "ds_read_b64_tr_b16 a[232:233], v216 offset:0\n"
    "s_add_u32 m0, s65, 0\n"
    "v_mfma_f32_16x16x32_bf16 a[4:7], a[216:219], v[120:123], a[4:7]\n"
    "ds_read_b64_tr_b16 a[234:235], v216 offset:6144\n"
    "v_mfma_f32_16x16x32_bf16 a[4:7], a[220:223], v[124:127], a[4:7]\n"
    "ds_read_b64_tr_b16 a[236:237], v216 offset:12288\n"
    "v_mfma_f32_16x16x32_bf16 a[8:11], a[216:219], v[128:131], a[8:11]\n"
    "ds_read_b64_tr_b16 a[238:239], v216 offset:18432\n"
    "v_mfma_f32_16x16x32_bf16 a[8:11], a[220:223], v[132:135], a[8:11]\n"
    "v_exp_f32 v176, v176\n"
    "v_mfma_f32_16x16x32_bf16 v[180:183], a[192:195], v[40:43], v[196:199]\n"
    "v_exp_f32 v177, v177\n"
    "v_mfma_f32_16x16x32_bf16 v[180:183], a[196:199], v[44:47], v[180:183]\n"
    "v_exp_f32 v178, v178\n"
    "v_mfma_f32_16x16x32_bf16 v[180:183], a[200:203], v[48:51], v[180:183]\n"
    "v_exp_f32 v179, v179\n"
    "v_mfma_f32_16x16x32_bf16 v[180:183], a[204:207], v[52:55], v[180:183]\n"
    "buffer_load_dwordx4 v218, s[44:47], s66 offen offset:0 lds\n"
    "v_mfma_f32_16x16x32_bf16 v[180:183], a[208:211], v[56:59], v[180:183]\n"
    "buffer_load_dwordx4 v219, s[44:47], s66 offen offset:1024 lds\n"
    "v_mfma_f32_16x16x32_bf16 v[180:183], a[212:215], v[60:63], v[180:183]\n"
    "buffer_load_dwordx4 v220, s[44:47], s66 offen offset:2048 lds\n"
    "v_mfma_f32_16x16x32_bf16 a[12:15], a[216:219], v[136:139], a[12:15]\n"
    "buffer_load_dwordx4 v218, s[44:47], s66 offen offset:3072 lds\n"
    "v_mfma_f32_16x16x32_bf16 a[12:15], a[220:223], v[140:143], a[12:15]\n"
    "ds_read_b64_tr_b16 a[216:217], v217 offset:0\n"
    "s_waitcnt lgkmcnt(5)\n"
    "v_mfma_f32_16x16x32_bf16 a[16:19], a[224:227], v[112:115], a[16:19]\n"
    "ds_read_b64_tr_b16 a[218:219], v217 offset:6144\n"
    "s_add_u32 m0, s65, 4096\n"
    "v_mfma_f32_16x16x32_bf16 a[16:19], a[228:231], v[116:119], a[16:19]\n"
    "ds_read_b64_tr_b16 a[220:221], v217 offset:12288\n"
    "v_cvt_pk_bf16_f32 v144, v176, v177\n"
    "v_mfma_f32_16x16x32_bf16 a[20:23], a[224:227], v[120:123], a[20:23]\n"
    "ds_read_b64_tr_b16 a[222:223], v217 offset:18432\n"
    "v_cvt_pk_bf16_f32 v145, v178, v179\n"
    "v_mfma_f32_16x16x32_bf16 a[20:23], a[228:231], v[124:127], a[20:23]\n"
    "v_exp_f32 v180, v180\n"
    "v_mfma_f32_16x16x32_bf16 v[184:187], a[192:195], v[64:67], v[200:203]\n"
    "v_exp_f32 v181, v181\n"
    "v_mfma_f32_16x16x32_bf16 v[184:187], a[196:199], v[68:71], v[184:187]\n"
    "v_exp_f32 v182, v182\n"
    "v_mfma_f32_16x16x32_bf16 v[184:187], a[200:203], v[72:75], v[184:187]\n"
    "v_exp_f32 v183, v183\n"
    "v_mfma_f32_16x16x32_bf16 v[184:187], a[204:207], v[76:79], v[184:187]\n"
    "buffer_load_dwordx4 v219, s[48:51], s66 offen offset:0 lds\n"
    "v_mfma_f32_16x16x32_bf16 v[184:187], a[208:211], v[80:83], v[184:187]\n"
    "buffer_load_dwordx4 v220, s[48:51], s66 offen offset:1024 lds\n"
    "v_mfma_f32_16x16x32_bf16 v[184:187], a[212:215], v[84:87], v[184:187]\n"
    "v_cvt_pk_bf16_f32 v152, v180, v181\n"
    "v_cvt_pk_bf16_f32 v153, v182, v183\n"
    "v_mfma_f32_16x16x32_bf16 a[24:27], a[224:227], v[128:131], a[24:27]\n"
    "v_mfma_f32_16x16x32_bf16 a[24:27], a[228:231], v[132:135], a[24:27]\n"
    "v_mfma_f32_16x16x32_bf16 a[28:31], a[224:227], v[136:139], a[28:31]\n"
    "s_add_u32 m0, s65, 73728\n"
    "v_mfma_f32_16x16x32_bf16 a[28:31], a[228:231], v[140:143], a[28:31]\n"
    "ds_read_b64_tr_b16 a[224:225], v214 offset:128\n"
    "s_waitcnt lgkmcnt(5)\n"
    "v_mfma_f32_16x16x32_bf16 a[32:35], a[232:235], v[112:115], a[32:35]\n"
    "ds_read_b64_tr_b16 a[226:227], v214 offset:6272\n"
    "v_mfma_f32_16x16x32_bf16 a[32:35], a[236:239], v[116:119], a[32:35]\n"
    "v_exp_f32 v184, v184\n"
    "v_mfma_f32_16x16x32_bf16 a[240:243], v[208:211], v[112:115], a[240:243]\n"
    "v_exp_f32 v185, v185\n"
    "v_mfma_f32_16x16x32_bf16 a[240:243], v[208:211], v[116:119], a[240:243]\n"
    "v_exp_f32 v186, v186\n"
    "v_mfma_f32_16x16x32_bf16 v[188:191], a[192:195], v[88:91], v[204:207]\n"
    "ds_read_b128 a[192:195], v212 offset:30720\n"
    "v_cvt_pk_bf16_f32 v160, v184, v185\n"
    "v_mfma_f32_16x16x32_bf16 v[188:191], a[196:199], v[92:95], v[188:191]\n"
    "ds_read_b128 a[196:199], v213 offset:30720\n"
    "v_mfma_f32_16x16x32_bf16 v[188:191], a[200:203], v[96:99], v[188:191]\n"
    "ds_read_b128 a[200:203], v212 offset:30848\n"
    "v_mfma_f32_16x16x32_bf16 v[188:191], a[204:207], v[100:103], v[188:191]\n"
    "ds_read_b128 a[204:207], v213 offset:30848\n"
    "v_mfma_f32_16x16x32_bf16 v[188:191], a[208:211], v[104:107], v[188:191]\n"
    "ds_read_b128 a[208:211], v212 offset:30976\n"
    "v_mfma_f32_16x16x32_bf16 v[188:191], a[212:215], v[108:111], v[188:191]\n"
    "ds_read_b128 a[212:215], v213 offset:30976\n"
    "v_mfma_f32_16x16x32_bf16 a[36:39], a[232:235], v[120:123], a[36:39]\n"
    "v_exp_f32 v187, v187\n"
    "v_mfma_f32_16x16x32_bf16 a[36:39], a[236:239], v[124:127], a[36:39]\n"
    "ds_read_b64_tr_b16 a[228:229], v214 offset:12416\n"
    "v_cvt_pk_bf16_f32 v161, v186, v187\n"
    "v_mfma_f32_16x16x32_bf16 a[40:43], a[232:235], v[128:131], a[40:43]\n"
    "ds_read_b64_tr_b16 a[230:231], v214 offset:18560\n"
    "v_mfma_f32_16x16x32_bf16 a[40:43], a[236:239], v[132:135], a[40:43]\n"
    "buffer_load_dwordx4 v218, s[52:55], s67 offen offset:0 lds\n"
    "v_mfma_f32_16x16x32_bf16 a[44:47], a[232:235], v[136:139], a[44:47]\n"
    "s_waitcnt lgkmcnt(5)\n"
    "s_waitcnt lgkmcnt(2)\n"
    "v_mfma_f32_16x16x32_bf16 a[44:47], a[236:239], v[140:143], a[44:47]\n"
    "v_exp_f32 v188, v188\n"
    "v_mfma_f32_16x16x32_bf16 v[176:179], a[192:195], v[16:19], v[192:195]\n"
    "v_exp_f32 v189, v189\n"
    "v_mfma_f32_16x16x32_bf16 v[176:179], a[196:199], v[20:23], v[176:179]\n"
    "v_exp_f32 v190, v190\n"
    "v_mfma_f32_16x16x32_bf16 v[176:179], a[200:203], v[24:27], v[176:179]\n"
    "v_exp_f32 v191, v191\n"
    "v_mfma_f32_16x16x32_bf16 v[176:179], a[204:207], v[28:31], v[176:179]\n"
    "ds_read_b64_tr_b16 a[232:233], v215 offset:128\n"
    "buffer_load_dwordx4 v219, s[52:55], s67 offen offset:1024 lds\n"
    "v_mfma_f32_16x16x32_bf16 v[176:179], a[208:211], v[32:35], v[176:179]\n"
    "ds_read_b64_tr_b16 a[234:235], v215 offset:6272\n"
    "v_cvt_pk_bf16_f32 v168, v188, v189\n"
    "v_mfma_f32_16x16x32_bf16 v[176:179], a[212:215], v[36:39], v[176:179]\n"
    "ds_read_b64_tr_b16 a[236:237], v215 offset:12416\n"
    "v_mfma_f32_16x16x32_bf16 a[48:51], a[216:219], v[112:115], a[48:51]\n"
    "ds_read_b64_tr_b16 a[238:239], v215 offset:18560\n"
    "v_cvt_pk_bf16_f32 v169, v190, v191\n"
    "v_mfma_f32_16x16x32_bf16 a[48:51], a[220:223], v[116:119], a[48:51]\n"
    "buffer_load_dwordx4 v220, s[52:55], s67 offen offset:2048 lds\n"
    "v_mfma_f32_16x16x32_bf16 a[52:55], a[216:219], v[120:123], a[52:55]\n"
    "buffer_load_dwordx4 v218, s[52:55], s67 offen offset:3072 lds\n"
    "v_mfma_f32_16x16x32_bf16 a[52:55], a[220:223], v[124:127], a[52:55]\n"
    "s_add_u32 m0, s65, 77824\n"
    "v_mfma_f32_16x16x32_bf16 a[56:59], a[216:219], v[128:131], a[56:59]\n"
    "buffer_load_dwordx4 v219, s[56:59], s67 offen offset:0 lds\n"
    "v_mfma_f32_16x16x32_bf16 a[56:59], a[220:223], v[132:135], a[56:59]\n"
    "v_exp_f32 v176, v176\n"
    "v_mfma_f32_16x16x32_bf16 v[180:183], a[192:195], v[40:43], v[196:199]\n"
    "v_exp_f32 v177, v177\n"
    "v_mfma_f32_16x16x32_bf16 v[180:183], a[196:199], v[44:47], v[180:183]\n"
    "v_exp_f32 v178, v178\n"
    "v_mfma_f32_16x16x32_bf16 v[180:183], a[200:203], v[48:51], v[180:183]\n"
    "v_exp_f32 v179, v179\n"
    "v_mfma_f32_16x16x32_bf16 v[180:183], a[204:207], v[52:55], v[180:183]\n"
    "buffer_load_dwordx4 v220, s[56:59], s67 offen offset:1024 lds\n"
    "v_mfma_f32_16x16x32_bf16 v[180:183], a[208:211], v[56:59], v[180:183]\n"
    "s_add_u32 s66, s66, 24576\n"
    "v_cvt_pk_bf16_f32 v146, v176, v177\n"
    "v_mfma_f32_16x16x32_bf16 v[180:183], a[212:215], v[60:63], v[180:183]\n"
    "v_cvt_pk_bf16_f32 v147, v178, v179\n"
    "v_mfma_f32_16x16x32_bf16 a[60:63], a[216:219], v[136:139], a[60:63]\n"
    "s_add_u32 s67, s67, 24576\n"
    "v_mfma_f32_16x16x32_bf16 a[60:63], a[220:223], v[140:143], a[60:63]\n"
    "ds_read_b64_tr_b16 a[216:217], v216 offset:128\n"
    "s_waitcnt lgkmcnt(5)\n"
    "v_mfma_f32_16x16x32_bf16 a[64:67], a[224:227], v[112:115], a[64:67]\n"
    "ds_read_b64_tr_b16 a[218:219], v216 offset:6272\n"
    "v_mfma_f32_16x16x32_bf16 a[64:67], a[228:231], v[116:119], a[64:67]\n"
    "ds_read_b64_tr_b16 a[220:221], v216 offset:12416\n"
    "v_mfma_f32_16x16x32_bf16 a[68:71], a[224:227], v[120:123], a[68:71]\n"
    "ds_read_b64_tr_b16 a[222:223], v216 offset:18560\n"
    "v_mfma_f32_16x16x32_bf16 a[68:71], a[228:231], v[124:127], a[68:71]\n"
    "v_exp_f32 v180, v180\n"
    "v_mfma_f32_16x16x32_bf16 v[184:187], a[192:195], v[64:67], v[200:203]\n"
    "v_exp_f32 v181, v181\n"
    "v_mfma_f32_16x16x32_bf16 v[184:187], a[196:199], v[68:71], v[184:187]\n"
    "v_exp_f32 v182, v182\n"
    "v_mfma_f32_16x16x32_bf16 v[184:187], a[200:203], v[72:75], v[184:187]\n"
    "v_exp_f32 v183, v183\n"
    "v_mfma_f32_16x16x32_bf16 v[184:187], a[204:207], v[76:79], v[184:187]\n"
    "buffer_load_dword v15, v9, s[12:15], s3 offen offset:0\n"
    "v_mfma_f32_16x16x32_bf16 v[184:187], a[208:211], v[80:83], v[184:187]\n"
    "s_add_u32 s3, s3, 49152\n"
    "v_cvt_pk_bf16_f32 v154, v180, v181\n"
    "v_mfma_f32_16x16x32_bf16 v[184:187], a[212:215], v[84:87], v[184:187]\n"
    "v_cvt_pk_bf16_f32 v155, v182, v183\n"
    "v_mfma_f32_16x16x32_bf16 a[72:75], a[224:227], v[128:131], a[72:75]\n"
    "buffer_load_dword v15, v12, s[36:39], s96 offen offset:0\n"
    "v_mfma_f32_16x16x32_bf16 a[72:75], a[228:231], v[132:135], a[72:75]\n"
    "v_mfma_f32_16x16x32_bf16 a[76:79], a[224:227], v[136:139], a[76:79]\n"
    "v_mfma_f32_16x16x32_bf16 a[76:79], a[228:231], v[140:143], a[76:79]\n"
    "ds_read_b64_tr_b16 a[224:225], v217 offset:128\n"
    "s_waitcnt lgkmcnt(5)\n"
    "v_mfma_f32_16x16x32_bf16 a[80:83], a[232:235], v[112:115], a[80:83]\n"
    "ds_read_b64_tr_b16 a[226:227], v217 offset:6272\n"
    "s_add_u32 s96, s96, 512\n"
    "v_mfma_f32_16x16x32_bf16 a[80:83], a[236:239], v[116:119], a[80:83]\n"
    "v_exp_f32 v184, v184\n"
    "v_mfma_f32_16x16x32_bf16 a[244:247], v[208:211], v[120:123], a[244:247]\n"
    "v_exp_f32 v185, v185\n"
    "v_mfma_f32_16x16x32_bf16 a[244:247], v[208:211], v[124:127], a[244:247]\n"
    "v_exp_f32 v186, v186\n"
    "v_mfma_f32_16x16x32_bf16 v[188:191], a[192:195], v[88:91], v[204:207]\n"
    "ds_read_b128 a[192:195], v212 offset:36864\n"
    "v_cvt_pk_bf16_f32 v162, v184, v185\n"
    "v_mfma_f32_16x16x32_bf16 v[188:191], a[196:199], v[92:95], v[188:191]\n"
    "ds_read_b128 a[196:199], v213 offset:36864\n"
    "v_mfma_f32_16x16x32_bf16 v[188:191], a[200:203], v[96:99], v[188:191]\n"
    "ds_read_b128 a[200:203], v212 offset:36992\n"
    "v_mfma_f32_16x16x32_bf16 v[188:191], a[204:207], v[100:103], v[188:191]\n"
    "ds_read_b128 a[204:207], v213 offset:36992\n"
    "v_mfma_f32_16x16x32_bf16 v[188:191], a[208:211], v[104:107], v[188:191]\n"
    "ds_read_b128 a[208:211], v212 offset:37120\n"
    "v_mfma_f32_16x16x32_bf16 v[188:191], a[212:215], v[108:111], v[188:191]\n"
    "ds_read_b128 a[212:215], v213 offset:37120\n"
    "v_mfma_f32_16x16x32_bf16 a[84:87], a[232:235], v[120:123], a[84:87]\n"
    "v_exp_f32 v187, v187\n"
    "v_mfma_f32_16x16x32_bf16 a[84:87], a[236:239], v[124:127], a[84:87]\n"
    "ds_read_b64_tr_b16 a[228:229], v217 offset:12416\n"
    "v_cvt_pk_bf16_f32 v163, v186, v187\n"
    "v_mfma_f32_16x16x32_bf16 a[88:91], a[232:235], v[128:131], a[88:91]\n"
    "ds_read_b64_tr_b16 a[230:231], v217 offset:18560\n"
    "v_mfma_f32_16x16x32_bf16 a[88:91], a[236:239], v[132:135], a[88:91]\n"
    "v_mfma_f32_16x16x32_bf16 a[92:95], a[232:235], v[136:139], a[92:95]\n"
    "s_waitcnt lgkmcnt(5)\n"
    "s_waitcnt lgkmcnt(2)\n"
    "v_mfma_f32_16x16x32_bf16 a[92:95], a[236:239], v[140:143], a[92:95]\n"
    "v_exp_f32 v188, v188\n"
    "v_mfma_f32_16x16x32_bf16 v[176:179], a[192:195], v[16:19], v[192:195]\n"
    "v_exp_f32 v189, v189\n"
    "v_mfma_f32_16x16x32_bf16 v[176:179], a[196:199], v[20:23], v[176:179]\n"
    "v_exp_f32 v190, v190\n"
    "v_mfma_f32_16x16x32_bf16 v[176:179], a[200:203], v[24:27], v[176:179]\n"
    "v_exp_f32 v191, v191\n"
    "v_mfma_f32_16x16x32_bf16 v[176:179], a[204:207], v[28:31], v[176:179]\n"
    "ds_read_b64_tr_b16 a[232:233], v214 offset:256\n"
    "v_cvt_pk_bf16_f32 v170, v188, v189\n"
    "v_mfma_f32_16x16x32_bf16 v[176:179], a[208:211], v[32:35], v[176:179]\n"
    "ds_read_b64_tr_b16 a[234:235], v214 offset:6400\n"
    "v_cvt_pk_bf16_f32 v171, v190, v191\n"
    "v_mfma_f32_16x16x32_bf16 v[176:179], a[212:215], v[36:39], v[176:179]\n"
    "ds_read_b64_tr_b16 a[236:237], v214 offset:12544\n"
    "v_mfma_f32_16x16x32_bf16 a[96:99], a[216:219], v[112:115], a[96:99]\n"
    "ds_read_b64_tr_b16 a[238:239], v214 offset:18688\n"
    "v_mfma_f32_16x16x32_bf16 a[96:99], a[220:223], v[116:119], a[96:99]\n"
    "v_mfma_f32_16x16x32_bf16 a[100:103], a[216:219], v[120:123], a[100:103]\n"
    "v_mfma_f32_16x16x32_bf16 a[100:103], a[220:223], v[124:127], a[100:103]\n"
    "v_mfma_f32_16x16x32_bf16 a[104:107], a[216:219], v[128:131], a[104:107]\n"
    "v_mfma_f32_16x16x32_bf16 a[104:107], a[220:223], v[132:135], a[104:107]\n"
    "v_exp_f32 v176, v176\n"
    "v_mfma_f32_16x16x32_bf16 v[180:183], a[192:195], v[40:43], v[196:199]\n"
    "v_exp_f32 v177, v177\n"
    "v_mfma_f32_16x16x32_bf16 v[180:183], a[196:199], v[44:47], v[180:183]\n"
    "v_exp_f32 v178, v178\n"
    "v_mfma_f32_16x16x32_bf16 v[180:183], a[200:203], v[48:51], v[180:183]\n"
    "v_exp_f32 v179, v179\n"
    "v_mfma_f32_16x16x32_bf16 v[180:183], a[204:207], v[52:55], v[180:183]\n"
    "v_cvt_pk_bf16_f32 v148, v176, v177\n"
    "v_cvt_pk_bf16_f32 v149, v178, v179\n"
    "v_mfma_f32_16x16x32_bf16 v[180:183], a[208:211], v[56:59], v[180:183]\n"
    "v_mfma_f32_16x16x32_bf16 v[180:183], a[212:215], v[60:63], v[180:183]\n"
    "v_mfma_f32_16x16x32_bf16 a[108:111], a[216:219], v[136:139], a[108:111]\n"
    "v_mfma_f32_16x16x32_bf16 a[108:111], a[220:223], v[140:143], a[108:111]\n"
    "ds_read_b64_tr_b16 a[216:217], v215 offset:256\n"
    "s_waitcnt lgkmcnt(5)\n"
    "v_mfma_f32_16x16x32_bf16 a[112:115], a[224:227], v[112:115], a[112:115]\n"
    "ds_read_b64_tr_b16 a[218:219], v215 offset:6400\n"
    "v_mfma_f32_16x16x32_bf16 a[112:115], a[228:231], v[116:119], a[112:115]\n"
    "ds_read_b64_tr_b16 a[220:221], v215 offset:12544\n"
    "v_mfma_f32_16x16x32_bf16 a[116:119], a[224:227], v[120:123], a[116:119]\n"
    "ds_read_b64_tr_b16 a[222:223], v215 offset:18688\n"
    "v_mfma_f32_16x16x32_bf16 a[116:119], a[228:231], v[124:127], a[116:119]\n"
    "v_exp_f32 v180, v180\n"
    "v_mfma_f32_16x16x32_bf16 v[184:187], a[192:195], v[64:67], v[200:203]\n"
    "v_exp_f32 v181, v181\n"
    "v_mfma_f32_16x16x32_bf16 v[184:187], a[196:199], v[68:71], v[184:187]\n"
    "v_exp_f32 v182, v182\n"
    "v_mfma_f32_16x16x32_bf16 v[184:187], a[200:203], v[72:75], v[184:187]\n"
    "v_exp_f32 v183, v183\n"
    "v_mfma_f32_16x16x32_bf16 v[184:187], a[204:207], v[76:79], v[184:187]\n"
    "v_cvt_pk_bf16_f32 v156, v180, v181\n"
    "v_cvt_pk_bf16_f32 v157, v182, v183\n"
    "v_mfma_f32_16x16x32_bf16 v[184:187], a[208:211], v[80:83], v[184:187]\n"
    "v_mfma_f32_16x16x32_bf16 v[184:187], a[212:215], v[84:87], v[184:187]\n"
    "v_mfma_f32_16x16x32_bf16 a[120:123], a[224:227], v[128:131], a[120:123]\n"
    "v_mfma_f32_16x16x32_bf16 a[120:123], a[228:231], v[132:135], a[120:123]\n"
    "v_mfma_f32_16x16x32_bf16 a[124:127], a[224:227], v[136:139], a[124:127]\n"
    "v_mfma_f32_16x16x32_bf16 a[124:127], a[228:231], v[140:143], a[124:127]\n"
    "ds_read_b64_tr_b16 a[224:225], v216 offset:256\n"
    "s_waitcnt lgkmcnt(5)\n"
    "v_mfma_f32_16x16x32_bf16 a[128:131], a[232:235], v[112:115], a[128:131]\n"
    "ds_read_b64_tr_b16 a[226:227], v216 offset:6400\n"
    "v_mfma_f32_16x16x32_bf16 a[128:131], a[236:239], v[116:119], a[128:131]\n"
    "v_exp_f32 v184, v184\n"
    "v_mfma_f32_16x16x32_bf16 a[248:251], v[208:211], v[128:131], a[248:251]\n"
    "v_exp_f32 v185, v185\n"
    "v_mfma_f32_16x16x32_bf16 a[248:251], v[208:211], v[132:135], a[248:251]\n"
    "v_exp_f32 v186, v186\n"
    "v_mfma_f32_16x16x32_bf16 v[188:191], a[192:195], v[88:91], v[204:207]\n"
    "ds_read_b128 a[192:195], v212 offset:43008\n"
    "v_cvt_pk_bf16_f32 v164, v184, v185\n"
    "v_mfma_f32_16x16x32_bf16 v[188:191], a[196:199], v[92:95], v[188:191]\n"
    "ds_read_b128 a[196:199], v213 offset:43008\n"
    "v_mfma_f32_16x16x32_bf16 v[188:191], a[200:203], v[96:99], v[188:191]\n"
    "ds_read_b128 a[200:203], v212 offset:43136\n"
    "v_mfma_f32_16x16x32_bf16 v[188:191], a[204:207], v[100:103], v[188:191]\n"
    "ds_read_b128 a[204:207], v213 offset:43136\n"
    "v_mfma_f32_16x16x32_bf16 v[188:191], a[208:211], v[104:107], v[188:191]\n"
    "ds_read_b128 a[208:211], v212 offset:43264\n"
    "v_mfma_f32_16x16x32_bf16 v[188:191], a[212:215], v[108:111], v[188:191]\n"
    "ds_read_b128 a[212:215], v213 offset:43264\n"
    "v_mfma_f32_16x16x32_bf16 a[132:135], a[232:235], v[120:123], a[132:135]\n"
    "v_exp_f32 v187, v187\n"
    "v_mfma_f32_16x16x32_bf16 a[132:135], a[236:239], v[124:127], a[132:135]\n"
    "ds_read_b64_tr_b16 a[228:229], v216 offset:12544\n"
    "v_cvt_pk_bf16_f32 v165, v186, v187\n"
    "v_mfma_f32_16x16x32_bf16 a[136:139], a[232:235], v[128:131], a[136:139]\n"
    "ds_read_b64_tr_b16 a[230:231], v216 offset:18688\n"
    "v_mfma_f32_16x16x32_bf16 a[136:139], a[236:239], v[132:135], a[136:139]\n"
    "v_mfma_f32_16x16x32_bf16 a[140:143], a[232:235], v[136:139], a[140:143]\n"
    "s_waitcnt lgkmcnt(5)\n"
    "s_waitcnt lgkmcnt(2)\n"
    "v_mfma_f32_16x16x32_bf16 a[140:143], a[236:239], v[140:143], a[140:143]\n"
    "v_exp_f32 v188, v188\n"
    "v_mfma_f32_16x16x32_bf16 v[176:179], a[192:195], v[16:19], v[192:195]\n"
    "v_exp_f32 v189, v189\n"
    "v_mfma_f32_16x16x32_bf16 v[176:179], a[196:199], v[20:23], v[176:179]\n"
    "v_exp_f32 v190, v190\n"
    "v_mfma_f32_16x16x32_bf16 v[176:179], a[200:203], v[24:27], v[176:179]\n"
    "v_exp_f32 v191, v191\n"
    "v_mfma_f32_16x16x32_bf16 v[176:179], a[204:207], v[28:31], v[176:179]\n"
    "ds_read_b64_tr_b16 a[232:233], v217 offset:256\n"
    "v_cvt_pk_bf16_f32 v172, v188, v189\n"
    "v_mfma_f32_16x16x32_bf16 v[176:179], a[208:211], v[32:35], v[176:179]\n"
    "ds_read_b64_tr_b16 a[234:235], v217 offset:6400\n"
    "v_cvt_pk_bf16_f32 v173, v190, v191\n"
    "v_mfma_f32_16x16x32_bf16 v[176:179], a[212:215], v[36:39], v[176:179]\n"
    "ds_read_b64_tr_b16 a[236:237], v217 offset:12544\n"
    "v_mfma_f32_16x16x32_bf16 a[144:147], a[216:219], v[112:115], a[144:147]\n"
    "ds_read_b64_tr_b16 a[238:239], v217 offset:18688\n"
    "v_mfma_f32_16x16x32_bf16 a[144:147], a[220:223], v[116:119], a[144:147]\n"
    "v_mfma_f32_16x16x32_bf16 a[148:151], a[216:219], v[120:123], a[148:151]\n"
    "v_mfma_f32_16x16x32_bf16 a[148:151], a[220:223], v[124:127], a[148:151]\n"
    "v_mfma_f32_16x16x32_bf16 a[152:155], a[216:219], v[128:131], a[152:155]\n"
    "v_mfma_f32_16x16x32_bf16 a[152:155], a[220:223], v[132:135], a[152:155]\n"
    "v_exp_f32 v176, v176\n"
    "v_mfma_f32_16x16x32_bf16 v[180:183], a[192:195], v[40:43], v[196:199]\n"
    "v_exp_f32 v177, v177\n"
    "v_mfma_f32_16x16x32_bf16 v[180:183], a[196:199], v[44:47], v[180:183]\n"
    "v_exp_f32 v178, v178\n"
    "v_mfma_f32_16x16x32_bf16 v[180:183], a[200:203], v[48:51], v[180:183]\n"
    "v_exp_f32 v179, v179\n"
    "v_mfma_f32_16x16x32_bf16 v[180:183], a[204:207], v[52:55], v[180:183]\n"
    "v_cvt_pk_bf16_f32 v150, v176, v177\n"
    "v_cvt_pk_bf16_f32 v151, v178, v179\n"
    "v_mfma_f32_16x16x32_bf16 v[180:183], a[208:211], v[56:59], v[180:183]\n"
    "v_mfma_f32_16x16x32_bf16 v[180:183], a[212:215], v[60:63], v[180:183]\n"
    "v_mfma_f32_16x16x32_bf16 a[156:159], a[216:219], v[136:139], a[156:159]\n"
    "v_mfma_f32_16x16x32_bf16 a[156:159], a[220:223], v[140:143], a[156:159]\n"
    "s_waitcnt lgkmcnt(4)\n"
    "v_mfma_f32_16x16x32_bf16 a[160:163], a[224:227], v[112:115], a[160:163]\n"
    "v_mfma_f32_16x16x32_bf16 a[160:163], a[228:231], v[116:119], a[160:163]\n"
    "v_mfma_f32_16x16x32_bf16 a[164:167], a[224:227], v[120:123], a[164:167]\n"
    "v_mfma_f32_16x16x32_bf16 a[164:167], a[228:231], v[124:127], a[164:167]\n"
    "s_nop 0\n"
    "v_exp_f32 v180, v180\n"
    "v_mfma_f32_16x16x32_bf16 v[184:187], a[192:195], v[64:67], v[200:203]\n"
    "v_exp_f32 v181, v181\n"
    "v_mfma_f32_16x16x32_bf16 v[184:187], a[196:199], v[68:71], v[184:187]\n"
    "v_exp_f32 v182, v182\n"
    "v_mfma_f32_16x16x32_bf16 v[184:187], a[200:203], v[72:75], v[184:187]\n"
    "v_exp_f32 v183, v183\n"
    "v_mfma_f32_16x16x32_bf16 v[184:187], a[204:207], v[76:79], v[184:187]\n"
    "v_cvt_pk_bf16_f32 v158, v180, v181\n"
    "v_cvt_pk_bf16_f32 v159, v182, v183\n"
    "v_mfma_f32_16x16x32_bf16 v[184:187], a[208:211], v[80:83], v[184:187]\n"
    "v_mfma_f32_16x16x32_bf16 v[184:187], a[212:215], v[84:87], v[184:187]\n"
    "v_mfma_f32_16x16x32_bf16 a[168:171], a[224:227], v[128:131], a[168:171]\n"
    "v_mfma_f32_16x16x32_bf16 a[168:171], a[228:231], v[132:135], a[168:171]\n"
    "v_mfma_f32_16x16x32_bf16 a[172:175], a[224:227], v[136:139], a[172:175]\n"
    "v_mfma_f32_16x16x32_bf16 a[172:175], a[228:231], v[140:143], a[172:175]\n"
    "s_waitcnt lgkmcnt(0)\n"
    "v_mfma_f32_16x16x32_bf16 a[176:179], a[232:235], v[112:115], a[176:179]\n"
    "v_mfma_f32_16x16x32_bf16 a[176:179], a[236:239], v[116:119], a[176:179]\n"
    "s_nop 0\n"
    "v_exp_f32 v184, v184\n"
    "v_mfma_f32_16x16x32_bf16 a[252:255], v[208:211], v[136:139], a[252:255]\n"
    "v_exp_f32 v185, v185\n"
    "v_mfma_f32_16x16x32_bf16 a[252:255], v[208:211], v[140:143], a[252:255]\n"
    "s_waitcnt vmcnt(2)\n"
    "s_barrier\n"
    "v_mfma_f32_16x16x32_bf16 v[188:191], a[192:195], v[88:91], v[204:207]\n"
    "ds_read_b128 a[192:195], v212 offset:0\n"
    "v_mfma_f32_16x16x32_bf16 v[188:191], a[196:199], v[92:95], v[188:191]\n"
    "ds_read_b128 a[196:199], v213 offset:0\n"
    "v_mfma_f32_16x16x32_bf16 v[188:191], a[200:203], v[96:99], v[188:191]\n"
    "ds_read_b128 a[200:203], v212 offset:128\n"
    "v_mfma_f32_16x16x32_bf16 v[188:191], a[204:207], v[100:103], v[188:191]\n"
    "ds_read_b128 a[204:207], v213 offset:128\n"
    "v_mfma_f32_16x16x32_bf16 v[188:191], a[208:211], v[104:107], v[188:191]\n"
    "ds_read_b128 a[208:211], v212 offset:256\n"
    "v_mfma_f32_16x16x32_bf16 v[188:191], a[212:215], v[108:111], v[188:191]\n"
    "ds_read_b128 a[212:215], v213 offset:256\n"
    "v_mfma_f32_16x16x32_bf16 a[180:183], a[232:235], v[120:123], a[180:183]\n"
    "v_exp_f32 v186, v186\n"
    "v_mfma_f32_16x16x32_bf16 a[180:183], a[236:239], v[124:127], a[180:183]\n"
    "v_exp_f32 v187, v187\n"
    "v_mfma_f32_16x16x32_bf16 a[184:187], a[232:235], v[128:131], a[184:187]\n"
    "ds_read_b64_tr_b16 a[216:217], v214 offset:24576\n"
    "v_cvt_pk_bf16_f32 v166, v184, v185\n"
    "v_mfma_f32_16x16x32_bf16 a[184:187], a[236:239], v[132:135], a[184:187]\n"
    "ds_read_b64_tr_b16 a[218:219], v214 offset:30720\n"
    "v_cvt_pk_bf16_f32 v167, v186, v187\n"
    "v_mfma_f32_16x16x32_bf16 a[188:191], a[232:235], v[136:139], a[188:191]\n"
    "ds_read_b64_tr_b16 a[220:221], v214 offset:36864\n"
    "v_mfma_f32_16x16x32_bf16 a[188:191], a[236:239], v[140:143], a[188:191]\n"
    "ds_read_b64_tr_b16 a[222:223], v214 offset:43008\n"
    "s_sub_u32 s68, s68, 1\n"
    "s_cbranch_scc0 L_aloop\n"
    "s_add_u32 s20, s75, 4096\n"
    "s_add_u32 s21, s76, 4096\n"
    "s_add_u32 s22, s77, 4096\n"
    "s_add_u32 s23, s78, 4096\n"
    "; iter 31\n"
    "v_exp_f32 v188, v188\n"
    "ds_read_b64_tr_b16 a[224:225], v215 offset:24576\n"
    "ds_read_b64_tr_b16 a[226:227], v215 offset:30720\n"
    "ds_read_b64_tr_b16 a[228:229], v215 offset:36864\n"
    "s_waitcnt lgkmcnt(3)\n"
    "v_mfma_f32_16x16x32_bf16 a[0:3], a[216:219], v[144:147], a[0:3]\n"
    "v_exp_f32 v189, v189\n"
    "v_mfma_f32_16x16x32_bf16 a[0:3], a[220:223], v[148:151], a[0:3]\n"
    "v_exp_f32 v190, v190\n"
    "v_mfma_f32_16x16x32_bf16 a[4:7], a[216:219], v[152:155], a[4:7]\n"
    "v_exp_f32 v191, v191\n"
    "v_mfma_f32_16x16x32_bf16 a[4:7], a[220:223], v[156:159], a[4:7]\n"
    "ds_read_b64_tr_b16 a[230:231], v215 offset:43008\n"
    "v_cvt_pk_bf16_f32 v174, v188, v189\n"
    "v_mfma_f32_16x16x32_bf16 a[8:11], a[216:219], v[160:163], a[8:11]\n"
    "ds_read_b64_tr_b16 a[232:233], v216 offset:24576\n"
    "v_cvt_pk_bf16_f32 v175, v190, v191\n"
    "v_mfma_f32_16x16x32_bf16 a[8:11], a[220:223], v[164:167], a[8:11]\n"
    "ds_read_b64_tr_b16 a[234:235], v216 offset:30720\n"
    "v_mfma_f32_16x16x32_bf16 a[12:15], a[216:219], v[168:171], a[12:15]\n"
    "ds_read_b64_tr_b16 a[236:237], v216 offset:36864\n"
    "v_mfma_f32_16x16x32_bf16 a[12:15], a[220:223], v[172:175], a[12:15]\n"
    "ds_read_b64_tr_b16 a[238:239], v216 offset:43008\n"
    "s_waitcnt lgkmcnt(4)\n"
    "v_mfma_f32_16x16x32_bf16 a[16:19], a[224:227], v[144:147], a[16:19]\n"
    "ds_read_b64_tr_b16 a[216:217], v217 offset:24576\n"
    "v_mfma_f32_16x16x32_bf16 a[16:19], a[228:231], v[148:151], a[16:19]\n"
    "ds_read_b64_tr_b16 a[218:219], v217 offset:30720\n"
    "v_mfma_f32_16x16x32_bf16 a[20:23], a[224:227], v[152:155], a[20:23]\n"
    "ds_read_b64_tr_b16 a[220:221], v217 offset:36864\n"
    "v_mfma_f32_16x16x32_bf16 a[20:23], a[228:231], v[156:159], a[20:23]\n"
    "ds_read_b64_tr_b16 a[222:223], v217 offset:43008\n"
    "v_mfma_f32_16x16x32_bf16 a[240:243], v[208:211], v[144:147], a[240:243]\n"
    "v_mfma_f32_16x16x32_bf16 a[240:243], v[208:211], v[148:151], a[240:243]\n"
    "v_mfma_f32_16x16x32_bf16 a[244:247], v[208:211], v[152:155], a[244:247]\n"
    "v_mfma_f32_16x16x32_bf16 a[244:247], v[208:211], v[156:159], a[244:247]\n"
    "v_mfma_f32_16x16x32_bf16 a[248:251], v[208:211], v[160:163], a[248:251]\n"
    "v_mfma_f32_16x16x32_bf16 a[248:251], v[208:211], v[164:167], a[248:251]\n"
    "v_mfma_f32_16x16x32_bf16 a[252:255], v[208:211], v[168:171], a[252:255]\n"
    "s_nop 2\n"
    "v_accvgpr_read_b32 v252, a240\n"
    "v_mfma_f32_16x16x32_bf16 a[252:255], v[208:211], v[172:175], a[252:255]\n"
    "v_rcp_f32 v252, v252\n"
    "v_mfma_f32_16x16x32_bf16 a[24:27], a[224:227], v[160:163], a[24:27]\n"
    "s_nop 0\n"
    "v_accvgpr_read_b32 v253, a244\n"
    "v_mfma_f32_16x16x32_bf16 a[24:27], a[228:231], v[164:167], a[24:27]\n"
    "v_rcp_f32 v253, v253\n"
    "v_mfma_f32_16x16x32_bf16 a[28:31], a[224:227], v[168:171], a[28:31]\n"
    "v_accvgpr_read_b32 v254, a248\n"
    "v_mfma_f32_16x16x32_bf16 a[28:31], a[228:231], v[172:175], a[28:31]\n"
    "ds_read_b64_tr_b16 a[224:225], v214 offset:24704\n"
    "s_waitcnt lgkmcnt(5)\n"
    "v_mfma_f32_16x16x32_bf16 a[32:35], a[232:235], v[144:147], a[32:35]\n"
    "ds_read_b64_tr_b16 a[226:227], v214 offset:30848\n"
    "v_mfma_f32_16x16x32_bf16 a[32:35], a[236:239], v[148:151], a[32:35]\n"
    "ds_read_b64_tr_b16 a[228:229], v214 offset:36992\n"
    "v_mfma_f32_16x16x32_bf16 a[36:39], a[232:235], v[152:155], a[36:39]\n"
    "ds_read_b64_tr_b16 a[230:231], v214 offset:43136\n"
    "v_mfma_f32_16x16x32_bf16 a[36:39], a[236:239], v[156:159], a[36:39]\n"
    "v_rcp_f32 v254, v254\n"
    "v_mfma_f32_16x16x32_bf16 a[40:43], a[232:235], v[160:163], a[40:43]\n"
    "v_accvgpr_read_b32 v255, a252\n"
    "v_mfma_f32_16x16x32_bf16 a[40:43], a[236:239], v[164:167], a[40:43]\n"
    "v_rcp_f32 v255, v255\n"
    "v_mfma_f32_16x16x32_bf16 a[44:47], a[232:235], v[168:171], a[44:47]\n"
    "v_accvgpr_read_b32 v112, a0\n"
    "v_accvgpr_read_b32 v113, a1\n"
    "v_mfma_f32_16x16x32_bf16 a[44:47], a[236:239], v[172:175], a[44:47]\n"
    "ds_read_b64_tr_b16 a[232:233], v215 offset:24704\n"
    "s_waitcnt lgkmcnt(5)\n"
    "v_mfma_f32_16x16x32_bf16 a[48:51], a[216:219], v[144:147], a[48:51]\n"
    "ds_read_b64_tr_b16 a[234:235], v215 offset:30848\n"
    "v_accvgpr_read_b32 v114, a2\n"
    "v_mfma_f32_16x16x32_bf16 a[48:51], a[220:223], v[148:151], a[48:51]\n"
    "ds_read_b64_tr_b16 a[236:237], v215 offset:36992\n"
    "v_accvgpr_read_b32 v115, a3\n"
    "v_mfma_f32_16x16x32_bf16 a[52:55], a[216:219], v[152:155], a[52:55]\n"
    "ds_read_b64_tr_b16 a[238:239], v215 offset:43136\n"
    "v_accvgpr_read_b32 v116, a16\n"
    "v_mfma_f32_16x16x32_bf16 a[52:55], a[220:223], v[156:159], a[52:55]\n"
    "v_accvgpr_read_b32 v117, a17\n"
    "v_accvgpr_read_b32 v118, a18\n"
    "v_mfma_f32_16x16x32_bf16 a[56:59], a[216:219], v[160:163], a[56:59]\n"
    "v_accvgpr_read_b32 v119, a19\n"
    "v_mul_f32 v112, v252, v112\n"
    "v_mfma_f32_16x16x32_bf16 a[56:59], a[220:223], v[164:167], a[56:59]\n"
    "v_mul_f32 v113, v252, v113\n"
    "v_mul_f32 v114, v252, v114\n"
    "v_mfma_f32_16x16x32_bf16 a[60:63], a[216:219], v[168:171], a[60:63]\n"
    "v_mul_f32 v115, v252, v115\n"
    "v_mul_f32 v116, v252, v116\n"
    "v_mfma_f32_16x16x32_bf16 a[60:63], a[220:223], v[172:175], a[60:63]\n"
    "ds_read_b64_tr_b16 a[216:217], v216 offset:24704\n"
    "s_waitcnt lgkmcnt(5)\n"
    "v_mfma_f32_16x16x32_bf16 a[64:67], a[224:227], v[144:147], a[64:67]\n"
    "ds_read_b64_tr_b16 a[218:219], v216 offset:30848\n"
    "v_mul_f32 v117, v252, v117\n"
    "v_mfma_f32_16x16x32_bf16 a[64:67], a[228:231], v[148:151], a[64:67]\n"
    "ds_read_b64_tr_b16 a[220:221], v216 offset:36992\n"
    "v_mul_f32 v118, v252, v118\n"
    "v_mfma_f32_16x16x32_bf16 a[68:71], a[224:227], v[152:155], a[68:71]\n"
    "ds_read_b64_tr_b16 a[222:223], v216 offset:43136\n"
    "v_mul_f32 v119, v252, v119\n"
    "v_mfma_f32_16x16x32_bf16 a[68:71], a[228:231], v[156:159], a[68:71]\n"
    "v_cvt_pk_bf16_f32 v120, v112, v113\n"
    "v_cvt_pk_bf16_f32 v121, v114, v115\n"
    "v_mfma_f32_16x16x32_bf16 a[72:75], a[224:227], v[160:163], a[72:75]\n"
    "v_cvt_pk_bf16_f32 v122, v116, v117\n"
    "v_cvt_pk_bf16_f32 v123, v118, v119\n"
    "v_mfma_f32_16x16x32_bf16 a[72:75], a[228:231], v[164:167], a[72:75]\n"
    "buffer_store_dwordx4 v[120:123], v226, s[60:63], s75 offen offset:0 sc1\n"
    "v_mfma_f32_16x16x32_bf16 a[76:79], a[224:227], v[168:171], a[76:79]\n"
    "v_accvgpr_read_b32 v124, a4\n"
    "v_accvgpr_read_b32 v125, a5\n"
    "v_mfma_f32_16x16x32_bf16 a[76:79], a[228:231], v[172:175], a[76:79]\n"
    "ds_read_b64_tr_b16 a[224:225], v217 offset:24704\n"
    "s_waitcnt lgkmcnt(5)\n"
    "v_mfma_f32_16x16x32_bf16 a[80:83], a[232:235], v[144:147], a[80:83]\n"
    "ds_read_b64_tr_b16 a[226:227], v217 offset:30848\n"
    "v_accvgpr_read_b32 v126, a6\n"
    "v_mfma_f32_16x16x32_bf16 a[80:83], a[236:239], v[148:151], a[80:83]\n"
    "ds_read_b64_tr_b16 a[228:229], v217 offset:36992\n"
    "v_accvgpr_read_b32 v127, a7\n"
    "v_mfma_f32_16x16x32_bf16 a[84:87], a[232:235], v[152:155], a[84:87]\n"
    "ds_read_b64_tr_b16 a[230:231], v217 offset:43136\n"
    "v_accvgpr_read_b32 v128, a20\n"
    "v_mfma_f32_16x16x32_bf16 a[84:87], a[236:239], v[156:159], a[84:87]\n"
    "v_accvgpr_read_b32 v129, a21\n"
    "v_accvgpr_read_b32 v130, a22\n"
    "v_mfma_f32_16x16x32_bf16 a[88:91], a[232:235], v[160:163], a[88:91]\n"
    "v_accvgpr_read_b32 v131, a23\n"
    "v_mul_f32 v124, v253, v124\n"
    "v_mfma_f32_16x16x32_bf16 a[88:91], a[236:239], v[164:167], a[88:91]\n"
    "v_mul_f32 v125, v253, v125\n"
    "v_mul_f32 v126, v253, v126\n"
    "v_mfma_f32_16x16x32_bf16 a[92:95], a[232:235], v[168:171], a[92:95]\n"
    "v_mul_f32 v127, v253, v127\n"
    "v_mul_f32 v128, v253, v128\n"
    "v_mfma_f32_16x16x32_bf16 a[92:95], a[236:239], v[172:175], a[92:95]\n"
    "ds_read_b64_tr_b16 a[232:233], v214 offset:24832\n"
    "s_waitcnt lgkmcnt(5)\n"
    "v_mfma_f32_16x16x32_bf16 a[96:99], a[216:219], v[144:147], a[96:99]\n"
    "ds_read_b64_tr_b16 a[234:235], v214 offset:30976\n"
    "v_mul_f32 v129, v253, v129\n"
    "v_mfma_f32_16x16x32_bf16 a[96:99], a[220:223], v[148:151], a[96:99]\n"
    "ds_read_b64_tr_b16 a[236:237], v214 offset:37120\n"
    "v_mul_f32 v130, v253, v130\n"
    "v_mfma_f32_16x16x32_bf16 a[100:103], a[216:219], v[152:155], a[100:103]\n"
    "ds_read_b64_tr_b16 a[238:239], v214 offset:43264\n"
    "v_mul_f32 v131, v253, v131\n"
    "v_mfma_f32_16x16x32_bf16 a[100:103], a[220:223], v[156:159], a[100:103]\n"
    "v_cvt_pk_bf16_f32 v132, v124, v125\n"
    "v_cvt_pk_bf16_f32 v133, v126, v127\n"
    "v_mfma_f32_16x16x32_bf16 a[104:107], a[216:219], v[160:163], a[104:107]\n"
    "v_cvt_pk_bf16_f32 v134, v128, v129\n"
    "v_cvt_pk_bf16_f32 v135, v130, v131\n"
    "v_mfma_f32_16x16x32_bf16 a[104:107], a[220:223], v[164:167], a[104:107]\n"
    "buffer_store_dwordx4 v[132:135], v226, s[60:63], s76 offen offset:0 sc1\n"
    "v_mfma_f32_16x16x32_bf16 a[108:111], a[216:219], v[168:171], a[108:111]\n"
    "v_accvgpr_read_b32 v176, a8\n"
    "v_accvgpr_read_b32 v177, a9\n"
    "v_mfma_f32_16x16x32_bf16 a[108:111], a[220:223], v[172:175], a[108:111]\n"
    "ds_read_b64_tr_b16 a[216:217], v215 offset:24832\n"
    "s_waitcnt lgkmcnt(5)\n"
    "v_mfma_f32_16x16x32_bf16 a[112:115], a[224:227], v[144:147], a[112:115]\n"
    "ds_read_b64_tr_b16 a[218:219], v215 offset:30976\n"
    "v_accvgpr_read_b32 v178, a10\n"
    "v_mfma_f32_16x16x32_bf16 a[112:115], a[228:231], v[148:151], a[112:115]\n"
    "ds_read_b64_tr_b16 a[220:221], v215 offset:37120\n"
    "v_accvgpr_read_b32 v179, a11\n"
    "v_mfma_f32_16x16x32_bf16 a[116:119], a[224:227], v[152:155], a[116:119]\n"
    "ds_read_b64_tr_b16 a[222:223], v215 offset:43264\n"
    "v_accvgpr_read_b32 v180, a24\n"
    "v_mfma_f32_16x16x32_bf16 a[116:119], a[228:231], v[156:159], a[116:119]\n"
    "v_accvgpr_read_b32 v181, a25\n"
    "v_accvgpr_read_b32 v182, a26\n"
    "v_mfma_f32_16x16x32_bf16 a[120:123], a[224:227], v[160:163], a[120:123]\n"
    "v_accvgpr_read_b32 v183, a27\n"
    "v_mul_f32 v176, v254, v176\n"
    "v_mfma_f32_16x16x32_bf16 a[120:123], a[228:231], v[164:167], a[120:123]\n"
    "v_mul_f32 v177, v254, v177\n"
    "v_mul_f32 v178, v254, v178\n"
    "v_mfma_f32_16x16x32_bf16 a[124:127], a[224:227], v[168:171], a[124:127]\n"
    "v_mul_f32 v179, v254, v179\n"
    "v_mul_f32 v180, v254, v180\n"
    "v_mfma_f32_16x16x32_bf16 a[124:127], a[228:231], v[172:175], a[124:127]\n"
    "ds_read_b64_tr_b16 a[224:225], v216 offset:24832\n"
    "s_waitcnt lgkmcnt(5)\n"
    "v_mfma_f32_16x16x32_bf16 a[128:131], a[232:235], v[144:147], a[128:131]\n"
    "ds_read_b64_tr_b16 a[226:227], v216 offset:30976\n"
    "v_mul_f32 v181, v254, v181\n"
    "v_mfma_f32_16x16x32_bf16 a[128:131], a[236:239], v[148:151], a[128:131]\n"
    "ds_read_b64_tr_b16 a[228:229], v216 offset:37120\n"
    "v_mul_f32 v182, v254, v182\n"
    "v_mfma_f32_16x16x32_bf16 a[132:135], a[232:235], v[152:155], a[132:135]\n"
    "ds_read_b64_tr_b16 a[230:231], v216 offset:43264\n"
    "v_mul_f32 v183, v254, v183\n"
    "v_mfma_f32_16x16x32_bf16 a[132:135], a[236:239], v[156:159], a[132:135]\n"
    "v_cvt_pk_bf16_f32 v184, v176, v177\n"
    "v_cvt_pk_bf16_f32 v185, v178, v179\n"
    "v_mfma_f32_16x16x32_bf16 a[136:139], a[232:235], v[160:163], a[136:139]\n"
    "v_cvt_pk_bf16_f32 v186, v180, v181\n"
    "v_cvt_pk_bf16_f32 v187, v182, v183\n"
    "v_mfma_f32_16x16x32_bf16 a[136:139], a[236:239], v[164:167], a[136:139]\n"
    "buffer_store_dwordx4 v[184:187], v226, s[60:63], s77 offen offset:0 sc1\n"
    "v_mfma_f32_16x16x32_bf16 a[140:143], a[232:235], v[168:171], a[140:143]\n"
    "v_accvgpr_read_b32 v188, a12\n"
    "v_accvgpr_read_b32 v189, a13\n"
    "v_mfma_f32_16x16x32_bf16 a[140:143], a[236:239], v[172:175], a[140:143]\n"
    "ds_read_b64_tr_b16 a[232:233], v217 offset:24832\n"
    "s_waitcnt lgkmcnt(5)\n"
    "v_mfma_f32_16x16x32_bf16 a[144:147], a[216:219], v[144:147], a[144:147]\n"
    "ds_read_b64_tr_b16 a[234:235], v217 offset:30976\n"
    "v_accvgpr_read_b32 v190, a14\n"
    "v_mfma_f32_16x16x32_bf16 a[144:147], a[220:223], v[148:151], a[144:147]\n"
    "ds_read_b64_tr_b16 a[236:237], v217 offset:37120\n"
    "v_accvgpr_read_b32 v191, a15\n"
    "v_mfma_f32_16x16x32_bf16 a[148:151], a[216:219], v[152:155], a[148:151]\n"
    "ds_read_b64_tr_b16 a[238:239], v217 offset:43264\n"
    "v_accvgpr_read_b32 v192, a28\n"
    "v_mfma_f32_16x16x32_bf16 a[148:151], a[220:223], v[156:159], a[148:151]\n"
    "v_accvgpr_read_b32 v193, a29\n"
    "v_accvgpr_read_b32 v194, a30\n"
    "v_mfma_f32_16x16x32_bf16 a[152:155], a[216:219], v[160:163], a[152:155]\n"
    "v_accvgpr_read_b32 v195, a31\n"
    "v_mul_f32 v188, v255, v188\n"
    "v_mfma_f32_16x16x32_bf16 a[152:155], a[220:223], v[164:167], a[152:155]\n"
    "v_mul_f32 v189, v255, v189\n"
    "v_mul_f32 v190, v255, v190\n"
    "v_mfma_f32_16x16x32_bf16 a[156:159], a[216:219], v[168:171], a[156:159]\n"
    "v_mul_f32 v191, v255, v191\n"
    "v_mul_f32 v192, v255, v192\n"
    "v_mfma_f32_16x16x32_bf16 a[156:159], a[220:223], v[172:175], a[156:159]\n"
    "s_waitcnt lgkmcnt(4)\n"
    "v_mul_f32 v193, v255, v193\n"
    "v_mfma_f32_16x16x32_bf16 a[160:163], a[224:227], v[144:147], a[160:163]\n"
    "v_mul_f32 v194, v255, v194\n"
    "v_mul_f32 v195, v255, v195\n"
    "v_mfma_f32_16x16x32_bf16 a[160:163], a[228:231], v[148:151], a[160:163]\n"
    "v_cvt_pk_bf16_f32 v196, v188, v189\n"
    "v_cvt_pk_bf16_f32 v197, v190, v191\n"
    "v_mfma_f32_16x16x32_bf16 a[164:167], a[224:227], v[152:155], a[164:167]\n"
    "v_cvt_pk_bf16_f32 v198, v192, v193\n"
    "v_cvt_pk_bf16_f32 v199, v194, v195\n"
    "v_mfma_f32_16x16x32_bf16 a[164:167], a[228:231], v[156:159], a[164:167]\n"
    "buffer_store_dwordx4 v[196:199], v226, s[60:63], s78 offen offset:0 sc1\n"
    "v_mfma_f32_16x16x32_bf16 a[168:171], a[224:227], v[160:163], a[168:171]\n"
    "v_accvgpr_read_b32 v112, a32\n"
    "v_accvgpr_read_b32 v113, a33\n"
    "v_mfma_f32_16x16x32_bf16 a[168:171], a[228:231], v[164:167], a[168:171]\n"
    "v_accvgpr_read_b32 v114, a34\n"
    "v_accvgpr_read_b32 v115, a35\n"
    "v_mfma_f32_16x16x32_bf16 a[172:175], a[224:227], v[168:171], a[172:175]\n"
    "v_accvgpr_read_b32 v116, a48\n"
    "v_accvgpr_read_b32 v117, a49\n"
    "v_mfma_f32_16x16x32_bf16 a[172:175], a[228:231], v[172:175], a[172:175]\n"
    "s_waitcnt lgkmcnt(0)\n"
    "v_accvgpr_read_b32 v118, a50\n"
    "v_mfma_f32_16x16x32_bf16 a[176:179], a[232:235], v[144:147], a[176:179]\n"
    "v_accvgpr_read_b32 v119, a51\n"
    "v_mul_f32 v112, v252, v112\n"
    "v_mfma_f32_16x16x32_bf16 a[176:179], a[236:239], v[148:151], a[176:179]\n"
    "v_mul_f32 v113, v252, v113\n"
    "v_mul_f32 v114, v252, v114\n"
    "v_mfma_f32_16x16x32_bf16 a[180:183], a[232:235], v[152:155], a[180:183]\n"
    "v_mul_f32 v115, v252, v115\n"
    "v_mul_f32 v116, v252, v116\n"
    "v_mfma_f32_16x16x32_bf16 a[180:183], a[236:239], v[156:159], a[180:183]\n"
    "v_mul_f32 v117, v252, v117\n"
    "v_mul_f32 v118, v252, v118\n"
    "v_mfma_f32_16x16x32_bf16 a[184:187], a[232:235], v[160:163], a[184:187]\n"
    "v_mul_f32 v119, v252, v119\n"
    "v_cvt_pk_bf16_f32 v120, v112, v113\n"
    "v_mfma_f32_16x16x32_bf16 a[184:187], a[236:239], v[164:167], a[184:187]\n"
    "v_cvt_pk_bf16_f32 v121, v114, v115\n"
    "v_cvt_pk_bf16_f32 v122, v116, v117\n"
    "v_mfma_f32_16x16x32_bf16 a[188:191], a[232:235], v[168:171], a[188:191]\n"
    "v_cvt_pk_bf16_f32 v123, v118, v119\n"
    "v_mfma_f32_16x16x32_bf16 a[188:191], a[236:239], v[172:175], a[188:191]\n"
    "buffer_store_dwordx4 v[120:123], v226, s[60:63], s75 offen offset:1024 sc1\n"
    "v_accvgpr_read_b32 v124, a36\n"
    "v_accvgpr_read_b32 v125, a37\n"
    "v_accvgpr_read_b32 v126, a38\n"
    "v_accvgpr_read_b32 v127, a39\n"
    "v_accvgpr_read_b32 v128, a52\n"
    "v_accvgpr_read_b32 v129, a53\n"
    "v_accvgpr_read_b32 v130, a54\n"
    "v_accvgpr_read_b32 v131, a55\n"
    "v_mul_f32 v124, v253, v124\n"
    "v_mul_f32 v125, v253, v125\n"
    "v_mul_f32 v126, v253, v126\n"
    "v_mul_f32 v127, v253, v127\n"
    "v_mul_f32 v128, v253, v128\n"
    "v_mul_f32 v129, v253, v129\n"
    "v_mul_f32 v130, v253, v130\n"
    "v_mul_f32 v131, v253, v131\n"
    "v_cvt_pk_bf16_f32 v132, v124, v125\n"
    "v_cvt_pk_bf16_f32 v133, v126, v127\n"
    "v_cvt_pk_bf16_f32 v134, v128, v129\n"
    "v_cvt_pk_bf16_f32 v135, v130, v131\n"
    "buffer_store_dwordx4 v[132:135], v226, s[60:63], s76 offen offset:1024 sc1\n"
    "v_accvgpr_read_b32 v176, a40\n"
    "v_accvgpr_read_b32 v177, a41\n"
    "v_accvgpr_read_b32 v178, a42\n"
    "v_accvgpr_read_b32 v179, a43\n"
    "v_accvgpr_read_b32 v180, a56\n"
    "v_accvgpr_read_b32 v181, a57\n"
    "v_accvgpr_read_b32 v182, a58\n"
    "v_accvgpr_read_b32 v183, a59\n"
    "v_mul_f32 v176, v254, v176\n"
    "v_mul_f32 v177, v254, v177\n"
    "v_mul_f32 v178, v254, v178\n"
    "v_mul_f32 v179, v254, v179\n"
    "v_mul_f32 v180, v254, v180\n"
    "v_mul_f32 v181, v254, v181\n"
    "v_mul_f32 v182, v254, v182\n"
    "v_mul_f32 v183, v254, v183\n"
    "v_cvt_pk_bf16_f32 v184, v176, v177\n"
    "v_cvt_pk_bf16_f32 v185, v178, v179\n"
    "v_cvt_pk_bf16_f32 v186, v180, v181\n"
    "v_cvt_pk_bf16_f32 v187, v182, v183\n"
    "buffer_store_dwordx4 v[184:187], v226, s[60:63], s77 offen offset:1024 sc1\n"
    "v_accvgpr_read_b32 v188, a44\n"
    "v_accvgpr_read_b32 v189, a45\n"
    "v_accvgpr_read_b32 v190, a46\n"
    "v_accvgpr_read_b32 v191, a47\n"
    "v_accvgpr_read_b32 v192, a60\n"
    "v_accvgpr_read_b32 v193, a61\n"
    "v_accvgpr_read_b32 v194, a62\n"
    "v_accvgpr_read_b32 v195, a63\n"
    "v_mul_f32 v188, v255, v188\n"
    "v_mul_f32 v189, v255, v189\n"
    "v_mul_f32 v190, v255, v190\n"
    "v_mul_f32 v191, v255, v191\n"
    "v_mul_f32 v192, v255, v192\n"
    "v_mul_f32 v193, v255, v193\n"
    "v_mul_f32 v194, v255, v194\n"
    "v_mul_f32 v195, v255, v195\n"
    "v_cvt_pk_bf16_f32 v196, v188, v189\n"
    "v_cvt_pk_bf16_f32 v197, v190, v191\n"
    "v_cvt_pk_bf16_f32 v198, v192, v193\n"
    "v_cvt_pk_bf16_f32 v199, v194, v195\n"
    "buffer_store_dwordx4 v[196:199], v226, s[60:63], s78 offen offset:1024 sc1\n"
    "v_accvgpr_read_b32 v112, a64\n"
    "v_accvgpr_read_b32 v113, a65\n"
    "v_accvgpr_read_b32 v114, a66\n"
    "v_accvgpr_read_b32 v115, a67\n"
    "v_accvgpr_read_b32 v116, a80\n"
    "v_accvgpr_read_b32 v117, a81\n"
    "v_accvgpr_read_b32 v118, a82\n"
    "v_accvgpr_read_b32 v119, a83\n"
    "v_mul_f32 v112, v252, v112\n"
    "v_mul_f32 v113, v252, v113\n"
    "v_mul_f32 v114, v252, v114\n"
    "v_mul_f32 v115, v252, v115\n"
    "v_mul_f32 v116, v252, v116\n"
    "v_mul_f32 v117, v252, v117\n"
    "v_mul_f32 v118, v252, v118\n"
    "v_mul_f32 v119, v252, v119\n"
    "v_cvt_pk_bf16_f32 v120, v112, v113\n"
    "v_cvt_pk_bf16_f32 v121, v114, v115\n"
    "v_cvt_pk_bf16_f32 v122, v116, v117\n"
    "v_cvt_pk_bf16_f32 v123, v118, v119\n"
    "buffer_store_dwordx4 v[120:123], v226, s[60:63], s75 offen offset:2048 sc1\n"
    "v_accvgpr_read_b32 v124, a68\n"
    "v_accvgpr_read_b32 v125, a69\n"
    "v_accvgpr_read_b32 v126, a70\n"
    "v_accvgpr_read_b32 v127, a71\n"
    "v_accvgpr_read_b32 v128, a84\n"
    "v_accvgpr_read_b32 v129, a85\n"
    "v_accvgpr_read_b32 v130, a86\n"
    "v_accvgpr_read_b32 v131, a87\n"
    "v_mul_f32 v124, v253, v124\n"
    "v_mul_f32 v125, v253, v125\n"
    "v_mul_f32 v126, v253, v126\n"
    "v_mul_f32 v127, v253, v127\n"
    "v_mul_f32 v128, v253, v128\n"
    "v_mul_f32 v129, v253, v129\n"
    "v_mul_f32 v130, v253, v130\n"
    "v_mul_f32 v131, v253, v131\n"
    "v_cvt_pk_bf16_f32 v132, v124, v125\n"
    "v_cvt_pk_bf16_f32 v133, v126, v127\n"
    "v_cvt_pk_bf16_f32 v134, v128, v129\n"
    "v_cvt_pk_bf16_f32 v135, v130, v131\n"
    "buffer_store_dwordx4 v[132:135], v226, s[60:63], s76 offen offset:2048 sc1\n"
    "v_accvgpr_read_b32 v176, a72\n"
    "v_accvgpr_read_b32 v177, a73\n"
    "v_accvgpr_read_b32 v178, a74\n"
    "v_accvgpr_read_b32 v179, a75\n"
    "v_accvgpr_read_b32 v180, a88\n"
    "v_accvgpr_read_b32 v181, a89\n"
    "v_accvgpr_read_b32 v182, a90\n"
    "v_accvgpr_read_b32 v183, a91\n"
    "v_mul_f32 v176, v254, v176\n"
    "v_mul_f32 v177, v254, v177\n"
    "v_mul_f32 v178, v254, v178\n"
    "v_mul_f32 v179, v254, v179\n"
    "v_mul_f32 v180, v254, v180\n"
    "v_mul_f32 v181, v254, v181\n"
    "v_mul_f32 v182, v254, v182\n"
    "v_mul_f32 v183, v254, v183\n"
    "v_cvt_pk_bf16_f32 v184, v176, v177\n"
    "v_cvt_pk_bf16_f32 v185, v178, v179\n"
    "v_cvt_pk_bf16_f32 v186, v180, v181\n"
    "v_cvt_pk_bf16_f32 v187, v182, v183\n"
    "buffer_store_dwordx4 v[184:187], v226, s[60:63], s77 offen offset:2048 sc1\n"
    "v_accvgpr_read_b32 v188, a76\n"
    "v_accvgpr_read_b32 v189, a77\n"
    "v_accvgpr_read_b32 v190, a78\n"
    "v_accvgpr_read_b32 v191, a79\n"
    "v_accvgpr_read_b32 v192, a92\n"
    "v_accvgpr_read_b32 v193, a93\n"
    "v_accvgpr_read_b32 v194, a94\n"
    "v_accvgpr_read_b32 v195, a95\n"
    "v_mul_f32 v188, v255, v188\n"
    "v_mul_f32 v189, v255, v189\n"
    "v_mul_f32 v190, v255, v190\n"
    "v_mul_f32 v191, v255, v191\n"
    "v_mul_f32 v192, v255, v192\n"
    "v_mul_f32 v193, v255, v193\n"
    "v_mul_f32 v194, v255, v194\n"
    "v_mul_f32 v195, v255, v195\n"
    "v_cvt_pk_bf16_f32 v196, v188, v189\n"
    "v_cvt_pk_bf16_f32 v197, v190, v191\n"
    "v_cvt_pk_bf16_f32 v198, v192, v193\n"
    "v_cvt_pk_bf16_f32 v199, v194, v195\n"
    "buffer_store_dwordx4 v[196:199], v226, s[60:63], s78 offen offset:2048 sc1\n"
    "v_accvgpr_read_b32 v112, a96\n"
    "v_accvgpr_read_b32 v113, a97\n"
    "v_accvgpr_read_b32 v114, a98\n"
    "v_accvgpr_read_b32 v115, a99\n"
    "v_accvgpr_read_b32 v116, a112\n"
    "v_accvgpr_read_b32 v117, a113\n"
    "v_accvgpr_read_b32 v118, a114\n"
    "v_accvgpr_read_b32 v119, a115\n"
    "v_mul_f32 v112, v252, v112\n"
    "v_mul_f32 v113, v252, v113\n"
    "v_mul_f32 v114, v252, v114\n"
    "v_mul_f32 v115, v252, v115\n"
    "v_mul_f32 v116, v252, v116\n"
    "v_mul_f32 v117, v252, v117\n"
    "v_mul_f32 v118, v252, v118\n"
    "v_mul_f32 v119, v252, v119\n"
    "v_cvt_pk_bf16_f32 v120, v112, v113\n"
    "v_cvt_pk_bf16_f32 v121, v114, v115\n"
    "v_cvt_pk_bf16_f32 v122, v116, v117\n"
    "v_cvt_pk_bf16_f32 v123, v118, v119\n"
    "buffer_store_dwordx4 v[120:123], v226, s[60:63], s75 offen offset:3072 sc1\n"
    "v_accvgpr_read_b32 v124, a100\n"
    "v_accvgpr_read_b32 v125, a101\n"
    "v_accvgpr_read_b32 v126, a102\n"
    "v_accvgpr_read_b32 v127, a103\n"
    "v_accvgpr_read_b32 v128, a116\n"
    "v_accvgpr_read_b32 v129, a117\n"
    "v_accvgpr_read_b32 v130, a118\n"
    "v_accvgpr_read_b32 v131, a119\n"
    "v_mul_f32 v124, v253, v124\n"
    "v_mul_f32 v125, v253, v125\n"
    "v_mul_f32 v126, v253, v126\n"
    "v_mul_f32 v127, v253, v127\n"
    "v_mul_f32 v128, v253, v128\n"
    "v_mul_f32 v129, v253, v129\n"
    "v_mul_f32 v130, v253, v130\n"
    "v_mul_f32 v131, v253, v131\n"
    "v_cvt_pk_bf16_f32 v132, v124, v125\n"
    "v_cvt_pk_bf16_f32 v133, v126, v127\n"
    "v_cvt_pk_bf16_f32 v134, v128, v129\n"
    "v_cvt_pk_bf16_f32 v135, v130, v131\n"
    "buffer_store_dwordx4 v[132:135], v226, s[60:63], s76 offen offset:3072 sc1\n"
    "v_accvgpr_read_b32 v176, a104\n"
    "v_accvgpr_read_b32 v177, a105\n"
    "v_accvgpr_read_b32 v178, a106\n"
    "v_accvgpr_read_b32 v179, a107\n"
    "v_accvgpr_read_b32 v180, a120\n"
    "v_accvgpr_read_b32 v181, a121\n"
    "v_accvgpr_read_b32 v182, a122\n"
    "v_accvgpr_read_b32 v183, a123\n"
    "v_mul_f32 v176, v254, v176\n"
    "v_mul_f32 v177, v254, v177\n"
    "v_mul_f32 v178, v254, v178\n"
    "v_mul_f32 v179, v254, v179\n"
    "v_mul_f32 v180, v254, v180\n"
    "v_mul_f32 v181, v254, v181\n"
    "v_mul_f32 v182, v254, v182\n"
    "v_mul_f32 v183, v254, v183\n"
    "v_cvt_pk_bf16_f32 v184, v176, v177\n"
    "v_cvt_pk_bf16_f32 v185, v178, v179\n"
    "v_cvt_pk_bf16_f32 v186, v180, v181\n"
    "v_cvt_pk_bf16_f32 v187, v182, v183\n"
    "buffer_store_dwordx4 v[184:187], v226, s[60:63], s77 offen offset:3072 sc1\n"
    "v_accvgpr_read_b32 v188, a108\n"
    "v_accvgpr_read_b32 v189, a109\n"
    "v_accvgpr_read_b32 v190, a110\n"
    "v_accvgpr_read_b32 v191, a111\n"
    "v_accvgpr_read_b32 v192, a124\n"
    "v_accvgpr_read_b32 v193, a125\n"
    "v_accvgpr_read_b32 v194, a126\n"
    "v_accvgpr_read_b32 v195, a127\n"
    "v_mul_f32 v188, v255, v188\n"
    "v_mul_f32 v189, v255, v189\n"
    "v_mul_f32 v190, v255, v190\n"
    "v_mul_f32 v191, v255, v191\n"
    "v_mul_f32 v192, v255, v192\n"
    "v_mul_f32 v193, v255, v193\n"
    "v_mul_f32 v194, v255, v194\n"
    "v_mul_f32 v195, v255, v195\n"
    "v_cvt_pk_bf16_f32 v196, v188, v189\n"
    "v_cvt_pk_bf16_f32 v197, v190, v191\n"
    "v_cvt_pk_bf16_f32 v198, v192, v193\n"
    "v_cvt_pk_bf16_f32 v199, v194, v195\n"
    "buffer_store_dwordx4 v[196:199], v226, s[60:63], s78 offen offset:3072 sc1\n"
    "; l check\n"
    "s_nop 7\n"
    "s_mov_b64 s[94:95], exec\n"
    "s_mov_b32 s91, 0x53800000\n"
    "v_accvgpr_read_b32 v228, a240\n"
    "v_accvgpr_read_b32 v229, a244\n"
    "v_accvgpr_read_b32 v230, a248\n"
    "v_accvgpr_read_b32 v231, a252\n"
    "v_cmp_gt_f32 vcc, s91, v228\n"
    "s_nop 1\n"
    "s_and_b64 s[94:95], s[94:95], vcc\n"
    "v_cmp_gt_f32 vcc, s91, v229\n"
    "s_nop 1\n"
    "s_and_b64 s[94:95], s[94:95], vcc\n"
    "v_cmp_gt_f32 vcc, s91, v230\n"
    "s_nop 1\n"
    "s_and_b64 s[94:95], s[94:95], vcc\n"
    "v_cmp_gt_f32 vcc, s91, v231\n"
    "s_nop 1\n"
    "s_and_b64 s[94:95], s[94:95], vcc\n"
    "s_cmp_eq_u64 s[94:95], exec\n"
    "s_cselect_b32 s91, 0, 1\n"
    "s_lshl_b32 s92, s64, 2\n"
    "s_add_u32 s92, s92, 155648\n"
    "v_mov_b32 v232, s91\n"
    "v_mov_b32 v233, s92\n"
    "ds_write_b32 v233, v232 offset:0\n"
    "; O epilogue\n"
    "v_accvgpr_read_b32 v112, a128\n"
    "v_accvgpr_read_b32 v113, a129\n"
    "v_accvgpr_read_b32 v114, a130\n"
    "v_accvgpr_read_b32 v115, a131\n"
    "v_accvgpr_read_b32 v116, a144\n"
    "v_accvgpr_read_b32 v117, a145\n"
    "v_accvgpr_read_b32 v118, a146\n"
    "v_accvgpr_read_b32 v119, a147\n"
    "v_pk_mul_f32 v[112:113], v[112:113], v[252:253] op_sel:[0,0] op_sel_hi:[1,0]\n"
    "v_pk_mul_f32 v[114:115], v[114:115], v[252:253] op_sel:[0,0] op_sel_hi:[1,0]\n"
    "v_pk_mul_f32 v[116:117], v[116:117], v[252:253] op_sel:[0,0] op_sel_hi:[1,0]\n"
    "v_pk_mul_f32 v[118:119], v[118:119], v[252:253] op_sel:[0,0] op_sel_hi:[1,0]\n"
    "v_cvt_pk_bf16_f32 v120, v112, v113\n"
    "v_cvt_pk_bf16_f32 v121, v114, v115\n"
    "v_cvt_pk_bf16_f32 v122, v116, v117\n"
    "v_cvt_pk_bf16_f32 v123, v118, v119\n"
    "buffer_store_dwordx4 v[120:123], v226, s[60:63], s20 offen offset:0 sc1\n"
    "v_accvgpr_read_b32 v124, a132\n"
    "v_accvgpr_read_b32 v125, a133\n"
    "v_accvgpr_read_b32 v126, a134\n"
    "v_accvgpr_read_b32 v127, a135\n"
    "v_accvgpr_read_b32 v128, a148\n"
    "v_accvgpr_read_b32 v129, a149\n"
    "v_accvgpr_read_b32 v130, a150\n"
    "v_accvgpr_read_b32 v131, a151\n"
    "v_pk_mul_f32 v[124:125], v[124:125], v[252:253] op_sel:[0,1] op_sel_hi:[1,1]\n"
    "v_pk_mul_f32 v[126:127], v[126:127], v[252:253] op_sel:[0,1] op_sel_hi:[1,1]\n"
    "v_pk_mul_f32 v[128:129], v[128:129], v[252:253] op_sel:[0,1] op_sel_hi:[1,1]\n"
    "v_pk_mul_f32 v[130:131], v[130:131], v[252:253] op_sel:[0,1] op_sel_hi:[1,1]\n"
    "v_cvt_pk_bf16_f32 v132, v124, v125\n"
    "v_cvt_pk_bf16_f32 v133, v126, v127\n"
    "v_cvt_pk_bf16_f32 v134, v128, v129\n"
    "v_cvt_pk_bf16_f32 v135, v130, v131\n"
    "buffer_store_dwordx4 v[132:135], v226, s[60:63], s21 offen offset:0 sc1\n"
    "v_accvgpr_read_b32 v176, a136\n"
    "v_accvgpr_read_b32 v177, a137\n"
    "v_accvgpr_read_b32 v178, a138\n"
    "v_accvgpr_read_b32 v179, a139\n"
    "v_accvgpr_read_b32 v180, a152\n"
    "v_accvgpr_read_b32 v181, a153\n"
    "v_accvgpr_read_b32 v182, a154\n"
    "v_accvgpr_read_b32 v183, a155\n"
    "v_pk_mul_f32 v[176:177], v[176:177], v[254:255] op_sel:[0,0] op_sel_hi:[1,0]\n"
    "v_pk_mul_f32 v[178:179], v[178:179], v[254:255] op_sel:[0,0] op_sel_hi:[1,0]\n"
    "v_pk_mul_f32 v[180:181], v[180:181], v[254:255] op_sel:[0,0] op_sel_hi:[1,0]\n"
    "v_pk_mul_f32 v[182:183], v[182:183], v[254:255] op_sel:[0,0] op_sel_hi:[1,0]\n"
    "v_cvt_pk_bf16_f32 v184, v176, v177\n"
    "v_cvt_pk_bf16_f32 v185, v178, v179\n"
    "v_cvt_pk_bf16_f32 v186, v180, v181\n"
    "v_cvt_pk_bf16_f32 v187, v182, v183\n"
    "buffer_store_dwordx4 v[184:187], v226, s[60:63], s22 offen offset:0 sc1\n"
    "v_accvgpr_read_b32 v188, a140\n"
    "v_accvgpr_read_b32 v189, a141\n"
    "v_accvgpr_read_b32 v190, a142\n"
    "v_accvgpr_read_b32 v191, a143\n"
    "v_accvgpr_read_b32 v192, a156\n"
    "v_accvgpr_read_b32 v193, a157\n"
    "v_accvgpr_read_b32 v194, a158\n"
    "v_accvgpr_read_b32 v195, a159\n"
    "v_pk_mul_f32 v[188:189], v[188:189], v[254:255] op_sel:[0,1] op_sel_hi:[1,1]\n"
    "v_pk_mul_f32 v[190:191], v[190:191], v[254:255] op_sel:[0,1] op_sel_hi:[1,1]\n"
    "v_pk_mul_f32 v[192:193], v[192:193], v[254:255] op_sel:[0,1] op_sel_hi:[1,1]\n"
    "v_pk_mul_f32 v[194:195], v[194:195], v[254:255] op_sel:[0,1] op_sel_hi:[1,1]\n"
    "v_cvt_pk_bf16_f32 v196, v188, v189\n"
    "v_cvt_pk_bf16_f32 v197, v190, v191\n"
    "v_cvt_pk_bf16_f32 v198, v192, v193\n"
    "v_cvt_pk_bf16_f32 v199, v194, v195\n"
    "buffer_store_dwordx4 v[196:199], v226, s[60:63], s23 offen offset:0 sc1\n"
    "v_accvgpr_read_b32 v112, a160\n"
    "v_accvgpr_read_b32 v113, a161\n"
    "v_accvgpr_read_b32 v114, a162\n"
    "v_accvgpr_read_b32 v115, a163\n"
    "v_accvgpr_read_b32 v116, a176\n"
    "v_accvgpr_read_b32 v117, a177\n"
    "v_accvgpr_read_b32 v118, a178\n"
    "v_accvgpr_read_b32 v119, a179\n"
    "v_pk_mul_f32 v[112:113], v[112:113], v[252:253] op_sel:[0,0] op_sel_hi:[1,0]\n"
    "v_pk_mul_f32 v[114:115], v[114:115], v[252:253] op_sel:[0,0] op_sel_hi:[1,0]\n"
    "v_pk_mul_f32 v[116:117], v[116:117], v[252:253] op_sel:[0,0] op_sel_hi:[1,0]\n"
    "v_pk_mul_f32 v[118:119], v[118:119], v[252:253] op_sel:[0,0] op_sel_hi:[1,0]\n"
    "v_cvt_pk_bf16_f32 v120, v112, v113\n"
    "v_cvt_pk_bf16_f32 v121, v114, v115\n"
    "v_cvt_pk_bf16_f32 v122, v116, v117\n"
    "v_cvt_pk_bf16_f32 v123, v118, v119\n"
    "buffer_store_dwordx4 v[120:123], v226, s[60:63], s20 offen offset:1024 sc1\n"
    "v_accvgpr_read_b32 v124, a164\n"
    "v_accvgpr_read_b32 v125, a165\n"
    "v_accvgpr_read_b32 v126, a166\n"
    "v_accvgpr_read_b32 v127, a167\n"
    "v_accvgpr_read_b32 v128, a180\n"
    "v_accvgpr_read_b32 v129, a181\n"
    "v_accvgpr_read_b32 v130, a182\n"
    "v_accvgpr_read_b32 v131, a183\n"
    "v_pk_mul_f32 v[124:125], v[124:125], v[252:253] op_sel:[0,1] op_sel_hi:[1,1]\n"
    "v_pk_mul_f32 v[126:127], v[126:127], v[252:253] op_sel:[0,1] op_sel_hi:[1,1]\n"
    "v_pk_mul_f32 v[128:129], v[128:129], v[252:253] op_sel:[0,1] op_sel_hi:[1,1]\n"
    "v_pk_mul_f32 v[130:131], v[130:131], v[252:253] op_sel:[0,1] op_sel_hi:[1,1]\n"
    "v_cvt_pk_bf16_f32 v132, v124, v125\n"
    "v_cvt_pk_bf16_f32 v133, v126, v127\n"
    "v_cvt_pk_bf16_f32 v134, v128, v129\n"
    "v_cvt_pk_bf16_f32 v135, v130, v131\n"
    "buffer_store_dwordx4 v[132:135], v226, s[60:63], s21 offen offset:1024 sc1\n"
    "v_accvgpr_read_b32 v176, a168\n"
    "v_accvgpr_read_b32 v177, a169\n"
    "v_accvgpr_read_b32 v178, a170\n"
    "v_accvgpr_read_b32 v179, a171\n"
    "v_accvgpr_read_b32 v180, a184\n"
    "v_accvgpr_read_b32 v181, a185\n"
    "v_accvgpr_read_b32 v182, a186\n"
    "v_accvgpr_read_b32 v183, a187\n"
    "v_pk_mul_f32 v[176:177], v[176:177], v[254:255] op_sel:[0,0] op_sel_hi:[1,0]\n"
    "v_pk_mul_f32 v[178:179], v[178:179], v[254:255] op_sel:[0,0] op_sel_hi:[1,0]\n"
    "v_pk_mul_f32 v[180:181], v[180:181], v[254:255] op_sel:[0,0] op_sel_hi:[1,0]\n"
    "v_pk_mul_f32 v[182:183], v[182:183], v[254:255] op_sel:[0,0] op_sel_hi:[1,0]\n"
    "v_cvt_pk_bf16_f32 v184, v176, v177\n"
    "v_cvt_pk_bf16_f32 v185, v178, v179\n"
    "v_cvt_pk_bf16_f32 v186, v180, v181\n"
    "v_cvt_pk_bf16_f32 v187, v182, v183\n"
    "buffer_store_dwordx4 v[184:187], v226, s[60:63], s22 offen offset:1024 sc1\n"
    "v_accvgpr_read_b32 v188, a172\n"
    "v_accvgpr_read_b32 v189, a173\n"
    "v_accvgpr_read_b32 v190, a174\n"
    "v_accvgpr_read_b32 v191, a175\n"
    "v_accvgpr_read_b32 v192, a188\n"
    "v_accvgpr_read_b32 v193, a189\n"
    "v_accvgpr_read_b32 v194, a190\n"
    "v_accvgpr_read_b32 v195, a191\n"
    "v_pk_mul_f32 v[188:189], v[188:189], v[254:255] op_sel:[0,1] op_sel_hi:[1,1]\n"
    "v_pk_mul_f32 v[190:191], v[190:191], v[254:255] op_sel:[0,1] op_sel_hi:[1,1]\n"
    "v_pk_mul_f32 v[192:193], v[192:193], v[254:255] op_sel:[0,1] op_sel_hi:[1,1]\n"
    "v_pk_mul_f32 v[194:195], v[194:195], v[254:255] op_sel:[0,1] op_sel_hi:[1,1]\n"
    "v_cvt_pk_bf16_f32 v196, v188, v189\n"
    "v_cvt_pk_bf16_f32 v197, v190, v191\n"
    "v_cvt_pk_bf16_f32 v198, v192, v193\n"
    "v_cvt_pk_bf16_f32 v199, v194, v195\n"
    "buffer_store_dwordx4 v[196:199], v226, s[60:63], s23 offen offset:1024 sc1\n"
    "; fallback\n"
    "s_waitcnt lgkmcnt(0)\n"
    "s_barrier\n"
    "v_mov_b32 v233, 155648\n"
    "ds_read_b128 v[228:231], v233 offset:0\n"
    "s_waitcnt lgkmcnt(0)\n"
    "v_or3_b32 v228, v228, v229, v230\n"
    "v_or_b32 v228, v228, v231\n"
    "s_nop 0\n"
    "v_readfirstlane_b32 s91, v228\n"
    "s_nop 3\n"
    "s_andn2_b32 s91, s91, s98\n"
    "s_cmp_eq_u32 s91, 0\n"
    "s_cbranch_scc1 L_done\n"
    "s_mov_b32 s98, 1\n"
    "v_mov_b32 v228, 0xff800000\n"
    "v_mov_b32 v229, 0xff800000\n"
    "v_mov_b32 v230, 0xff800000\n"
    "v_mov_b32 v231, 0xff800000\n"
    "s_mov_b32 s93, s65\n"
    "s_mov_b32 s68, 31\n"
    "L_fb_loop:\n"
    "s_waitcnt vmcnt(0) lgkmcnt(0)\n"
    "s_barrier\n"
    "s_add_u32 m0, s65, 0\n"
    "s_nop 0\n"
    "buffer_load_dwordx4 v218, s[44:47], s93 offen offset:0 lds\n"
    "buffer_load_dwordx4 v219, s[44:47], s93 offen offset:1024 lds\n"
    "buffer_load_dwordx4 v220, s[44:47], s93 offen offset:2048 lds\n"
    "buffer_load_dwordx4 v218, s[44:47], s93 offen offset:3072 lds\n"
    "s_add_u32 m0, s65, 4096\n"
    "s_nop 0\n"
    "buffer_load_dwordx4 v219, s[48:51], s93 offen offset:0 lds\n"
    "buffer_load_dwordx4 v220, s[48:51], s93 offen offset:1024 lds\n"
    "s_add_u32 s93, s93, 24576\n"
    "s_waitcnt vmcnt(0)\n"
    "s_barrier\n"
    "ds_read_b128 a[192:195], v212 offset:0\n"
    "ds_read_b128 a[196:199], v213 offset:0\n"
    "ds_read_b128 a[200:203], v212 offset:128\n"
    "ds_read_b128 a[204:207], v213 offset:128\n"
    "ds_read_b128 a[208:211], v212 offset:256\n"
    "ds_read_b128 a[212:215], v213 offset:256\n"
    "s_waitcnt lgkmcnt(0)\n"
    "v_mfma_f32_16x16x32_bf16 v[176:179], a[192:195], v[16:19], 0\n"
    "v_mfma_f32_16x16x32_bf16 v[176:179], a[196:199], v[20:23], v[176:179]\n"
    "v_mfma_f32_16x16x32_bf16 v[176:179], a[200:203], v[24:27], v[176:179]\n"
    "v_mfma_f32_16x16x32_bf16 v[176:179], a[204:207], v[28:31], v[176:179]\n"
    "v_mfma_f32_16x16x32_bf16 v[176:179], a[208:211], v[32:35], v[176:179]\n"
    "v_mfma_f32_16x16x32_bf16 v[176:179], a[212:215], v[36:39], v[176:179]\n"
    "v_mfma_f32_16x16x32_bf16 v[180:183], a[192:195], v[40:43], 0\n"
    "v_mfma_f32_16x16x32_bf16 v[180:183], a[196:199], v[44:47], v[180:183]\n"
    "v_mfma_f32_16x16x32_bf16 v[180:183], a[200:203], v[48:51], v[180:183]\n"
    "v_mfma_f32_16x16x32_bf16 v[180:183], a[204:207], v[52:55], v[180:183]\n"
    "v_mfma_f32_16x16x32_bf16 v[180:183], a[208:211], v[56:59], v[180:183]\n"
    "v_mfma_f32_16x16x32_bf16 v[180:183], a[212:215], v[60:63], v[180:183]\n"
    "v_mfma_f32_16x16x32_bf16 v[184:187], a[192:195], v[64:67], 0\n"
    "v_mfma_f32_16x16x32_bf16 v[184:187], a[196:199], v[68:71], v[184:187]\n"
    "v_mfma_f32_16x16x32_bf16 v[184:187], a[200:203], v[72:75], v[184:187]\n"
    "v_mfma_f32_16x16x32_bf16 v[184:187], a[204:207], v[76:79], v[184:187]\n"
    "v_mfma_f32_16x16x32_bf16 v[184:187], a[208:211], v[80:83], v[184:187]\n"
    "v_mfma_f32_16x16x32_bf16 v[184:187], a[212:215], v[84:87], v[184:187]\n"
    "v_mfma_f32_16x16x32_bf16 v[188:191], a[192:195], v[88:91], 0\n"
    "v_mfma_f32_16x16x32_bf16 v[188:191], a[196:199], v[92:95], v[188:191]\n"
    "v_mfma_f32_16x16x32_bf16 v[188:191], a[200:203], v[96:99], v[188:191]\n"
    "v_mfma_f32_16x16x32_bf16 v[188:191], a[204:207], v[100:103], v[188:191]\n"
    "v_mfma_f32_16x16x32_bf16 v[188:191], a[208:211], v[104:107], v[188:191]\n"
    "v_mfma_f32_16x16x32_bf16 v[188:191], a[212:215], v[108:111], v[188:191]\n"
    "s_nop 7\n"
    "v_max3_f32 v228, v228, v176, v177\n"
    "v_max3_f32 v228, v228, v178, v179\n"
    "v_max3_f32 v229, v229, v180, v181\n"
    "v_max3_f32 v229, v229, v182, v183\n"
    "v_max3_f32 v230, v230, v184, v185\n"
    "v_max3_f32 v230, v230, v186, v187\n"
    "v_max3_f32 v231, v231, v188, v189\n"
    "v_max3_f32 v231, v231, v190, v191\n"
    "s_nop 3\n"
    "ds_read_b128 a[192:195], v212 offset:6144\n"
    "ds_read_b128 a[196:199], v213 offset:6144\n"
    "ds_read_b128 a[200:203], v212 offset:6272\n"
    "ds_read_b128 a[204:207], v213 offset:6272\n"
    "ds_read_b128 a[208:211], v212 offset:6400\n"
    "ds_read_b128 a[212:215], v213 offset:6400\n"
    "s_waitcnt lgkmcnt(0)\n"
    "v_mfma_f32_16x16x32_bf16 v[176:179], a[192:195], v[16:19], 0\n"
    "v_mfma_f32_16x16x32_bf16 v[176:179], a[196:199], v[20:23], v[176:179]\n"
    "v_mfma_f32_16x16x32_bf16 v[176:179], a[200:203], v[24:27], v[176:179]\n"
    "v_mfma_f32_16x16x32_bf16 v[176:179], a[204:207], v[28:31], v[176:179]\n"
    "v_mfma_f32_16x16x32_bf16 v[176:179], a[208:211], v[32:35], v[176:179]\n"
    "v_mfma_f32_16x16x32_bf16 v[176:179], a[212:215], v[36:39], v[176:179]\n"
    "v_mfma_f32_16x16x32_bf16 v[180:183], a[192:195], v[40:43], 0\n"
    "v_mfma_f32_16x16x32_bf16 v[180:183], a[196:199], v[44:47], v[180:183]\n"
    "v_mfma_f32_16x16x32_bf16 v[180:183], a[200:203], v[48:51], v[180:183]\n"
    "v_mfma_f32_16x16x32_bf16 v[180:183], a[204:207], v[52:55], v[180:183]\n"
    "v_mfma_f32_16x16x32_bf16 v[180:183], a[208:211], v[56:59], v[180:183]\n"
    "v_mfma_f32_16x16x32_bf16 v[180:183], a[212:215], v[60:63], v[180:183]\n"
    "v_mfma_f32_16x16x32_bf16 v[184:187], a[192:195], v[64:67], 0\n"
    "v_mfma_f32_16x16x32_bf16 v[184:187], a[196:199], v[68:71], v[184:187]\n"
    "v_mfma_f32_16x16x32_bf16 v[184:187], a[200:203], v[72:75], v[184:187]\n"
    "v_mfma_f32_16x16x32_bf16 v[184:187], a[204:207], v[76:79], v[184:187]\n"
    "v_mfma_f32_16x16x32_bf16 v[184:187], a[208:211], v[80:83], v[184:187]\n"
    "v_mfma_f32_16x16x32_bf16 v[184:187], a[212:215], v[84:87], v[184:187]\n"
    "v_mfma_f32_16x16x32_bf16 v[188:191], a[192:195], v[88:91], 0\n"
    "v_mfma_f32_16x16x32_bf16 v[188:191], a[196:199], v[92:95], v[188:191]\n"
    "v_mfma_f32_16x16x32_bf16 v[188:191], a[200:203], v[96:99], v[188:191]\n"
    "v_mfma_f32_16x16x32_bf16 v[188:191], a[204:207], v[100:103], v[188:191]\n"
    "v_mfma_f32_16x16x32_bf16 v[188:191], a[208:211], v[104:107], v[188:191]\n"
    "v_mfma_f32_16x16x32_bf16 v[188:191], a[212:215], v[108:111], v[188:191]\n"
    "s_nop 7\n"
    "v_max3_f32 v228, v228, v176, v177\n"
    "v_max3_f32 v228, v228, v178, v179\n"
    "v_max3_f32 v229, v229, v180, v181\n"
    "v_max3_f32 v229, v229, v182, v183\n"
    "v_max3_f32 v230, v230, v184, v185\n"
    "v_max3_f32 v230, v230, v186, v187\n"
    "v_max3_f32 v231, v231, v188, v189\n"
    "v_max3_f32 v231, v231, v190, v191\n"
    "s_nop 3\n"
    "ds_read_b128 a[192:195], v212 offset:12288\n"
    "ds_read_b128 a[196:199], v213 offset:12288\n"
    "ds_read_b128 a[200:203], v212 offset:12416\n"
    "ds_read_b128 a[204:207], v213 offset:12416\n"
    "ds_read_b128 a[208:211], v212 offset:12544\n"
    "ds_read_b128 a[212:215], v213 offset:12544\n"
    "s_waitcnt lgkmcnt(0)\n"
    "v_mfma_f32_16x16x32_bf16 v[176:179], a[192:195], v[16:19], 0\n"
    "v_mfma_f32_16x16x32_bf16 v[176:179], a[196:199], v[20:23], v[176:179]\n"
    "v_mfma_f32_16x16x32_bf16 v[176:179], a[200:203], v[24:27], v[176:179]\n"
    "v_mfma_f32_16x16x32_bf16 v[176:179], a[204:207], v[28:31], v[176:179]\n"
    "v_mfma_f32_16x16x32_bf16 v[176:179], a[208:211], v[32:35], v[176:179]\n"
    "v_mfma_f32_16x16x32_bf16 v[176:179], a[212:215], v[36:39], v[176:179]\n"
    "v_mfma_f32_16x16x32_bf16 v[180:183], a[192:195], v[40:43], 0\n"
    "v_mfma_f32_16x16x32_bf16 v[180:183], a[196:199], v[44:47], v[180:183]\n"
    "v_mfma_f32_16x16x32_bf16 v[180:183], a[200:203], v[48:51], v[180:183]\n"
    "v_mfma_f32_16x16x32_bf16 v[180:183], a[204:207], v[52:55], v[180:183]\n"
    "v_mfma_f32_16x16x32_bf16 v[180:183], a[208:211], v[56:59], v[180:183]\n"
    "v_mfma_f32_16x16x32_bf16 v[180:183], a[212:215], v[60:63], v[180:183]\n"
    "v_mfma_f32_16x16x32_bf16 v[184:187], a[192:195], v[64:67], 0\n"
    "v_mfma_f32_16x16x32_bf16 v[184:187], a[196:199], v[68:71], v[184:187]\n"
    "v_mfma_f32_16x16x32_bf16 v[184:187], a[200:203], v[72:75], v[184:187]\n"
    "v_mfma_f32_16x16x32_bf16 v[184:187], a[204:207], v[76:79], v[184:187]\n"
    "v_mfma_f32_16x16x32_bf16 v[184:187], a[208:211], v[80:83], v[184:187]\n"
    "v_mfma_f32_16x16x32_bf16 v[184:187], a[212:215], v[84:87], v[184:187]\n"
    "v_mfma_f32_16x16x32_bf16 v[188:191], a[192:195], v[88:91], 0\n"
    "v_mfma_f32_16x16x32_bf16 v[188:191], a[196:199], v[92:95], v[188:191]\n"
    "v_mfma_f32_16x16x32_bf16 v[188:191], a[200:203], v[96:99], v[188:191]\n"
    "v_mfma_f32_16x16x32_bf16 v[188:191], a[204:207], v[100:103], v[188:191]\n"
    "v_mfma_f32_16x16x32_bf16 v[188:191], a[208:211], v[104:107], v[188:191]\n"
    "v_mfma_f32_16x16x32_bf16 v[188:191], a[212:215], v[108:111], v[188:191]\n"
    "s_nop 7\n"
    "v_max3_f32 v228, v228, v176, v177\n"
    "v_max3_f32 v228, v228, v178, v179\n"
    "v_max3_f32 v229, v229, v180, v181\n"
    "v_max3_f32 v229, v229, v182, v183\n"
    "v_max3_f32 v230, v230, v184, v185\n"
    "v_max3_f32 v230, v230, v186, v187\n"
    "v_max3_f32 v231, v231, v188, v189\n"
    "v_max3_f32 v231, v231, v190, v191\n"
    "s_nop 3\n"
    "ds_read_b128 a[192:195], v212 offset:18432\n"
    "ds_read_b128 a[196:199], v213 offset:18432\n"
    "ds_read_b128 a[200:203], v212 offset:18560\n"
    "ds_read_b128 a[204:207], v213 offset:18560\n"
    "ds_read_b128 a[208:211], v212 offset:18688\n"
    "ds_read_b128 a[212:215], v213 offset:18688\n"
    "s_waitcnt lgkmcnt(0)\n"
    "v_mfma_f32_16x16x32_bf16 v[176:179], a[192:195], v[16:19], 0\n"
    "v_mfma_f32_16x16x32_bf16 v[176:179], a[196:199], v[20:23], v[176:179]\n"
    "v_mfma_f32_16x16x32_bf16 v[176:179], a[200:203], v[24:27], v[176:179]\n"
    "v_mfma_f32_16x16x32_bf16 v[176:179], a[204:207], v[28:31], v[176:179]\n"
    "v_mfma_f32_16x16x32_bf16 v[176:179], a[208:211], v[32:35], v[176:179]\n"
    "v_mfma_f32_16x16x32_bf16 v[176:179], a[212:215], v[36:39], v[176:179]\n"
    "v_mfma_f32_16x16x32_bf16 v[180:183], a[192:195], v[40:43], 0\n"
    "v_mfma_f32_16x16x32_bf16 v[180:183], a[196:199], v[44:47], v[180:183]\n"
    "v_mfma_f32_16x16x32_bf16 v[180:183], a[200:203], v[48:51], v[180:183]\n"
    "v_mfma_f32_16x16x32_bf16 v[180:183], a[204:207], v[52:55], v[180:183]\n"
    "v_mfma_f32_16x16x32_bf16 v[180:183], a[208:211], v[56:59], v[180:183]\n"
    "v_mfma_f32_16x16x32_bf16 v[180:183], a[212:215], v[60:63], v[180:183]\n"
    "v_mfma_f32_16x16x32_bf16 v[184:187], a[192:195], v[64:67], 0\n"
    "v_mfma_f32_16x16x32_bf16 v[184:187], a[196:199], v[68:71], v[184:187]\n"
    "v_mfma_f32_16x16x32_bf16 v[184:187], a[200:203], v[72:75], v[184:187]\n"
    "v_mfma_f32_16x16x32_bf16 v[184:187], a[204:207], v[76:79], v[184:187]\n"
    "v_mfma_f32_16x16x32_bf16 v[184:187], a[208:211], v[80:83], v[184:187]\n"
    "v_mfma_f32_16x16x32_bf16 v[184:187], a[212:215], v[84:87], v[184:187]\n"
    "v_mfma_f32_16x16x32_bf16 v[188:191], a[192:195], v[88:91], 0\n"
    "v_mfma_f32_16x16x32_bf16 v[188:191], a[196:199], v[92:95], v[188:191]\n"
    "v_mfma_f32_16x16x32_bf16 v[188:191], a[200:203], v[96:99], v[188:191]\n"
    "v_mfma_f32_16x16x32_bf16 v[188:191], a[204:207], v[100:103], v[188:191]\n"
    "v_mfma_f32_16x16x32_bf16 v[188:191], a[208:211], v[104:107], v[188:191]\n"
    "v_mfma_f32_16x16x32_bf16 v[188:191], a[212:215], v[108:111], v[188:191]\n"
    "s_nop 7\n"
    "v_max3_f32 v228, v228, v176, v177\n"
    "v_max3_f32 v228, v228, v178, v179\n"
    "v_max3_f32 v229, v229, v180, v181\n"
    "v_max3_f32 v229, v229, v182, v183\n"
    "v_max3_f32 v230, v230, v184, v185\n"
    "v_max3_f32 v230, v230, v186, v187\n"
    "v_max3_f32 v231, v231, v188, v189\n"
    "v_max3_f32 v231, v231, v190, v191\n"
    "s_nop 3\n"
    "s_sub_u32 s68, s68, 1\n"
    "s_cbranch_scc0 L_fb_loop\n"
    "v_mov_b32 v248, v228\n"
    "s_nop 1\n"
    "v_permlane16_swap_b32 v228, v248\n"
    "v_max_f32 v228, v228, v248\n"
    "v_mov_b32 v248, v228\n"
    "s_nop 1\n"
    "v_permlane32_swap_b32 v228, v248\n"
    "v_max_f32 v228, v228, v248\n"
    "v_sub_f32 v192, 0, v228\n"
    "v_sub_f32 v193, 0, v228\n"
    "v_sub_f32 v194, 0, v228\n"
    "v_sub_f32 v195, 0, v228\n"
    "v_mov_b32 v249, v229\n"
    "s_nop 1\n"
    "v_permlane16_swap_b32 v229, v249\n"
    "v_max_f32 v229, v229, v249\n"
    "v_mov_b32 v249, v229\n"
    "s_nop 1\n"
    "v_permlane32_swap_b32 v229, v249\n"
    "v_max_f32 v229, v229, v249\n"
    "v_sub_f32 v196, 0, v229\n"
    "v_sub_f32 v197, 0, v229\n"
    "v_sub_f32 v198, 0, v229\n"
    "v_sub_f32 v199, 0, v229\n"
    "v_mov_b32 v250, v230\n"
    "s_nop 1\n"
    "v_permlane16_swap_b32 v230, v250\n"
    "v_max_f32 v230, v230, v250\n"
    "v_mov_b32 v250, v230\n"
    "s_nop 1\n"
    "v_permlane32_swap_b32 v230, v250\n"
    "v_max_f32 v230, v230, v250\n"
    "v_sub_f32 v200, 0, v230\n"
    "v_sub_f32 v201, 0, v230\n"
    "v_sub_f32 v202, 0, v230\n"
    "v_sub_f32 v203, 0, v230\n"
    "v_mov_b32 v251, v231\n"
    "s_nop 1\n"
    "v_permlane16_swap_b32 v231, v251\n"
    "v_max_f32 v231, v231, v251\n"
    "v_mov_b32 v251, v231\n"
    "s_nop 1\n"
    "v_permlane32_swap_b32 v231, v251\n"
    "v_max_f32 v231, v231, v251\n"
    "v_sub_f32 v204, 0, v231\n"
    "v_sub_f32 v205, 0, v231\n"
    "v_sub_f32 v206, 0, v231\n"
    "v_sub_f32 v207, 0, v231\n"
    "s_waitcnt vmcnt(0) lgkmcnt(0)\n"
    "s_barrier\n"
    "s_add_u32 m0, s65, 0\n"
    "s_nop 0\n"
    "buffer_load_dwordx4 v218, s[44:47], s65 offen offset:0 lds\n"
    "buffer_load_dwordx4 v219, s[44:47], s65 offen offset:1024 lds\n"
    "buffer_load_dwordx4 v220, s[44:47], s65 offen offset:2048 lds\n"
    "buffer_load_dwordx4 v218, s[44:47], s65 offen offset:3072 lds\n"
    "s_add_u32 m0, s65, 4096\n"
    "s_nop 0\n"
    "buffer_load_dwordx4 v219, s[48:51], s65 offen offset:0 lds\n"
    "buffer_load_dwordx4 v220, s[48:51], s65 offen offset:1024 lds\n"
    "s_add_u32 m0, s65, 49152\n"
    "s_nop 0\n"
    "buffer_load_dwordx4 v218, s[52:55], s65 offen offset:0 lds\n"
    "buffer_load_dwordx4 v219, s[52:55], s65 offen offset:1024 lds\n"
    "buffer_load_dwordx4 v220, s[52:55], s65 offen offset:2048 lds\n"
    "buffer_load_dwordx4 v218, s[52:55], s65 offen offset:3072 lds\n"
    "s_add_u32 m0, s65, 53248\n"
    "s_nop 0\n"
    "buffer_load_dwordx4 v219, s[56:59], s65 offen offset:0 lds\n"
    "buffer_load_dwordx4 v220, s[56:59], s65 offen offset:1024 lds\n"
    "s_add_u32 m0, s65, 24576\n"
    "s_nop 0\n"
    "buffer_load_dwordx4 v218, s[44:47], s80 offen offset:0 lds\n"
    "buffer_load_dwordx4 v219, s[44:47], s80 offen offset:1024 lds\n"
    "buffer_load_dwordx4 v220, s[44:47], s80 offen offset:2048 lds\n"
    "buffer_load_dwordx4 v218, s[44:47], s80 offen offset:3072 lds\n"
    "s_add_u32 m0, s65, 28672\n"
    "s_nop 0\n"
    "buffer_load_dwordx4 v219, s[48:51], s80 offen offset:0 lds\n"
    "buffer_load_dwordx4 v220, s[48:51], s80 offen offset:1024 lds\n"
    "s_add_u32 s66, s65, 49152\n"
    "s_add_u32 s67, s65, 24576\n"
    "s_waitcnt vmcnt(0)\n"
    "s_barrier\n"
    "ds_read_b128 a[192:195], v212 offset:0\n"
    "ds_read_b128 a[196:199], v213 offset:0\n"
    "ds_read_b128 a[200:203], v212 offset:128\n"
    "ds_read_b128 a[204:207], v213 offset:128\n"
    "ds_read_b128 a[208:211], v212 offset:256\n"
    "ds_read_b128 a[212:215], v213 offset:256\n"
    "s_waitcnt lgkmcnt(0)\n"
    "s_nop 1\n"
    "s_branch L_iter_m1\n"
    "L_done:\n"
    "s_mov_b64 s[20:21], s[34:35]\n"
    "s_mov_b64 s[36:37], s[60:61]\n"
    "s_mov_b64 s[40:41], s[4:5]\n"
    "s_mov_b64 s[44:45], s[30:31]\n"
    "s_mov_b64 s[48:49], s[16:17]\n"
    "s_mov_b64 s[52:53], s[18:19]\n"
    "s_mov_b64 s[56:57], s[10:11]\n"
    "s_mov_b64 s[60:61], s[6:7]\n"
    "s_mov_b64 s[98:99], s[34:35]\n"
    "v_and_b32 v228, 63, v0\n"
    "v_lshrrev_b32 v233, 6, v0\n"
    "s_nop 0\n"
    "v_readfirstlane_b32 s64, v233\n"
    "s_mov_b32 s81, %0\n"
    "s_and_b32 s65, s81, 7\n"
    "s_lshr_b32 s68, s81, 3\n"
    "s_and_b32 s67, s68, 3\n"
    "s_lshr_b32 s66, s68, 2\n"
    "s_mul_i32 s78, s64, 10240\n"
    "s_and_b32 s37, s37, 0xffff\n"
    "s_mov_b32 s38, 25165824\n"
    "s_mov_b32 s39, 0x00020000\n"
    "s_and_b32 s41, s41, 0xffff\n"
    "s_mov_b32 s42, 7864320\n"
    "s_mov_b32 s43, 0x00020000\n"
    "s_and_b32 s45, s45, 0xffff\n"
    "s_mov_b32 s46, 20971520\n"
    "s_mov_b32 s47, 0x00020000\n"
    "s_and_b32 s49, s49, 0xffff\n"
    "s_mov_b32 s50, 2621440\n"
    "s_mov_b32 s51, 0x00020000\n"
    "s_and_b32 s53, s53, 0xffff\n"
    "s_mov_b32 s54, 2621440\n"
    "s_mov_b32 s55, 0x00020000\n"
    "s_and_b32 s57, s57, 0xffff\n"
    "s_mov_b32 s58, 6291456\n"
    "s_mov_b32 s59, 0x00020000\n"
    "s_and_b32 s61, s61, 0xffff\n"
    "s_mov_b32 s62, 1966080\n"
    "s_mov_b32 s63, 0x00020000\n"
    "s_lshl_b32 s82, s68, 8\n"
    "s_add_u32 s98, s98, s82\n"
    "s_addc_u32 s99, s99, 0\n"
    "s_cmp_ge_u32 s65, 6\n"
    "s_cselect_b64 s[36:37], s[56:57], s[36:37]\n"
    "s_cselect_b32 s38, s58, s38\n"
    "s_cselect_b64 s[40:41], s[60:61], s[40:41]\n"
    "s_cselect_b32 s90, 48, 192\n"
    "s_cselect_b32 s89, 24, 32\n"
    "s_cselect_b32 s84, 6, 0\n"
    "s_cselect_b32 s74, 4, 6\n"
    "s_sub_u32 s84, s65, s84\n"
    "s_mov_b32 s75, 0x7fff0000\n"
    "s_mov_b32 s77, 1\n"
    "s_add_u32 s83, s84, 1\n"
    "s_mul_i32 s83, s83, s89\n"
    "s_mul_i32 s42, s83, 40960\n"
    "s_lshl_b32 s82, s66, 4\n"
    "s_lshl_b32 s83, s64, 2\n"
    "s_add_u32 s82, s82, s83\n"
    "s_mul_i32 s91, s84, s89\n"
    "s_add_u32 s83, s82, 0\n"
    "s_mul_i32 s85, s83, s90\n"
    "s_add_u32 s85, s85, s91\n"
    "s_lshl_b32 s69, s85, 10\n"
    "s_add_u32 s83, s82, 1\n"
    "s_mul_i32 s85, s83, s90\n"
    "s_add_u32 s85, s85, s91\n"
    "s_lshl_b32 s70, s85, 10\n"
    "s_add_u32 s83, s82, 2\n"
    "s_mul_i32 s85, s83, s90\n"
    "s_add_u32 s85, s85, s91\n"
    "s_lshl_b32 s71, s85, 10\n"
    "s_add_u32 s83, s82, 3\n"
    "s_mul_i32 s85, s83, s90\n"
    "s_add_u32 s85, s85, s91\n"
    "s_lshl_b32 s72, s85, 10\n"
    "s_add_u32 s83, s91, s64\n"
    "s_mul_i32 s83, s83, 40960\n"
    "s_mul_i32 s85, s67, 320\n"
    "s_add_u32 s73, s83, s85\n"
    "s_lshl_b32 s83, s68, 3\n"
    "s_add_u32 s83, s83, s65\n"
    "s_lshl_b32 s83, s83, 2\n"
    "s_add_u32 s83, s83, s64\n"
    "s_mul_i32 s79, s83, 20480\n"
    "v_lshrrev_b32 v229, 4, v228\n"
    "v_and_b32 v230, 15, v228\n"
    "v_lshlrev_b32 v226, 4, v228\n"
    "v_lshrrev_b32 v231, 2, v230\n"
    "v_and_b32 v232, 3, v228\n"
    "s_add_u32 s82, s66, 32\n"
    "s_lshl_b32 s82, s82, 8\n"
    "s_add_u32 s20, s20, s82\n"
    "s_addc_u32 s21, s21, 0\n"
    "v_add_u32 v233, 0, v228\n"
    "v_mul_u32_u24 v234, 3277, v233\n"
    "v_lshrrev_b32 v234, 16, v234\n"
    "v_mul_u32_u24 v235, 20, v234\n"
    "v_sub_u32 v235, v233, v235\n"
    "v_lshrrev_b32 v236, 1, v234\n"
    "v_and_b32 v236, 2, v236\n"
    "v_xor_b32 v235, v235, v236\n"
    "v_mul_u32_u24 v234, 1280, v234\n"
    "v_lshl_add_u32 v235, v235, 4, v234\n"
    "v_add_u32 v214, 0, v235\n"
    "v_add_u32 v233, 64, v228\n"
    "v_mul_u32_u24 v234, 3277, v233\n"
    "v_lshrrev_b32 v234, 16, v234\n"
    "v_mul_u32_u24 v235, 20, v234\n"
    "v_sub_u32 v235, v233, v235\n"
    "v_lshrrev_b32 v236, 1, v234\n"
    "v_and_b32 v236, 2, v236\n"
    "v_xor_b32 v235, v235, v236\n"
    "v_mul_u32_u24 v234, 1280, v234\n"
    "v_lshl_add_u32 v235, v235, 4, v234\n"
    "v_add_u32 v215, -1024, v235\n"
    "v_add_u32 v233, 128, v228\n"
    "v_mul_u32_u24 v234, 3277, v233\n"
    "v_lshrrev_b32 v234, 16, v234\n"
    "v_mul_u32_u24 v235, 20, v234\n"
    "v_sub_u32 v235, v233, v235\n"
    "v_lshrrev_b32 v236, 1, v234\n"
    "v_and_b32 v236, 2, v236\n"
    "v_xor_b32 v235, v235, v236\n"
    "v_mul_u32_u24 v234, 1280, v234\n"
    "v_lshl_add_u32 v235, v235, 4, v234\n"
    "v_add_u32 v216, -2048, v235\n"
    "v_add_u32 v233, 192, v228\n"
    "v_mul_u32_u24 v234, 3277, v233\n"
    "v_lshrrev_b32 v234, 16, v234\n"
    "v_mul_u32_u24 v235, 20, v234\n"
    "v_sub_u32 v235, v233, v235\n"
    "v_lshrrev_b32 v236, 1, v234\n"
    "v_and_b32 v236, 2, v236\n"
    "v_xor_b32 v235, v235, v236\n"
    "v_mul_u32_u24 v234, 1280, v234\n"
    "v_lshl_add_u32 v235, v235, 4, v234\n"
    "v_add_u32 v217, -3072, v235\n"
    "v_add_u32 v233, 256, v228\n"
    "v_mul_u32_u24 v234, 3277, v233\n"
    "v_lshrrev_b32 v234, 16, v234\n"
    "v_mul_u32_u24 v235, 20, v234\n"
    "v_sub_u32 v235, v233, v235\n"
    "v_lshrrev_b32 v236, 1, v234\n"
    "v_and_b32 v236, 2, v236\n"
    "v_xor_b32 v235, v235, v236\n"
    "v_mul_u32_u24 v234, 1280, v234\n"
    "v_lshl_add_u32 v235, v235, 4, v234\n"
    "v_add_u32 v218, 0, v235\n"
    "v_add_u32 v233, 0, v228\n"
    "v_mul_u32_u24 v234, 3277, v233\n"
    "v_lshrrev_b32 v234, 16, v234\n"
    "v_mul_u32_u24 v235, 20, v234\n"
    "v_sub_u32 v235, v233, v235\n"
    "v_lshrrev_b32 v236, 1, v234\n"
    "v_and_b32 v236, 2, v236\n"
    "v_xor_b32 v235, v235, v236\n"
    "v_mul_u32_u24 v234, 1280, v234\n"
    "v_lshl_add_u32 v235, v235, 4, v234\n"
    "v_add_u32 v219, 19456, v235\n"
    "v_add_u32 v233, 64, v228\n"
    "v_mul_u32_u24 v234, 3277, v233\n"
    "v_lshrrev_b32 v234, 16, v234\n"
    "v_mul_u32_u24 v235, 20, v234\n"
    "v_sub_u32 v235, v233, v235\n"
    "v_lshrrev_b32 v236, 1, v234\n"
    "v_and_b32 v236, 2, v236\n"
    "v_xor_b32 v235, v235, v236\n"
    "v_mul_u32_u24 v234, 1280, v234\n"
    "v_lshl_add_u32 v235, v235, 4, v234\n"
    "v_add_u32 v220, 18432, v235\n"
    "v_add_u32 v233, 128, v228\n"
    "v_mul_u32_u24 v234, 3277, v233\n"
    "v_lshrrev_b32 v234, 16, v234\n"
    "v_mul_u32_u24 v235, 20, v234\n"
    "v_sub_u32 v235, v233, v235\n"
    "v_lshrrev_b32 v236, 1, v234\n"
    "v_and_b32 v236, 2, v236\n"
    "v_xor_b32 v235, v235, v236\n"
    "v_mul_u32_u24 v234, 1280, v234\n"
    "v_lshl_add_u32 v235, v235, 4, v234\n"
    "v_add_u32 v221, 17408, v235\n"
    "v_add_u32 v233, 192, v228\n"
    "v_mul_u32_u24 v234, 3277, v233\n"
    "v_lshrrev_b32 v234, 16, v234\n"
    "v_mul_u32_u24 v235, 20, v234\n"
    "v_sub_u32 v235, v233, v235\n"
    "v_lshrrev_b32 v236, 1, v234\n"
    "v_and_b32 v236, 2, v236\n"
    "v_xor_b32 v235, v235, v236\n"
    "v_mul_u32_u24 v234, 1280, v234\n"
    "v_lshl_add_u32 v235, v235, 4, v234\n"
    "v_add_u32 v222, 20480, v235\n"
    "v_add_u32 v233, 256, v228\n"
    "v_mul_u32_u24 v234, 3277, v233\n"
    "v_lshrrev_b32 v234, 16, v234\n"
    "v_mul_u32_u24 v235, 20, v234\n"
    "v_sub_u32 v235, v233, v235\n"
    "v_lshrrev_b32 v236, 1, v234\n"
    "v_and_b32 v236, 2, v236\n"
    "v_xor_b32 v235, v235, v236\n"
    "v_mul_u32_u24 v234, 1280, v234\n"
    "v_lshl_add_u32 v235, v235, 4, v234\n"
    "v_add_u32 v223, 19456, v235\n"
    "v_lshl_add_u32 v233, v229, 2, v231\n"
    "v_mul_u32_u24 v233, 320, v233\n"
    "v_lshl_add_u32 v233, v232, 3, v233\n"
    "v_and_b32 v234, 1, v229\n"
    "v_lshlrev_b32 v234, 5, v234\n"
    "v_add_u32 v235, 0, v233\n"
    "v_add_u32 v208, v235, v234\n"
    "v_sub_u32 v209, v235, v234\n"
    "v_add_u32 v235, 40960, v233\n"
    "v_add_u32 v210, v235, v234\n"
    "v_sub_u32 v211, v235, v234\n"
    "v_add_u32 v235, 81920, v233\n"
    "v_add_u32 v212, v235, v234\n"
    "v_sub_u32 v213, v235, v234\n"
    "s_lshr_b32 s82, s65, 1\n"
    "s_and_b32 s83, s65, 1\n"
    "s_lshl_b32 s84, s64, 1\n"
    "s_add_u32 s84, s84, 0\n"
    "s_add_u32 s85, s79, 0\n"
    "s_cmp_eq_u32 s84, s65\n"
    "s_cselect_b32 s22, s75, s85\n"
    "s_cselect_b32 s85, 0, 10240\n"
    "s_add_u32 s85, s85, 81920\n"
    "v_add_u32 v14, s85, v226\n"
    "s_lshl_b32 s84, s64, 1\n"
    "s_add_u32 s84, s84, 1\n"
    "s_add_u32 s85, s79, 10240\n"
    "s_cmp_eq_u32 s84, s65\n"
    "s_cselect_b32 s23, s75, s85\n"
    "s_cselect_b32 s85, 0, 10240\n"
    "s_add_u32 s85, s85, 81920\n"
    "v_add_u32 v15, s85, v226\n"
    "v_mul_u32_u24 v233, 1280, v230\n"
    "v_lshl_add_u32 v224, v229, 3, v233\n"
    "s_waitcnt vmcnt(0)\n"
    "s_barrier\n"
    "s_cmp_eq_u32 s64, 0\n"
    "s_cbranch_scc0 L_arrivedo\n"
    "v_mov_b32 v248, 0\n"
    "v_mov_b32 v249, 1\n"
    "s_mov_b64 s[96:97], exec\n"
    "s_mov_b64 exec, 1\n"
    "global_atomic_add v254, v248, v249, s[20:21] sc0\n"
    "s_mov_b64 exec, s[96:97]\n"
    "L_arrivedo:\n"
    "s_add_u32 m0, s78, 0\n"
    "s_nop 0\n"
    "buffer_load_dwordx4 v214, s[40:43], s73 offen offset:0 lds\n"
    "buffer_load_dwordx4 v215, s[40:43], s73 offen offset:1024 lds\n"
    "buffer_load_dwordx4 v216, s[40:43], s73 offen offset:2048 lds\n"
    "buffer_load_dwordx4 v217, s[40:43], s73 offen offset:3072 lds\n"
    "s_add_u32 m0, s78, 4096\n"
    "s_nop 0\n"
    "buffer_load_dwordx4 v218, s[40:43], s73 offen offset:0 lds\n"
    "buffer_load_dwordx4 v219, s[40:43], s73 offen offset:1024 lds\n"
    "buffer_load_dwordx4 v220, s[40:43], s73 offen offset:2048 lds\n"
    "buffer_load_dwordx4 v221, s[40:43], s73 offen offset:3072 lds\n"
    "s_add_u32 m0, s78, 8192\n"
    "s_nop 0\n"
    "buffer_load_dwordx4 v222, s[40:43], s73 offen offset:0 lds\n"
    "buffer_load_dwordx4 v223, s[40:43], s73 offen offset:1024 lds\n"
    "s_add_u32 s73, s73, 163840\n"
    "s_add_u32 m0, s78, 40960\n"
    "s_nop 0\n"
    "buffer_load_dwordx4 v214, s[40:43], s73 offen offset:0 lds\n"
    "buffer_load_dwordx4 v215, s[40:43], s73 offen offset:1024 lds\n"
    "buffer_load_dwordx4 v216, s[40:43], s73 offen offset:2048 lds\n"
    "buffer_load_dwordx4 v217, s[40:43], s73 offen offset:3072 lds\n"
    "s_add_u32 m0, s78, 45056\n"
    "s_nop 0\n"
    "buffer_load_dwordx4 v218, s[40:43], s73 offen offset:0 lds\n"
    "buffer_load_dwordx4 v219, s[40:43], s73 offen offset:1024 lds\n"
    "buffer_load_dwordx4 v220, s[40:43], s73 offen offset:2048 lds\n"
    "buffer_load_dwordx4 v221, s[40:43], s73 offen offset:3072 lds\n"
    "s_add_u32 m0, s78, 49152\n"
    "s_nop 0\n"
    "buffer_load_dwordx4 v222, s[40:43], s73 offen offset:0 lds\n"
    "buffer_load_dwordx4 v223, s[40:43], s73 offen offset:1024 lds\n"
    "s_add_u32 s73, s73, 163840\n"
    "s_add_u32 m0, s78, 81920\n"
    "s_nop 0\n"
    "buffer_load_dwordx4 v214, s[40:43], s73 offen offset:0 lds\n"
    "buffer_load_dwordx4 v215, s[40:43], s73 offen offset:1024 lds\n"
    "buffer_load_dwordx4 v216, s[40:43], s73 offen offset:2048 lds\n"
    "buffer_load_dwordx4 v217, s[40:43], s73 offen offset:3072 lds\n"
    "s_add_u32 m0, s78, 86016\n"
    "s_nop 0\n"
    "buffer_load_dwordx4 v218, s[40:43], s73 offen offset:0 lds\n"
    "buffer_load_dwordx4 v219, s[40:43], s73 offen offset:1024 lds\n"
    "buffer_load_dwordx4 v220, s[40:43], s73 offen offset:2048 lds\n"
    "buffer_load_dwordx4 v221, s[40:43], s73 offen offset:3072 lds\n"
    "s_add_u32 m0, s78, 90112\n"
    "s_nop 0\n"
    "buffer_load_dwordx4 v222, s[40:43], s73 offen offset:0 lds\n"
    "buffer_load_dwordx4 v223, s[40:43], s73 offen offset:1024 lds\n"
    "s_add_u32 s73, s73, 163840\n"
    "s_cmp_eq_u32 s64, 0\n"
    "s_cbranch_scc0 L_polledo\n"
    "s_waitcnt vmcnt(30)\n"
    "v_readfirstlane_b32 s89, v254\n"
    "s_nop 3\n"
    "s_cmp_ge_u32 s89, 31\n"
    "s_cbranch_scc1 L_polledo\n"
    "v_mov_b32 v248, 0\n"
    "s_mov_b64 s[96:97], exec\n"
    "s_mov_b64 exec, 1\n"
    "s_mov_b32 s88, 0x200000\n"
    "L_pollo:\n"
    "global_load_dword v250, v248, s[20:21] sc1\n"
    "s_waitcnt vmcnt(0)\n"
    "v_readfirstlane_b32 s89, v250\n"
    "s_nop 3\n"
    "s_cmp_ge_u32 s89, 32\n"
    "s_cbranch_scc1 L_polloko\n"
    "s_sleep 1\n"
    "s_sub_u32 s88, s88, 1\n"
    "s_cbranch_scc0 L_pollo\n"
    "L_polloko:\n"
    "s_mov_b64 exec, s[96:97]\n"
    "L_polledo:\n"
    "s_barrier\n"
    "buffer_load_dwordx4 v[16:19], v226, s[36:39], s69 offen offset:0 sc1\n"
    "buffer_load_dwordx4 v[20:23], v226, s[36:39], s69 offen offset:1024 sc1\n"
    "buffer_load_dwordx4 v[24:27], v226, s[36:39], s69 offen offset:2048 sc1\n"
    "buffer_load_dwordx4 v[28:31], v226, s[36:39], s69 offen offset:3072 sc1\n"
    "buffer_load_dwordx4 v[32:35], v226, s[36:39], s70 offen offset:0 sc1\n"
    "buffer_load_dwordx4 v[36:39], v226, s[36:39], s70 offen offset:1024 sc1\n"
    "buffer_load_dwordx4 v[40:43], v226, s[36:39], s70 offen offset:2048 sc1\n"
    "buffer_load_dwordx4 v[44:47], v226, s[36:39], s70 offen offset:3072 sc1\n"
    "buffer_load_dwordx4 v[48:51], v226, s[36:39], s71 offen offset:0 sc1\n"
    "buffer_load_dwordx4 v[52:55], v226, s[36:39], s71 offen offset:1024 sc1\n"
    "buffer_load_dwordx4 v[56:59], v226, s[36:39], s71 offen offset:2048 sc1\n"
    "buffer_load_dwordx4 v[60:63], v226, s[36:39], s71 offen offset:3072 sc1\n"
    "buffer_load_dwordx4 v[64:67], v226, s[36:39], s72 offen offset:0 sc1\n"
    "buffer_load_dwordx4 v[68:71], v226, s[36:39], s72 offen offset:1024 sc1\n"
    "buffer_load_dwordx4 v[72:75], v226, s[36:39], s72 offen offset:2048 sc1\n"
    "buffer_load_dwordx4 v[76:79], v226, s[36:39], s72 offen offset:3072 sc1\n"
    "s_add_u32 s69, s69, 4096\n"
    "s_add_u32 s70, s70, 4096\n"
    "s_add_u32 s71, s71, 4096\n"
    "s_add_u32 s72, s72, 4096\n"
    "s_barrier\n"
    "ds_read_b64_tr_b16 a[160:161], v208 offset:0\n"
    "ds_read_b64_tr_b16 a[162:163], v208 offset:5120\n"
    "ds_read_b64_tr_b16 a[164:165], v208 offset:10240\n"
    "ds_read_b64_tr_b16 a[166:167], v208 offset:15360\n"
    "ds_read_b64_tr_b16 a[168:169], v208 offset:20480\n"
    "ds_read_b64_tr_b16 a[170:171], v208 offset:25600\n"
    "ds_read_b64_tr_b16 a[172:173], v208 offset:30720\n"
    "ds_read_b64_tr_b16 a[174:175], v208 offset:35840\n"
    "s_waitcnt lgkmcnt(0)\n"
    "ds_read_b64_tr_b16 a[176:177], v209 offset:32\n"
    "s_waitcnt vmcnt(12)\n"
    "v_mfma_f32_16x16x32_bf16 a[0:3], a[160:163], v[16:19], 0\n"
    "buffer_load_dwordx4 v[80:83], v226, s[36:39], s69 offen offset:0 sc1\n"
    "v_mfma_f32_16x16x32_bf16 a[0:3], a[164:167], v[20:23], a[0:3]\n"
    "ds_read_b64_tr_b16 a[178:179], v209 offset:5152\n"
    "v_mfma_f32_16x16x32_bf16 a[0:3], a[168:171], v[24:27], a[0:3]\n"
    "ds_read_b64_tr_b16 a[180:181], v209 offset:10272\n"
    "v_mfma_f32_16x16x32_bf16 a[0:3], a[172:175], v[28:31], a[0:3]\n"
    "ds_read_b64_tr_b16 a[182:183], v209 offset:15392\n"
    "s_waitcnt vmcnt(9)\n"
    "v_mfma_f32_16x16x32_bf16 a[4:7], a[160:163], v[32:35], 0\n"
    "buffer_load_dwordx4 v[84:87], v226, s[36:39], s69 offen offset:1024 sc1\n"
    "v_mfma_f32_16x16x32_bf16 a[4:7], a[164:167], v[36:39], a[4:7]\n"
    "ds_read_b64_tr_b16 a[184:185], v209 offset:20512\n"
    "v_mfma_f32_16x16x32_bf16 a[4:7], a[168:171], v[40:43], a[4:7]\n"
    "ds_read_b64_tr_b16 a[186:187], v209 offset:25632\n"
    "v_mfma_f32_16x16x32_bf16 a[4:7], a[172:175], v[44:47], a[4:7]\n"
    "ds_read_b64_tr_b16 a[188:189], v209 offset:30752\n"
    "s_waitcnt vmcnt(6)\n"
    "v_mfma_f32_16x16x32_bf16 a[8:11], a[160:163], v[48:51], 0\n"
    "ds_read_b64_tr_b16 a[190:191], v209 offset:35872\n"
    "v_mfma_f32_16x16x32_bf16 a[8:11], a[164:167], v[52:55], a[8:11]\n"
    "buffer_load_dwordx4 v[88:91], v226, s[36:39], s69 offen offset:2048 sc1\n"
    "v_mfma_f32_16x16x32_bf16 a[8:11], a[168:171], v[56:59], a[8:11]\n"
    "ds_read_b64_tr_b16 a[192:193], v208 offset:64\n"
    "v_mfma_f32_16x16x32_bf16 a[8:11], a[172:175], v[60:63], a[8:11]\n"
    "ds_read_b64_tr_b16 a[194:195], v208 offset:5184\n"
    "s_waitcnt vmcnt(3)\n"
    "v_mfma_f32_16x16x32_bf16 a[12:15], a[160:163], v[64:67], 0\n"
    "ds_read_b64_tr_b16 a[196:197], v208 offset:10304\n"
    "v_mfma_f32_16x16x32_bf16 a[12:15], a[164:167], v[68:71], a[12:15]\n"
    "ds_read_b64_tr_b16 a[198:199], v208 offset:15424\n"
    "v_mfma_f32_16x16x32_bf16 a[12:15], a[168:171], v[72:75], a[12:15]\n"
    "buffer_load_dwordx4 v[92:95], v226, s[36:39], s69 offen offset:3072 sc1\n"
    "v_mfma_f32_16x16x32_bf16 a[12:15], a[172:175], v[76:79], a[12:15]\n"
    "ds_read_b64_tr_b16 a[200:201], v208 offset:20544\n"
    "s_waitcnt lgkmcnt(5)\n"
    "v_mfma_f32_16x16x32_bf16 a[16:19], a[176:179], v[16:19], 0\n"
    "ds_read_b64_tr_b16 a[202:203], v208 offset:25664\n"
    "v_mfma_f32_16x16x32_bf16 a[16:19], a[180:183], v[20:23], a[16:19]\n"
    "ds_read_b64_tr_b16 a[204:205], v208 offset:30784\n"
    "v_mfma_f32_16x16x32_bf16 a[16:19], a[184:187], v[24:27], a[16:19]\n"
    "buffer_load_dwordx4 v[96:99], v226, s[36:39], s70 offen offset:0 sc1\n"
    "v_mfma_f32_16x16x32_bf16 a[16:19], a[188:191], v[28:31], a[16:19]\n"
    "ds_read_b64_tr_b16 a[206:207], v208 offset:35904\n"
    "v_mfma_f32_16x16x32_bf16 a[20:23], a[176:179], v[32:35], 0\n"
    "v_mfma_f32_16x16x32_bf16 a[20:23], a[180:183], v[36:39], a[20:23]\n"
    "v_mfma_f32_16x16x32_bf16 a[20:23], a[184:187], v[40:43], a[20:23]\n"
    "v_mfma_f32_16x16x32_bf16 a[20:23], a[188:191], v[44:47], a[20:23]\n"
    "buffer_load_dwordx4 v[100:103], v226, s[36:39], s70 offen offset:1024 sc1\n"
    "v_mfma_f32_16x16x32_bf16 a[24:27], a[176:179], v[48:51], 0\n"
    "ds_read_b64_tr_b16 a[208:209], v209 offset:96\n"
    "v_mfma_f32_16x16x32_bf16 a[24:27], a[180:183], v[52:55], a[24:27]\n"
    "ds_read_b64_tr_b16 a[210:211], v209 offset:5216\n"
    "v_mfma_f32_16x16x32_bf16 a[24:27], a[184:187], v[56:59], a[24:27]\n"
    "ds_read_b64_tr_b16 a[212:213], v209 offset:10336\n"
    "v_mfma_f32_16x16x32_bf16 a[24:27], a[188:191], v[60:63], a[24:27]\n"
    "ds_read_b64_tr_b16 a[214:215], v209 offset:15456\n"
    "v_mfma_f32_16x16x32_bf16 a[28:31], a[176:179], v[64:67], 0\n"
    "buffer_load_dwordx4 v[104:107], v226, s[36:39], s70 offen offset:2048 sc1\n"
    "v_mfma_f32_16x16x32_bf16 a[28:31], a[180:183], v[68:71], a[28:31]\n"
    "ds_read_b64_tr_b16 a[216:217], v209 offset:20576\n"
    "v_mfma_f32_16x16x32_bf16 a[28:31], a[184:187], v[72:75], a[28:31]\n"
    "ds_read_b64_tr_b16 a[218:219], v209 offset:25696\n"
    "v_mfma_f32_16x16x32_bf16 a[28:31], a[188:191], v[76:79], a[28:31]\n"
    "ds_read_b64_tr_b16 a[220:221], v209 offset:30816\n"
    "s_waitcnt lgkmcnt(7)\n"
    "v_mfma_f32_16x16x32_bf16 a[32:35], a[192:195], v[16:19], 0\n"
    "buffer_load_dwordx4 v[108:111], v226, s[36:39], s70 offen offset:3072 sc1\n"
    "v_mfma_f32_16x16x32_bf16 a[32:35], a[196:199], v[20:23], a[32:35]\n"
    "ds_read_b64_tr_b16 a[222:223], v209 offset:35936\n"
    "v_mfma_f32_16x16x32_bf16 a[32:35], a[200:203], v[24:27], a[32:35]\n"
    "v_mfma_f32_16x16x32_bf16 a[32:35], a[204:207], v[28:31], a[32:35]\n"
    "v_mfma_f32_16x16x32_bf16 a[36:39], a[192:195], v[32:35], 0\n"
    "v_mfma_f32_16x16x32_bf16 a[36:39], a[196:199], v[36:39], a[36:39]\n"
    "buffer_load_dwordx4 v[112:115], v226, s[36:39], s71 offen offset:0 sc1\n"
    "v_mfma_f32_16x16x32_bf16 a[36:39], a[200:203], v[40:43], a[36:39]\n"
    "v_mfma_f32_16x16x32_bf16 a[36:39], a[204:207], v[44:47], a[36:39]\n"
    "v_mfma_f32_16x16x32_bf16 a[40:43], a[192:195], v[48:51], 0\n"
    "ds_read_b64_tr_b16 a[224:225], v208 offset:128\n"
    "v_mfma_f32_16x16x32_bf16 a[40:43], a[196:199], v[52:55], a[40:43]\n"
    "ds_read_b64_tr_b16 a[226:227], v208 offset:5248\n"
    "v_mfma_f32_16x16x32_bf16 a[40:43], a[200:203], v[56:59], a[40:43]\n"
    "buffer_load_dwordx4 v[116:119], v226, s[36:39], s71 offen offset:1024 sc1\n"
    "v_mfma_f32_16x16x32_bf16 a[40:43], a[204:207], v[60:63], a[40:43]\n"
    "ds_read_b64_tr_b16 a[228:229], v208 offset:10368\n"
    "v_mfma_f32_16x16x32_bf16 a[44:47], a[192:195], v[64:67], 0\n"
    "ds_read_b64_tr_b16 a[230:231], v208 offset:15488\n"
    "v_mfma_f32_16x16x32_bf16 a[44:47], a[196:199], v[68:71], a[44:47]\n"
    "ds_read_b64_tr_b16 a[232:233], v208 offset:20608\n"
    "v_mfma_f32_16x16x32_bf16 a[44:47], a[200:203], v[72:75], a[44:47]\n"
    "buffer_load_dwordx4 v[120:123], v226, s[36:39], s71 offen offset:2048 sc1\n"
    "v_mfma_f32_16x16x32_bf16 a[44:47], a[204:207], v[76:79], a[44:47]\n"
    "ds_read_b64_tr_b16 a[234:235], v208 offset:25728\n"
    "s_waitcnt lgkmcnt(6)\n"
    "v_mfma_f32_16x16x32_bf16 a[48:51], a[208:211], v[16:19], 0\n"
    "ds_read_b64_tr_b16 a[236:237], v208 offset:30848\n"
    "v_mfma_f32_16x16x32_bf16 a[48:51], a[212:215], v[20:23], a[48:51]\n"
    "ds_read_b64_tr_b16 a[238:239], v208 offset:35968\n"
    "v_mfma_f32_16x16x32_bf16 a[48:51], a[216:219], v[24:27], a[48:51]\n"
    "v_mfma_f32_16x16x32_bf16 a[48:51], a[220:223], v[28:31], a[48:51]\n"
    "buffer_load_dwordx4 v[124:127], v226, s[36:39], s71 offen offset:3072 sc1\n"
    "v_mfma_f32_16x16x32_bf16 a[52:55], a[208:211], v[32:35], 0\n"
    "v_mfma_f32_16x16x32_bf16 a[52:55], a[212:215], v[36:39], a[52:55]\n"
    "v_mfma_f32_16x16x32_bf16 a[52:55], a[216:219], v[40:43], a[52:55]\n"
    "v_mfma_f32_16x16x32_bf16 a[52:55], a[220:223], v[44:47], a[52:55]\n"
    "v_mfma_f32_16x16x32_bf16 a[56:59], a[208:211], v[48:51], 0\n"
    "buffer_load_dwordx4 v[128:131], v226, s[36:39], s72 offen offset:0 sc1\n"
    "v_mfma_f32_16x16x32_bf16 a[56:59], a[212:215], v[52:55], a[56:59]\n"
    "ds_read_b64_tr_b16 a[160:161], v209 offset:160\n"
    "v_mfma_f32_16x16x32_bf16 a[56:59], a[216:219], v[56:59], a[56:59]\n"
    "ds_read_b64_tr_b16 a[162:163], v209 offset:5280\n"
    "v_mfma_f32_16x16x32_bf16 a[56:59], a[220:223], v[60:63], a[56:59]\n"
    "ds_read_b64_tr_b16 a[164:165], v209 offset:10400\n"
    "v_mfma_f32_16x16x32_bf16 a[60:63], a[208:211], v[64:67], 0\n"
    "buffer_load_dwordx4 v[132:135], v226, s[36:39], s72 offen offset:1024 sc1\n"
    "v_mfma_f32_16x16x32_bf16 a[60:63], a[212:215], v[68:71], a[60:63]\n"
    "ds_read_b64_tr_b16 a[166:167], v209 offset:15520\n"
    "v_mfma_f32_16x16x32_bf16 a[60:63], a[216:219], v[72:75], a[60:63]\n"
    "ds_read_b64_tr_b16 a[168:169], v209 offset:20640\n"
    "v_mfma_f32_16x16x32_bf16 a[60:63], a[220:223], v[76:79], a[60:63]\n"
    "ds_read_b64_tr_b16 a[170:171], v209 offset:25760\n"
    "s_waitcnt lgkmcnt(6)\n"
    "v_mfma_f32_16x16x32_bf16 a[64:67], a[224:227], v[16:19], 0\n"
    "ds_read_b64_tr_b16 a[172:173], v209 offset:30880\n"
    "v_mfma_f32_16x16x32_bf16 a[64:67], a[228:231], v[20:23], a[64:67]\n"
    "buffer_load_dwordx4 v[136:139], v226, s[36:39], s72 offen offset:2048 sc1\n"
    "v_mfma_f32_16x16x32_bf16 a[64:67], a[232:235], v[24:27], a[64:67]\n"
    "ds_read_b64_tr_b16 a[174:175], v209 offset:36000\n"
    "v_mfma_f32_16x16x32_bf16 a[64:67], a[236:239], v[28:31], a[64:67]\n"
    "v_mfma_f32_16x16x32_bf16 a[68:71], a[224:227], v[32:35], 0\n"
    "v_mfma_f32_16x16x32_bf16 a[68:71], a[228:231], v[36:39], a[68:71]\n"
    "v_mfma_f32_16x16x32_bf16 a[68:71], a[232:235], v[40:43], a[68:71]\n"
    "buffer_load_dwordx4 v[140:143], v226, s[36:39], s72 offen offset:3072 sc1\n"
    "v_mfma_f32_16x16x32_bf16 a[68:71], a[236:239], v[44:47], a[68:71]\n"
    "v_mfma_f32_16x16x32_bf16 a[72:75], a[224:227], v[48:51], 0\n"
    "ds_read_b64_tr_b16 a[176:177], v208 offset:192\n"
    "v_mfma_f32_16x16x32_bf16 a[72:75], a[228:231], v[52:55], a[72:75]\n"
    "ds_read_b64_tr_b16 a[178:179], v208 offset:5312\n"
    "v_mfma_f32_16x16x32_bf16 a[72:75], a[232:235], v[56:59], a[72:75]\n"
    "ds_read_b64_tr_b16 a[180:181], v208 offset:10432\n"
    "v_mfma_f32_16x16x32_bf16 a[72:75], a[236:239], v[60:63], a[72:75]\n"
    "ds_read_b64_tr_b16 a[182:183], v208 offset:15552\n"
    "s_add_u32 s69, s69, 4096\n"
    "v_mfma_f32_16x16x32_bf16 a[76:79], a[224:227], v[64:67], 0\n"
    "ds_read_b64_tr_b16 a[184:185], v208 offset:20672\n"
    "s_add_u32 s70, s70, 4096\n"
    "v_mfma_f32_16x16x32_bf16 a[76:79], a[228:231], v[68:71], a[76:79]\n"
    "ds_read_b64_tr_b16 a[186:187], v208 offset:25792\n"
    "s_add_u32 s71, s71, 4096\n"
    "v_mfma_f32_16x16x32_bf16 a[76:79], a[232:235], v[72:75], a[76:79]\n"
    "ds_read_b64_tr_b16 a[188:189], v208 offset:30912\n"
    "s_add_u32 s72, s72, 4096\n"
    "v_mfma_f32_16x16x32_bf16 a[76:79], a[236:239], v[76:79], a[76:79]\n"
    "ds_read_b64_tr_b16 a[190:191], v208 offset:36032\n"
    "buffer_load_dwordx4 v[144:147], v226, s[36:39], s69 offen offset:0 sc1\n"
    "s_waitcnt lgkmcnt(8)\n"
    "v_mfma_f32_16x16x32_bf16 a[80:83], a[160:163], v[16:19], 0\n"
    "buffer_load_dwordx4 v[148:151], v226, s[36:39], s69 offen offset:1024 sc1\n"
    "v_mfma_f32_16x16x32_bf16 a[80:83], a[164:167], v[20:23], a[80:83]\n"
    "v_mfma_f32_16x16x32_bf16 a[80:83], a[168:171], v[24:27], a[80:83]\n"
    "v_mfma_f32_16x16x32_bf16 a[80:83], a[172:175], v[28:31], a[80:83]\n"
    "v_mfma_f32_16x16x32_bf16 a[84:87], a[160:163], v[32:35], 0\n"
    "buffer_load_dwordx4 v[152:155], v226, s[36:39], s69 offen offset:2048 sc1\n"
    "v_mfma_f32_16x16x32_bf16 a[84:87], a[164:167], v[36:39], a[84:87]\n"
    "v_mfma_f32_16x16x32_bf16 a[84:87], a[168:171], v[40:43], a[84:87]\n"
    "v_mfma_f32_16x16x32_bf16 a[84:87], a[172:175], v[44:47], a[84:87]\n"
    "v_mfma_f32_16x16x32_bf16 a[88:91], a[160:163], v[48:51], 0\n"
    "ds_read_b64_tr_b16 a[192:193], v209 offset:224\n"
    "v_mfma_f32_16x16x32_bf16 a[88:91], a[164:167], v[52:55], a[88:91]\n"
    "ds_read_b64_tr_b16 a[194:195], v209 offset:5344\n"
    "buffer_load_dwordx4 v[156:159], v226, s[36:39], s69 offen offset:3072 sc1\n"
    "v_mfma_f32_16x16x32_bf16 a[88:91], a[168:171], v[56:59], a[88:91]\n"
    "ds_read_b64_tr_b16 a[196:197], v209 offset:10464\n"
    "v_mfma_f32_16x16x32_bf16 a[88:91], a[172:175], v[60:63], a[88:91]\n"
    "ds_read_b64_tr_b16 a[198:199], v209 offset:15584\n"
    "v_mfma_f32_16x16x32_bf16 a[92:95], a[160:163], v[64:67], 0\n"
    "ds_read_b64_tr_b16 a[200:201], v209 offset:20704\n"
    "v_mfma_f32_16x16x32_bf16 a[92:95], a[164:167], v[68:71], a[92:95]\n"
    "ds_read_b64_tr_b16 a[202:203], v209 offset:25824\n"
    "v_mfma_f32_16x16x32_bf16 a[92:95], a[168:171], v[72:75], a[92:95]\n"
    "ds_read_b64_tr_b16 a[204:205], v209 offset:30944\n"
    "v_mfma_f32_16x16x32_bf16 a[92:95], a[172:175], v[76:79], a[92:95]\n"
    "ds_read_b64_tr_b16 a[206:207], v209 offset:36064\n"
    "s_waitcnt lgkmcnt(8)\n"
    "v_mfma_f32_16x16x32_bf16 a[96:99], a[176:179], v[16:19], 0\n"
    "buffer_load_dwordx4 v[160:163], v226, s[36:39], s70 offen offset:0 sc1\n"
    "v_mfma_f32_16x16x32_bf16 a[96:99], a[180:183], v[20:23], a[96:99]\n"
    "v_mfma_f32_16x16x32_bf16 a[96:99], a[184:187], v[24:27], a[96:99]\n"
    "buffer_load_dwordx4 v[164:167], v226, s[36:39], s70 offen offset:1024 sc1\n"
    "v_mfma_f32_16x16x32_bf16 a[96:99], a[188:191], v[28:31], a[96:99]\n"
    "v_mfma_f32_16x16x32_bf16 a[100:103], a[176:179], v[32:35], 0\n"
    "v_mfma_f32_16x16x32_bf16 a[100:103], a[180:183], v[36:39], a[100:103]\n"
    "v_mfma_f32_16x16x32_bf16 a[100:103], a[184:187], v[40:43], a[100:103]\n"
    "v_mfma_f32_16x16x32_bf16 a[100:103], a[188:191], v[44:47], a[100:103]\n"
    "buffer_load_dwordx4 v[168:171], v226, s[36:39], s70 offen offset:2048 sc1\n"
    "v_mfma_f32_16x16x32_bf16 a[104:107], a[176:179], v[48:51], 0\n"
    "ds_read_b64_tr_b16 a[208:209], v208 offset:256\n"
    "v_mfma_f32_16x16x32_bf16 a[104:107], a[180:183], v[52:55], a[104:107]\n"
    "ds_read_b64_tr_b16 a[210:211], v208 offset:5376\n"
    "v_mfma_f32_16x16x32_bf16 a[104:107], a[184:187], v[56:59], a[104:107]\n"
    "ds_read_b64_tr_b16 a[212:213], v208 offset:10496\n"
    "v_mfma_f32_16x16x32_bf16 a[104:107], a[188:191], v[60:63], a[104:107]\n"
    "ds_read_b64_tr_b16 a[214:215], v208 offset:15616\n"
    "buffer_load_dwordx4 v[172:175], v226, s[36:39], s70 offen offset:3072 sc1\n"
    "v_mfma_f32_16x16x32_bf16 a[108:111], a[176:179], v[64:67], 0\n"
    "ds_read_b64_tr_b16 a[216:217], v208 offset:20736\n"
    "v_mfma_f32_16x16x32_bf16 a[108:111], a[180:183], v[68:71], a[108:111]\n"
    "ds_read_b64_tr_b16 a[218:219], v208 offset:25856\n"
    "v_mfma_f32_16x16x32_bf16 a[108:111], a[184:187], v[72:75], a[108:111]\n"
    "ds_read_b64_tr_b16 a[220:221], v208 offset:30976\n"
    "v_mfma_f32_16x16x32_bf16 a[108:111], a[188:191], v[76:79], a[108:111]\n"
    "ds_read_b64_tr_b16 a[222:223], v208 offset:36096\n"
    "s_waitcnt lgkmcnt(8)\n"
    "v_mfma_f32_16x16x32_bf16 a[112:115], a[192:195], v[16:19], 0\n"
    "buffer_load_dwordx4 v[176:179], v226, s[36:39], s71 offen offset:0 sc1\n"
    "v_mfma_f32_16x16x32_bf16 a[112:115], a[196:199], v[20:23], a[112:115]\n"
    "v_mfma_f32_16x16x32_bf16 a[112:115], a[200:203], v[24:27], a[112:115]\n"
    "v_mfma_f32_16x16x32_bf16 a[112:115], a[204:207], v[28:31], a[112:115]\n"
    "v_mfma_f32_16x16x32_bf16 a[116:119], a[192:195], v[32:35], 0\n"
    "v_mfma_f32_16x16x32_bf16 a[116:119], a[196:199], v[36:39], a[116:119]\n"
    "buffer_load_dwordx4 v[180:183], v226, s[36:39], s71 offen offset:1024 sc1\n"
    "v_mfma_f32_16x16x32_bf16 a[116:119], a[200:203], v[40:43], a[116:119]\n"
    "v_mfma_f32_16x16x32_bf16 a[116:119], a[204:207], v[44:47], a[116:119]\n"
    "v_mfma_f32_16x16x32_bf16 a[120:123], a[192:195], v[48:51], 0\n"
    "ds_read_b64_tr_b16 a[224:225], v209 offset:288\n"
    "v_mfma_f32_16x16x32_bf16 a[120:123], a[196:199], v[52:55], a[120:123]\n"
    "ds_read_b64_tr_b16 a[226:227], v209 offset:5408\n"
    "buffer_load_dwordx4 v[184:187], v226, s[36:39], s71 offen offset:2048 sc1\n"
    "v_mfma_f32_16x16x32_bf16 a[120:123], a[200:203], v[56:59], a[120:123]\n"
    "ds_read_b64_tr_b16 a[228:229], v209 offset:10528\n"
    "v_mfma_f32_16x16x32_bf16 a[120:123], a[204:207], v[60:63], a[120:123]\n"
    "ds_read_b64_tr_b16 a[230:231], v209 offset:15648\n"
    "v_mfma_f32_16x16x32_bf16 a[124:127], a[192:195], v[64:67], 0\n"
    "ds_read_b64_tr_b16 a[232:233], v209 offset:20768\n"
    "v_mfma_f32_16x16x32_bf16 a[124:127], a[196:199], v[68:71], a[124:127]\n"
    "ds_read_b64_tr_b16 a[234:235], v209 offset:25888\n"
    "v_mfma_f32_16x16x32_bf16 a[124:127], a[200:203], v[72:75], a[124:127]\n"
    "ds_read_b64_tr_b16 a[236:237], v209 offset:31008\n"
    "v_mfma_f32_16x16x32_bf16 a[124:127], a[204:207], v[76:79], a[124:127]\n"
    "ds_read_b64_tr_b16 a[238:239], v209 offset:36128\n"
    "s_waitcnt lgkmcnt(8)\n"
    "v_mfma_f32_16x16x32_bf16 a[128:131], a[208:211], v[16:19], 0\n"
    "buffer_load_dwordx4 v[188:191], v226, s[36:39], s71 offen offset:3072 sc1\n"
    "v_mfma_f32_16x16x32_bf16 a[128:131], a[212:215], v[20:23], a[128:131]\n"
    "v_mfma_f32_16x16x32_bf16 a[128:131], a[216:219], v[24:27], a[128:131]\n"
    "v_mfma_f32_16x16x32_bf16 a[128:131], a[220:223], v[28:31], a[128:131]\n"
    "buffer_load_dwordx4 v[192:195], v226, s[36:39], s72 offen offset:0 sc1\n"
    "v_mfma_f32_16x16x32_bf16 a[132:135], a[208:211], v[32:35], 0\n"
    "v_mfma_f32_16x16x32_bf16 a[132:135], a[212:215], v[36:39], a[132:135]\n"
    "v_mfma_f32_16x16x32_bf16 a[132:135], a[216:219], v[40:43], a[132:135]\n"
    "v_mfma_f32_16x16x32_bf16 a[132:135], a[220:223], v[44:47], a[132:135]\n"
    "buffer_load_dwordx4 v[196:199], v226, s[36:39], s72 offen offset:1024 sc1\n"
    "v_mfma_f32_16x16x32_bf16 a[136:139], a[208:211], v[48:51], 0\n"
    "v_mfma_f32_16x16x32_bf16 a[136:139], a[212:215], v[52:55], a[136:139]\n"
    "v_mfma_f32_16x16x32_bf16 a[136:139], a[216:219], v[56:59], a[136:139]\n"
    "v_mfma_f32_16x16x32_bf16 a[136:139], a[220:223], v[60:63], a[136:139]\n"
    "v_mfma_f32_16x16x32_bf16 a[140:143], a[208:211], v[64:67], 0\n"
    "buffer_load_dwordx4 v[200:203], v226, s[36:39], s72 offen offset:2048 sc1\n"
    "v_mfma_f32_16x16x32_bf16 a[140:143], a[212:215], v[68:71], a[140:143]\n"
    "v_mfma_f32_16x16x32_bf16 a[140:143], a[216:219], v[72:75], a[140:143]\n"
    "v_mfma_f32_16x16x32_bf16 a[140:143], a[220:223], v[76:79], a[140:143]\n"
    "s_waitcnt lgkmcnt(0)\n"
    "s_barrier\n"
    "v_mfma_f32_16x16x32_bf16 a[144:147], a[224:227], v[16:19], 0\n"
    "ds_read_b64_tr_b16 a[160:161], v210 offset:0\n"
    "v_mfma_f32_16x16x32_bf16 a[144:147], a[228:231], v[20:23], a[144:147]\n"
    "ds_read_b64_tr_b16 a[162:163], v210 offset:5120\n"
    "buffer_load_dwordx4 v[204:207], v226, s[36:39], s72 offen offset:3072 sc1\n"
    "v_mfma_f32_16x16x32_bf16 a[144:147], a[232:235], v[24:27], a[144:147]\n"
    "ds_read_b64_tr_b16 a[164:165], v210 offset:10240\n"
    "v_mfma_f32_16x16x32_bf16 a[144:147], a[236:239], v[28:31], a[144:147]\n"
    "ds_read_b64_tr_b16 a[166:167], v210 offset:15360\n"
    "v_mfma_f32_16x16x32_bf16 a[148:151], a[224:227], v[32:35], 0\n"
    "ds_read_b64_tr_b16 a[168:169], v210 offset:20480\n"
    "v_mfma_f32_16x16x32_bf16 a[148:151], a[228:231], v[36:39], a[148:151]\n"
    "ds_read_b64_tr_b16 a[170:171], v210 offset:25600\n"
    "v_mfma_f32_16x16x32_bf16 a[148:151], a[232:235], v[40:43], a[148:151]\n"
    "ds_read_b64_tr_b16 a[172:173], v210 offset:30720\n"
    "v_mfma_f32_16x16x32_bf16 a[148:151], a[236:239], v[44:47], a[148:151]\n"
    "ds_read_b64_tr_b16 a[174:175], v210 offset:35840\n"
    "v_mfma_f32_16x16x32_bf16 a[152:155], a[224:227], v[48:51], 0\n"
    "s_add_u32 s69, s69, 4096\n"
    "s_add_u32 s70, s70, 4096\n"
    "s_add_u32 s71, s71, 4096\n"
    "s_add_u32 s72, s72, 4096\n"
    "v_mfma_f32_16x16x32_bf16 a[152:155], a[228:231], v[52:55], a[152:155]\n"
    "v_mfma_f32_16x16x32_bf16 a[152:155], a[232:235], v[56:59], a[152:155]\n"
    "v_mfma_f32_16x16x32_bf16 a[152:155], a[236:239], v[60:63], a[152:155]\n"
    "v_mfma_f32_16x16x32_bf16 a[156:159], a[224:227], v[64:67], 0\n"
    "s_sub_u32 s74, s74, 1\n"
    "s_cmp_eq_u32 s74, 0\n"
    "s_cselect_b32 s69, s75, s69\n"
    "s_cselect_b32 s70, s75, s70\n"
    "s_cselect_b32 s71, s75, s71\n"
    "s_cselect_b32 s72, s75, s72\n"
    "v_mfma_f32_16x16x32_bf16 a[156:159], a[228:231], v[68:71], a[156:159]\n"
    "v_mfma_f32_16x16x32_bf16 a[156:159], a[232:235], v[72:75], a[156:159]\n"
    "v_mfma_f32_16x16x32_bf16 a[156:159], a[236:239], v[76:79], a[156:159]\n"
    "s_waitcnt lgkmcnt(0)\n"
    "ds_read_b64_tr_b16 a[176:177], v211 offset:32\n"
    "s_waitcnt vmcnt(16)\n"
    "v_mfma_f32_16x16x32_bf16 a[0:3], a[160:163], v[80:83], a[0:3]\n"
    "ds_read_b64_tr_b16 a[178:179], v211 offset:5152\n"
    "s_add_u32 m0, s78, 0\n"
    "v_mfma_f32_16x16x32_bf16 a[0:3], a[164:167], v[84:87], a[0:3]\n"
    "ds_read_b64_tr_b16 a[180:181], v211 offset:10272\n"
    "buffer_load_dwordx4 v214, s[40:43], s73 offen offset:0 lds\n"
    "v_mfma_f32_16x16x32_bf16 a[0:3], a[168:171], v[88:91], a[0:3]\n"
    "ds_read_b64_tr_b16 a[182:183], v211 offset:15392\n"
    "v_mfma_f32_16x16x32_bf16 a[0:3], a[172:175], v[92:95], a[0:3]\n"
    "ds_read_b64_tr_b16 a[184:185], v211 offset:20512\n"
    "v_mfma_f32_16x16x32_bf16 a[4:7], a[160:163], v[96:99], a[4:7]\n"
    "ds_read_b64_tr_b16 a[186:187], v211 offset:25632\n"
    "v_mfma_f32_16x16x32_bf16 a[4:7], a[164:167], v[100:103], a[4:7]\n"
    "ds_read_b64_tr_b16 a[188:189], v211 offset:30752\n"
    "v_mfma_f32_16x16x32_bf16 a[4:7], a[168:171], v[104:107], a[4:7]\n"
    "ds_read_b64_tr_b16 a[190:191], v211 offset:35872\n"
    "v_mfma_f32_16x16x32_bf16 a[4:7], a[172:175], v[108:111], a[4:7]\n"
    "buffer_load_dwordx4 v215, s[40:43], s73 offen offset:1024 lds\n"
    "v_mfma_f32_16x16x32_bf16 a[8:11], a[160:163], v[112:115], a[8:11]\n"
    "ds_read_b64_tr_b16 a[192:193], v210 offset:64\n"
    "v_mfma_f32_16x16x32_bf16 a[8:11], a[164:167], v[116:119], a[8:11]\n"
    "ds_read_b64_tr_b16 a[194:195], v210 offset:5184\n"
    "v_mfma_f32_16x16x32_bf16 a[8:11], a[168:171], v[120:123], a[8:11]\n"
    "ds_read_b64_tr_b16 a[196:197], v210 offset:10304\n"
    "v_mfma_f32_16x16x32_bf16 a[8:11], a[172:175], v[124:127], a[8:11]\n"
    "ds_read_b64_tr_b16 a[198:199], v210 offset:15424\n"
    "buffer_load_dwordx4 v216, s[40:43], s73 offen offset:2048 lds\n"
    "v_mfma_f32_16x16x32_bf16 a[12:15], a[160:163], v[128:131], a[12:15]\n"
    "ds_read_b64_tr_b16 a[200:201], v210 offset:20544\n"
    "v_mfma_f32_16x16x32_bf16 a[12:15], a[164:167], v[132:135], a[12:15]\n"
    "ds_read_b64_tr_b16 a[202:203], v210 offset:25664\n"
    "v_mfma_f32_16x16x32_bf16 a[12:15], a[168:171], v[136:139], a[12:15]\n"
    "ds_read_b64_tr_b16 a[204:205], v210 offset:30784\n"
    "v_mfma_f32_16x16x32_bf16 a[12:15], a[172:175], v[140:143], a[12:15]\n"
    "ds_read_b64_tr_b16 a[206:207], v210 offset:35904\n"
    "s_waitcnt lgkmcnt(8)\n"
    "v_mfma_f32_16x16x32_bf16 a[16:19], a[176:179], v[80:83], a[16:19]\n"
    "v_mfma_f32_16x16x32_bf16 a[16:19], a[180:183], v[84:87], a[16:19]\n"
    "buffer_load_dwordx4 v217, s[40:43], s73 offen offset:3072 lds\n"
    "v_mfma_f32_16x16x32_bf16 a[16:19], a[184:187], v[88:91], a[16:19]\n"
    "v_mfma_f32_16x16x32_bf16 a[16:19], a[188:191], v[92:95], a[16:19]\n"
    "v_mfma_f32_16x16x32_bf16 a[20:23], a[176:179], v[96:99], a[20:23]\n"
    "v_mfma_f32_16x16x32_bf16 a[20:23], a[180:183], v[100:103], a[20:23]\n"
    "v_mfma_f32_16x16x32_bf16 a[20:23], a[184:187], v[104:107], a[20:23]\n"
    "v_mfma_f32_16x16x32_bf16 a[20:23], a[188:191], v[108:111], a[20:23]\n"
    "s_add_u32 m0, s78, 4096\n"
    "s_nop 0\n"
    "buffer_load_dwordx4 v218, s[40:43], s73 offen offset:0 lds\n"
    "v_mfma_f32_16x16x32_bf16 a[24:27], a[176:179], v[112:115], a[24:27]\n"
    "ds_read_b64_tr_b16 a[208:209], v211 offset:96\n"
    "v_mfma_f32_16x16x32_bf16 a[24:27], a[180:183], v[116:119], a[24:27]\n"
    "ds_read_b64_tr_b16 a[210:211], v211 offset:5216\n"
    "v_mfma_f32_16x16x32_bf16 a[24:27], a[184:187], v[120:123], a[24:27]\n"
    "ds_read_b64_tr_b16 a[212:213], v211 offset:10336\n"
    "v_mfma_f32_16x16x32_bf16 a[24:27], a[188:191], v[124:127], a[24:27]\n"
    "ds_read_b64_tr_b16 a[214:215], v211 offset:15456\n"
    "v_mfma_f32_16x16x32_bf16 a[28:31], a[176:179], v[128:131], a[28:31]\n"
    "ds_read_b64_tr_b16 a[216:217], v211 offset:20576\n"
    "v_mfma_f32_16x16x32_bf16 a[28:31], a[180:183], v[132:135], a[28:31]\n"
    "ds_read_b64_tr_b16 a[218:219], v211 offset:25696\n"
    "v_mfma_f32_16x16x32_bf16 a[28:31], a[184:187], v[136:139], a[28:31]\n"
    "ds_read_b64_tr_b16 a[220:221], v211 offset:30816\n"
    "v_mfma_f32_16x16x32_bf16 a[28:31], a[188:191], v[140:143], a[28:31]\n"
    "ds_read_b64_tr_b16 a[222:223], v211 offset:35936\n"
    "s_waitcnt lgkmcnt(8)\n"
    "v_mfma_f32_16x16x32_bf16 a[32:35], a[192:195], v[80:83], a[32:35]\n"
    "buffer_load_dwordx4 v219, s[40:43], s73 offen offset:1024 lds\n"
    "v_mfma_f32_16x16x32_bf16 a[32:35], a[196:199], v[84:87], a[32:35]\n"
    "v_mfma_f32_16x16x32_bf16 a[32:35], a[200:203], v[88:91], a[32:35]\n"
    "buffer_load_dwordx4 v220, s[40:43], s73 offen offset:2048 lds\n"
    "v_mfma_f32_16x16x32_bf16 a[32:35], a[204:207], v[92:95], a[32:35]\n"
    "v_mfma_f32_16x16x32_bf16 a[36:39], a[192:195], v[96:99], a[36:39]\n"
    "v_mfma_f32_16x16x32_bf16 a[36:39], a[196:199], v[100:103], a[36:39]\n"
    "v_mfma_f32_16x16x32_bf16 a[36:39], a[200:203], v[104:107], a[36:39]\n"
    "v_mfma_f32_16x16x32_bf16 a[36:39], a[204:207], v[108:111], a[36:39]\n"
    "v_mfma_f32_16x16x32_bf16 a[40:43], a[192:195], v[112:115], a[40:43]\n"
    "ds_read_b64_tr_b16 a[224:225], v210 offset:128\n"
    "buffer_load_dwordx4 v221, s[40:43], s73 offen offset:3072 lds\n"
    "v_mfma_f32_16x16x32_bf16 a[40:43], a[196:199], v[116:119], a[40:43]\n"
    "ds_read_b64_tr_b16 a[226:227], v210 offset:5248\n"
    "v_mfma_f32_16x16x32_bf16 a[40:43], a[200:203], v[120:123], a[40:43]\n"
    "ds_read_b64_tr_b16 a[228:229], v210 offset:10368\n"
    "v_mfma_f32_16x16x32_bf16 a[40:43], a[204:207], v[124:127], a[40:43]\n"
    "ds_read_b64_tr_b16 a[230:231], v210 offset:15488\n"
    "v_mfma_f32_16x16x32_bf16 a[44:47], a[192:195], v[128:131], a[44:47]\n"
    "ds_read_b64_tr_b16 a[232:233], v210 offset:20608\n"
    "v_mfma_f32_16x16x32_bf16 a[44:47], a[196:199], v[132:135], a[44:47]\n"
    "ds_read_b64_tr_b16 a[234:235], v210 offset:25728\n"
    "v_mfma_f32_16x16x32_bf16 a[44:47], a[200:203], v[136:139], a[44:47]\n"
    "ds_read_b64_tr_b16 a[236:237], v210 offset:30848\n"
    "s_add_u32 m0, s78, 8192\n"
    "v_mfma_f32_16x16x32_bf16 a[44:47], a[204:207], v[140:143], a[44:47]\n"
    "ds_read_b64_tr_b16 a[238:239], v210 offset:35968\n"
    "s_waitcnt lgkmcnt(8)\n"
    "v_mfma_f32_16x16x32_bf16 a[48:51], a[208:211], v[80:83], a[48:51]\n"
    "buffer_load_dwordx4 v222, s[40:43], s73 offen offset:0 lds\n"
    "v_mfma_f32_16x16x32_bf16 a[48:51], a[212:215], v[84:87], a[48:51]\n"
    "v_mfma_f32_16x16x32_bf16 a[48:51], a[216:219], v[88:91], a[48:51]\n"
    "v_mfma_f32_16x16x32_bf16 a[48:51], a[220:223], v[92:95], a[48:51]\n"
    "buffer_load_dwordx4 v223, s[40:43], s73 offen offset:1024 lds\n"
    "v_mfma_f32_16x16x32_bf16 a[52:55], a[208:211], v[96:99], a[52:55]\n"
    "v_mfma_f32_16x16x32_bf16 a[52:55], a[212:215], v[100:103], a[52:55]\n"
    "v_mfma_f32_16x16x32_bf16 a[52:55], a[216:219], v[104:107], a[52:55]\n"
    "v_mfma_f32_16x16x32_bf16 a[52:55], a[220:223], v[108:111], a[52:55]\n"
    "v_mfma_f32_16x16x32_bf16 a[56:59], a[208:211], v[112:115], a[56:59]\n"
    "ds_read_b64_tr_b16 a[160:161], v211 offset:160\n"
    "v_mfma_f32_16x16x32_bf16 a[56:59], a[212:215], v[116:119], a[56:59]\n"
    "ds_read_b64_tr_b16 a[162:163], v211 offset:5280\n"
    "s_add_u32 s73, s73, 163840\n"
    "v_mfma_f32_16x16x32_bf16 a[56:59], a[216:219], v[120:123], a[56:59]\n"
    "ds_read_b64_tr_b16 a[164:165], v211 offset:10400\n"
    "buffer_load_dwordx4 v[16:19], v226, s[36:39], s69 offen offset:0 sc1\n"
    "v_mfma_f32_16x16x32_bf16 a[56:59], a[220:223], v[124:127], a[56:59]\n"
    "ds_read_b64_tr_b16 a[166:167], v211 offset:15520\n"
    "v_mfma_f32_16x16x32_bf16 a[60:63], a[208:211], v[128:131], a[60:63]\n"
    "ds_read_b64_tr_b16 a[168:169], v211 offset:20640\n"
    "v_mfma_f32_16x16x32_bf16 a[60:63], a[212:215], v[132:135], a[60:63]\n"
    "ds_read_b64_tr_b16 a[170:171], v211 offset:25760\n"
    "v_mfma_f32_16x16x32_bf16 a[60:63], a[216:219], v[136:139], a[60:63]\n"
    "ds_read_b64_tr_b16 a[172:173], v211 offset:30880\n"
    "v_mfma_f32_16x16x32_bf16 a[60:63], a[220:223], v[140:143], a[60:63]\n"
    "ds_read_b64_tr_b16 a[174:175], v211 offset:36000\n"
    "s_waitcnt lgkmcnt(8)\n"
    "v_mfma_f32_16x16x32_bf16 a[64:67], a[224:227], v[80:83], a[64:67]\n"
    "buffer_load_dwordx4 v[20:23], v226, s[36:39], s69 offen offset:1024 sc1\n"
    "v_mfma_f32_16x16x32_bf16 a[64:67], a[228:231], v[84:87], a[64:67]\n"
    "v_mfma_f32_16x16x32_bf16 a[64:67], a[232:235], v[88:91], a[64:67]\n"
    "v_mfma_f32_16x16x32_bf16 a[64:67], a[236:239], v[92:95], a[64:67]\n"
    "v_mfma_f32_16x16x32_bf16 a[68:71], a[224:227], v[96:99], a[68:71]\n"
    "v_mfma_f32_16x16x32_bf16 a[68:71], a[228:231], v[100:103], a[68:71]\n"
    "buffer_load_dwordx4 v[24:27], v226, s[36:39], s69 offen offset:2048 sc1\n"
    "v_mfma_f32_16x16x32_bf16 a[68:71], a[232:235], v[104:107], a[68:71]\n"
    "v_mfma_f32_16x16x32_bf16 a[68:71], a[236:239], v[108:111], a[68:71]\n"
    "v_mfma_f32_16x16x32_bf16 a[72:75], a[224:227], v[112:115], a[72:75]\n"
    "ds_read_b64_tr_b16 a[176:177], v210 offset:192\n"
    "v_mfma_f32_16x16x32_bf16 a[72:75], a[228:231], v[116:119], a[72:75]\n"
    "ds_read_b64_tr_b16 a[178:179], v210 offset:5312\n"
    "v_mfma_f32_16x16x32_bf16 a[72:75], a[232:235], v[120:123], a[72:75]\n"
    "ds_read_b64_tr_b16 a[180:181], v210 offset:10432\n"
    "v_mfma_f32_16x16x32_bf16 a[72:75], a[236:239], v[124:127], a[72:75]\n"
    "ds_read_b64_tr_b16 a[182:183], v210 offset:15552\n"
    "buffer_load_dwordx4 v[28:31], v226, s[36:39], s69 offen offset:3072 sc1\n"
    "v_mfma_f32_16x16x32_bf16 a[76:79], a[224:227], v[128:131], a[76:79]\n"
    "ds_read_b64_tr_b16 a[184:185], v210 offset:20672\n"
    "v_mfma_f32_16x16x32_bf16 a[76:79], a[228:231], v[132:135], a[76:79]\n"
    "ds_read_b64_tr_b16 a[186:187], v210 offset:25792\n"
    "v_mfma_f32_16x16x32_bf16 a[76:79], a[232:235], v[136:139], a[76:79]\n"
    "ds_read_b64_tr_b16 a[188:189], v210 offset:30912\n"
    "v_mfma_f32_16x16x32_bf16 a[76:79], a[236:239], v[140:143], a[76:79]\n"
    "ds_read_b64_tr_b16 a[190:191], v210 offset:36032\n"
    "s_waitcnt lgkmcnt(8)\n"
    "v_mfma_f32_16x16x32_bf16 a[80:83], a[160:163], v[80:83], a[80:83]\n"
    "buffer_load_dwordx4 v[32:35], v226, s[36:39], s70 offen offset:0 sc1\n"
    "v_mfma_f32_16x16x32_bf16 a[80:83], a[164:167], v[84:87], a[80:83]\n"
    "v_mfma_f32_16x16x32_bf16 a[80:83], a[168:171], v[88:91], a[80:83]\n"
    "v_mfma_f32_16x16x32_bf16 a[80:83], a[172:175], v[92:95], a[80:83]\n"
    "v_mfma_f32_16x16x32_bf16 a[84:87], a[160:163], v[96:99], a[84:87]\n"
    "v_mfma_f32_16x16x32_bf16 a[84:87], a[164:167], v[100:103], a[84:87]\n"
    "v_mfma_f32_16x16x32_bf16 a[84:87], a[168:171], v[104:107], a[84:87]\n"
    "buffer_load_dwordx4 v[36:39], v226, s[36:39], s70 offen offset:1024 sc1\n"
    "v_mfma_f32_16x16x32_bf16 a[84:87], a[172:175], v[108:111], a[84:87]\n"
    "v_mfma_f32_16x16x32_bf16 a[88:91], a[160:163], v[112:115], a[88:91]\n"
    "ds_read_b64_tr_b16 a[192:193], v211 offset:224\n"
    "v_mfma_f32_16x16x32_bf16 a[88:91], a[164:167], v[116:119], a[88:91]\n"
    "ds_read_b64_tr_b16 a[194:195], v211 offset:5344\n"
    "v_mfma_f32_16x16x32_bf16 a[88:91], a[168:171], v[120:123], a[88:91]\n"
    "ds_read_b64_tr_b16 a[196:197], v211 offset:10464\n"
    "v_mfma_f32_16x16x32_bf16 a[88:91], a[172:175], v[124:127], a[88:91]\n"
    "ds_read_b64_tr_b16 a[198:199], v211 offset:15584\n"
    "v_mfma_f32_16x16x32_bf16 a[92:95], a[160:163], v[128:131], a[92:95]\n"
    "ds_read_b64_tr_b16 a[200:201], v211 offset:20704\n"
    "v_mfma_f32_16x16x32_bf16 a[92:95], a[164:167], v[132:135], a[92:95]\n"
    "ds_read_b64_tr_b16 a[202:203], v211 offset:25824\n"
    "v_mfma_f32_16x16x32_bf16 a[92:95], a[168:171], v[136:139], a[92:95]\n"
    "ds_read_b64_tr_b16 a[204:205], v211 offset:30944\n"
    "v_mfma_f32_16x16x32_bf16 a[92:95], a[172:175], v[140:143], a[92:95]\n"
    "ds_read_b64_tr_b16 a[206:207], v211 offset:36064\n"
    "s_waitcnt lgkmcnt(8)\n"
    "v_mfma_f32_16x16x32_bf16 a[96:99], a[176:179], v[80:83], a[96:99]\n"
    "buffer_load_dwordx4 v[40:43], v226, s[36:39], s70 offen offset:2048 sc1\n"
    "v_mfma_f32_16x16x32_bf16 a[96:99], a[180:183], v[84:87], a[96:99]\n"
    "v_mfma_f32_16x16x32_bf16 a[96:99], a[184:187], v[88:91], a[96:99]\n"
    "buffer_load_dwordx4 v[44:47], v226, s[36:39], s70 offen offset:3072 sc1\n"
    "v_mfma_f32_16x16x32_bf16 a[96:99], a[188:191], v[92:95], a[96:99]\n"
    "v_mfma_f32_16x16x32_bf16 a[100:103], a[176:179], v[96:99], a[100:103]\n"
    "v_mfma_f32_16x16x32_bf16 a[100:103], a[180:183], v[100:103], a[100:103]\n"
    "v_mfma_f32_16x16x32_bf16 a[100:103], a[184:187], v[104:107], a[100:103]\n"
    "v_mfma_f32_16x16x32_bf16 a[100:103], a[188:191], v[108:111], a[100:103]\n"
    "buffer_load_dwordx4 v[48:51], v226, s[36:39], s71 offen offset:0 sc1\n"
    "v_mfma_f32_16x16x32_bf16 a[104:107], a[176:179], v[112:115], a[104:107]\n"
    "ds_read_b64_tr_b16 a[208:209], v210 offset:256\n"
    "v_mfma_f32_16x16x32_bf16 a[104:107], a[180:183], v[116:119], a[104:107]\n"
    "ds_read_b64_tr_b16 a[210:211], v210 offset:5376\n"
    "v_mfma_f32_16x16x32_bf16 a[104:107], a[184:187], v[120:123], a[104:107]\n"
    "ds_read_b64_tr_b16 a[212:213], v210 offset:10496\n"
    "v_mfma_f32_16x16x32_bf16 a[104:107], a[188:191], v[124:127], a[104:107]\n"
    "ds_read_b64_tr_b16 a[214:215], v210 offset:15616\n"
    "v_mfma_f32_16x16x32_bf16 a[108:111], a[176:179], v[128:131], a[108:111]\n"
    "ds_read_b64_tr_b16 a[216:217], v210 offset:20736\n"
    "v_mfma_f32_16x16x32_bf16 a[108:111], a[180:183], v[132:135], a[108:111]\n"
    "ds_read_b64_tr_b16 a[218:219], v210 offset:25856\n"
    "v_mfma_f32_16x16x32_bf16 a[108:111], a[184:187], v[136:139], a[108:111]\n"
    "ds_read_b64_tr_b16 a[220:221], v210 offset:30976\n"
    "v_mfma_f32_16x16x32_bf16 a[108:111], a[188:191], v[140:143], a[108:111]\n"
    "ds_read_b64_tr_b16 a[222:223], v210 offset:36096\n"
    "s_waitcnt lgkmcnt(8)\n"
    "v_mfma_f32_16x16x32_bf16 a[112:115], a[192:195], v[80:83], a[112:115]\n"
    "buffer_load_dwordx4 v[52:55], v226, s[36:39], s71 offen offset:1024 sc1\n"
    "v_mfma_f32_16x16x32_bf16 a[112:115], a[196:199], v[84:87], a[112:115]\n"
    "v_mfma_f32_16x16x32_bf16 a[112:115], a[200:203], v[88:91], a[112:115]\n"
    "v_mfma_f32_16x16x32_bf16 a[112:115], a[204:207], v[92:95], a[112:115]\n"
    "buffer_load_dwordx4 v[56:59], v226, s[36:39], s71 offen offset:2048 sc1\n"
    "v_mfma_f32_16x16x32_bf16 a[116:119], a[192:195], v[96:99], a[116:119]\n"
    "v_mfma_f32_16x16x32_bf16 a[116:119], a[196:199], v[100:103], a[116:119]\n"
    "v_mfma_f32_16x16x32_bf16 a[116:119], a[200:203], v[104:107], a[116:119]\n"
    "v_mfma_f32_16x16x32_bf16 a[116:119], a[204:207], v[108:111], a[116:119]\n"
    "v_mfma_f32_16x16x32_bf16 a[120:123], a[192:195], v[112:115], a[120:123]\n"
    "ds_read_b64_tr_b16 a[224:225], v211 offset:288\n"
    "v_mfma_f32_16x16x32_bf16 a[120:123], a[196:199], v[116:119], a[120:123]\n"
    "ds_read_b64_tr_b16 a[226:227], v211 offset:5408\n"
    "buffer_load_dwordx4 v[60:63], v226, s[36:39], s71 offen offset:3072 sc1\n"
    "v_mfma_f32_16x16x32_bf16 a[120:123], a[200:203], v[120:123], a[120:123]\n"
    "ds_read_b64_tr_b16 a[228:229], v211 offset:10528\n"
    "v_mfma_f32_16x16x32_bf16 a[120:123], a[204:207], v[124:127], a[120:123]\n"
    "ds_read_b64_tr_b16 a[230:231], v211 offset:15648\n"
    "v_mfma_f32_16x16x32_bf16 a[124:127], a[192:195], v[128:131], a[124:127]\n"
    "ds_read_b64_tr_b16 a[232:233], v211 offset:20768\n"
    "v_mfma_f32_16x16x32_bf16 a[124:127], a[196:199], v[132:135], a[124:127]\n"
    "ds_read_b64_tr_b16 a[234:235], v211 offset:25888\n"
    "v_mfma_f32_16x16x32_bf16 a[124:127], a[200:203], v[136:139], a[124:127]\n"
    "ds_read_b64_tr_b16 a[236:237], v211 offset:31008\n"
    "v_mfma_f32_16x16x32_bf16 a[124:127], a[204:207], v[140:143], a[124:127]\n"
    "ds_read_b64_tr_b16 a[238:239], v211 offset:36128\n"
    "s_waitcnt lgkmcnt(8)\n"
    "v_mfma_f32_16x16x32_bf16 a[128:131], a[208:211], v[80:83], a[128:131]\n"
    "buffer_load_dwordx4 v[64:67], v226, s[36:39], s72 offen offset:0 sc1\n"
    "v_mfma_f32_16x16x32_bf16 a[128:131], a[212:215], v[84:87], a[128:131]\n"
    "v_mfma_f32_16x16x32_bf16 a[128:131], a[216:219], v[88:91], a[128:131]\n"
    "v_mfma_f32_16x16x32_bf16 a[128:131], a[220:223], v[92:95], a[128:131]\n"
    "v_mfma_f32_16x16x32_bf16 a[132:135], a[208:211], v[96:99], a[132:135]\n"
    "buffer_load_dwordx4 v[68:71], v226, s[36:39], s72 offen offset:1024 sc1\n"
    "v_mfma_f32_16x16x32_bf16 a[132:135], a[212:215], v[100:103], a[132:135]\n"
    "v_mfma_f32_16x16x32_bf16 a[132:135], a[216:219], v[104:107], a[132:135]\n"
    "v_mfma_f32_16x16x32_bf16 a[132:135], a[220:223], v[108:111], a[132:135]\n"
    "v_mfma_f32_16x16x32_bf16 a[136:139], a[208:211], v[112:115], a[136:139]\n"
    "v_mfma_f32_16x16x32_bf16 a[136:139], a[212:215], v[116:119], a[136:139]\n"
    "v_mfma_f32_16x16x32_bf16 a[136:139], a[216:219], v[120:123], a[136:139]\n"
    "buffer_load_dwordx4 v[72:75], v226, s[36:39], s72 offen offset:2048 sc1\n"
    "v_mfma_f32_16x16x32_bf16 a[136:139], a[220:223], v[124:127], a[136:139]\n"
    "v_mfma_f32_16x16x32_bf16 a[140:143], a[208:211], v[128:131], a[140:143]\n"
    "v_mfma_f32_16x16x32_bf16 a[140:143], a[212:215], v[132:135], a[140:143]\n"
    "v_mfma_f32_16x16x32_bf16 a[140:143], a[216:219], v[136:139], a[140:143]\n"
    "v_mfma_f32_16x16x32_bf16 a[140:143], a[220:223], v[140:143], a[140:143]\n"
    "s_waitcnt lgkmcnt(0)\n"
    "s_barrier\n"
    "v_mfma_f32_16x16x32_bf16 a[144:147], a[224:227], v[80:83], a[144:147]\n"
    "ds_read_b64_tr_b16 a[160:161], v212 offset:0\n"
    "buffer_load_dwordx4 v[76:79], v226, s[36:39], s72 offen offset:3072 sc1\n"
    "v_mfma_f32_16x16x32_bf16 a[144:147], a[228:231], v[84:87], a[144:147]\n"
    "ds_read_b64_tr_b16 a[162:163], v212 offset:5120\n"
    "v_mfma_f32_16x16x32_bf16 a[144:147], a[232:235], v[88:91], a[144:147]\n"
    "ds_read_b64_tr_b16 a[164:165], v212 offset:10240\n"
    "v_mfma_f32_16x16x32_bf16 a[144:147], a[236:239], v[92:95], a[144:147]\n"
    "ds_read_b64_tr_b16 a[166:167], v212 offset:15360\n"
    "v_mfma_f32_16x16x32_bf16 a[148:151], a[224:227], v[96:99], a[148:151]\n"
    "ds_read_b64_tr_b16 a[168:169], v212 offset:20480\n"
    "v_mfma_f32_16x16x32_bf16 a[148:151], a[228:231], v[100:103], a[148:151]\n"
    "ds_read_b64_tr_b16 a[170:171], v212 offset:25600\n"
    "v_mfma_f32_16x16x32_bf16 a[148:151], a[232:235], v[104:107], a[148:151]\n"
    "ds_read_b64_tr_b16 a[172:173], v212 offset:30720\n"
    "v_mfma_f32_16x16x32_bf16 a[148:151], a[236:239], v[108:111], a[148:151]\n"
    "ds_read_b64_tr_b16 a[174:175], v212 offset:35840\n"
    "v_mfma_f32_16x16x32_bf16 a[152:155], a[224:227], v[112:115], a[152:155]\n"
    "s_add_u32 s69, s69, 4096\n"
    "s_add_u32 s70, s70, 4096\n"
    "s_add_u32 s71, s71, 4096\n"
    "s_add_u32 s72, s72, 4096\n"
    "v_mfma_f32_16x16x32_bf16 a[152:155], a[228:231], v[116:119], a[152:155]\n"
    "v_mfma_f32_16x16x32_bf16 a[152:155], a[232:235], v[120:123], a[152:155]\n"
    "v_mfma_f32_16x16x32_bf16 a[152:155], a[236:239], v[124:127], a[152:155]\n"
    "v_mfma_f32_16x16x32_bf16 a[156:159], a[224:227], v[128:131], a[156:159]\n"
    "s_sub_u32 s74, s74, 1\n"
    "s_cmp_eq_u32 s74, 0\n"
    "s_cselect_b32 s69, s75, s69\n"
    "s_cselect_b32 s70, s75, s70\n"
    "s_cselect_b32 s71, s75, s71\n"
    "s_cselect_b32 s72, s75, s72\n"
    "v_mfma_f32_16x16x32_bf16 a[156:159], a[228:231], v[132:135], a[156:159]\n"
    "v_mfma_f32_16x16x32_bf16 a[156:159], a[232:235], v[136:139], a[156:159]\n"
    "v_mfma_f32_16x16x32_bf16 a[156:159], a[236:239], v[140:143], a[156:159]\n"
    "s_waitcnt lgkmcnt(0)\n"
    "ds_read_b64_tr_b16 a[176:177], v213 offset:32\n"
    "s_waitcnt vmcnt(26)\n"
    "v_mfma_f32_16x16x32_bf16 a[0:3], a[160:163], v[144:147], a[0:3]\n"
    "ds_read_b64_tr_b16 a[178:179], v213 offset:5152\n"
    "s_add_u32 m0, s78, 40960\n"
    "v_mfma_f32_16x16x32_bf16 a[0:3], a[164:167], v[148:151], a[0:3]\n"
    "ds_read_b64_tr_b16 a[180:181], v213 offset:10272\n"
    "buffer_load_dwordx4 v214, s[40:43], s73 offen offset:0 lds\n"
    "v_mfma_f32_16x16x32_bf16 a[0:3], a[168:171], v[152:155], a[0:3]\n"
    "ds_read_b64_tr_b16 a[182:183], v213 offset:15392\n"
    "v_mfma_f32_16x16x32_bf16 a[0:3], a[172:175], v[156:159], a[0:3]\n"
    "ds_read_b64_tr_b16 a[184:185], v213 offset:20512\n"
    "v_mfma_f32_16x16x32_bf16 a[4:7], a[160:163], v[160:163], a[4:7]\n"
    "ds_read_b64_tr_b16 a[186:187], v213 offset:25632\n"
    "v_mfma_f32_16x16x32_bf16 a[4:7], a[164:167], v[164:167], a[4:7]\n"
    "ds_read_b64_tr_b16 a[188:189], v213 offset:30752\n"
    "v_mfma_f32_16x16x32_bf16 a[4:7], a[168:171], v[168:171], a[4:7]\n"
    "ds_read_b64_tr_b16 a[190:191], v213 offset:35872\n"
    "v_mfma_f32_16x16x32_bf16 a[4:7], a[172:175], v[172:175], a[4:7]\n"
    "buffer_load_dwordx4 v215, s[40:43], s73 offen offset:1024 lds\n"
    "v_mfma_f32_16x16x32_bf16 a[8:11], a[160:163], v[176:179], a[8:11]\n"
    "ds_read_b64_tr_b16 a[192:193], v212 offset:64\n"
    "v_mfma_f32_16x16x32_bf16 a[8:11], a[164:167], v[180:183], a[8:11]\n"
    "ds_read_b64_tr_b16 a[194:195], v212 offset:5184\n"
    "v_mfma_f32_16x16x32_bf16 a[8:11], a[168:171], v[184:187], a[8:11]\n"
    "ds_read_b64_tr_b16 a[196:197], v212 offset:10304\n"
    "v_mfma_f32_16x16x32_bf16 a[8:11], a[172:175], v[188:191], a[8:11]\n"
    "ds_read_b64_tr_b16 a[198:199], v212 offset:15424\n"
    "buffer_load_dwordx4 v216, s[40:43], s73 offen offset:2048 lds\n"
    "v_mfma_f32_16x16x32_bf16 a[12:15], a[160:163], v[192:195], a[12:15]\n"
    "ds_read_b64_tr_b16 a[200:201], v212 offset:20544\n"
    "v_mfma_f32_16x16x32_bf16 a[12:15], a[164:167], v[196:199], a[12:15]\n"
    "ds_read_b64_tr_b16 a[202:203], v212 offset:25664\n"
    "v_mfma_f32_16x16x32_bf16 a[12:15], a[168:171], v[200:203], a[12:15]\n"
    "ds_read_b64_tr_b16 a[204:205], v212 offset:30784\n"
    "v_mfma_f32_16x16x32_bf16 a[12:15], a[172:175], v[204:207], a[12:15]\n"
    "ds_read_b64_tr_b16 a[206:207], v212 offset:35904\n"
    "s_waitcnt lgkmcnt(8)\n"
    "v_mfma_f32_16x16x32_bf16 a[16:19], a[176:179], v[144:147], a[16:19]\n"
    "v_mfma_f32_16x16x32_bf16 a[16:19], a[180:183], v[148:151], a[16:19]\n"
    "buffer_load_dwordx4 v217, s[40:43], s73 offen offset:3072 lds\n"
    "v_mfma_f32_16x16x32_bf16 a[16:19], a[184:187], v[152:155], a[16:19]\n"
    "v_mfma_f32_16x16x32_bf16 a[16:19], a[188:191], v[156:159], a[16:19]\n"
    "v_mfma_f32_16x16x32_bf16 a[20:23], a[176:179], v[160:163], a[20:23]\n"
    "v_mfma_f32_16x16x32_bf16 a[20:23], a[180:183], v[164:167], a[20:23]\n"
    "v_mfma_f32_16x16x32_bf16 a[20:23], a[184:187], v[168:171], a[20:23]\n"
    "v_mfma_f32_16x16x32_bf16 a[20:23], a[188:191], v[172:175], a[20:23]\n"
    "s_add_u32 m0, s78, 45056\n"
    "s_nop 0\n"
    "buffer_load_dwordx4 v218, s[40:43], s73 offen offset:0 lds\n"
    "v_mfma_f32_16x16x32_bf16 a[24:27], a[176:179], v[176:179], a[24:27]\n"
    "ds_read_b64_tr_b16 a[208:209], v213 offset:96\n"
    "v_mfma_f32_16x16x32_bf16 a[24:27], a[180:183], v[180:183], a[24:27]\n"
    "ds_read_b64_tr_b16 a[210:211], v213 offset:5216\n"
    "v_mfma_f32_16x16x32_bf16 a[24:27], a[184:187], v[184:187], a[24:27]\n"
    "ds_read_b64_tr_b16 a[212:213], v213 offset:10336\n"
    "v_mfma_f32_16x16x32_bf16 a[24:27], a[188:191], v[188:191], a[24:27]\n"
    "ds_read_b64_tr_b16 a[214:215], v213 offset:15456\n"
    "v_mfma_f32_16x16x32_bf16 a[28:31], a[176:179], v[192:195], a[28:31]\n"
    "ds_read_b64_tr_b16 a[216:217], v213 offset:20576\n"
    "v_mfma_f32_16x16x32_bf16 a[28:31], a[180:183], v[196:199], a[28:31]\n"
    "ds_read_b64_tr_b16 a[218:219], v213 offset:25696\n"
    "v_mfma_f32_16x16x32_bf16 a[28:31], a[184:187], v[200:203], a[28:31]\n"
    "ds_read_b64_tr_b16 a[220:221], v213 offset:30816\n"
    "v_mfma_f32_16x16x32_bf16 a[28:31], a[188:191], v[204:207], a[28:31]\n"
    "ds_read_b64_tr_b16 a[222:223], v213 offset:35936\n"
    "s_waitcnt lgkmcnt(8)\n"
    "v_mfma_f32_16x16x32_bf16 a[32:35], a[192:195], v[144:147], a[32:35]\n"
    "buffer_load_dwordx4 v219, s[40:43], s73 offen offset:1024 lds\n"
    "v_mfma_f32_16x16x32_bf16 a[32:35], a[196:199], v[148:151], a[32:35]\n"
    "v_mfma_f32_16x16x32_bf16 a[32:35], a[200:203], v[152:155], a[32:35]\n"
    "buffer_load_dwordx4 v220, s[40:43], s73 offen offset:2048 lds\n"
    "v_mfma_f32_16x16x32_bf16 a[32:35], a[204:207], v[156:159], a[32:35]\n"
    "v_mfma_f32_16x16x32_bf16 a[36:39], a[192:195], v[160:163], a[36:39]\n"
    "v_mfma_f32_16x16x32_bf16 a[36:39], a[196:199], v[164:167], a[36:39]\n"
    "v_mfma_f32_16x16x32_bf16 a[36:39], a[200:203], v[168:171], a[36:39]\n"
    "v_mfma_f32_16x16x32_bf16 a[36:39], a[204:207], v[172:175], a[36:39]\n"
    "v_mfma_f32_16x16x32_bf16 a[40:43], a[192:195], v[176:179], a[40:43]\n"
    "ds_read_b64_tr_b16 a[224:225], v212 offset:128\n"
    "buffer_load_dwordx4 v221, s[40:43], s73 offen offset:3072 lds\n"
    "v_mfma_f32_16x16x32_bf16 a[40:43], a[196:199], v[180:183], a[40:43]\n"
    "ds_read_b64_tr_b16 a[226:227], v212 offset:5248\n"
    "v_mfma_f32_16x16x32_bf16 a[40:43], a[200:203], v[184:187], a[40:43]\n"
    "ds_read_b64_tr_b16 a[228:229], v212 offset:10368\n"
    "v_mfma_f32_16x16x32_bf16 a[40:43], a[204:207], v[188:191], a[40:43]\n"
    "ds_read_b64_tr_b16 a[230:231], v212 offset:15488\n"
    "v_mfma_f32_16x16x32_bf16 a[44:47], a[192:195], v[192:195], a[44:47]\n"
    "ds_read_b64_tr_b16 a[232:233], v212 offset:20608\n"
    "v_mfma_f32_16x16x32_bf16 a[44:47], a[196:199], v[196:199], a[44:47]\n"
    "ds_read_b64_tr_b16 a[234:235], v212 offset:25728\n"
    "v_mfma_f32_16x16x32_bf16 a[44:47], a[200:203], v[200:203], a[44:47]\n"
    "ds_read_b64_tr_b16 a[236:237], v212 offset:30848\n"
    "s_add_u32 m0, s78, 49152\n"
    "v_mfma_f32_16x16x32_bf16 a[44:47], a[204:207], v[204:207], a[44:47]\n"
    "ds_read_b64_tr_b16 a[238:239], v212 offset:35968\n"
    "s_waitcnt lgkmcnt(8)\n"
    "v_mfma_f32_16x16x32_bf16 a[48:51], a[208:211], v[144:147], a[48:51]\n"
    "buffer_load_dwordx4 v222, s[40:43], s73 offen offset:0 lds\n"
    "v_mfma_f32_16x16x32_bf16 a[48:51], a[212:215], v[148:151], a[48:51]\n"
    "v_mfma_f32_16x16x32_bf16 a[48:51], a[216:219], v[152:155], a[48:51]\n"
    "v_mfma_f32_16x16x32_bf16 a[48:51], a[220:223], v[156:159], a[48:51]\n"
    "buffer_load_dwordx4 v223, s[40:43], s73 offen offset:1024 lds\n"
    "v_mfma_f32_16x16x32_bf16 a[52:55], a[208:211], v[160:163], a[52:55]\n"
    "v_mfma_f32_16x16x32_bf16 a[52:55], a[212:215], v[164:167], a[52:55]\n"
    "v_mfma_f32_16x16x32_bf16 a[52:55], a[216:219], v[168:171], a[52:55]\n"
    "v_mfma_f32_16x16x32_bf16 a[52:55], a[220:223], v[172:175], a[52:55]\n"
    "v_mfma_f32_16x16x32_bf16 a[56:59], a[208:211], v[176:179], a[56:59]\n"
    "ds_read_b64_tr_b16 a[160:161], v213 offset:160\n"
    "v_mfma_f32_16x16x32_bf16 a[56:59], a[212:215], v[180:183], a[56:59]\n"
    "ds_read_b64_tr_b16 a[162:163], v213 offset:5280\n"
    "s_add_u32 s73, s73, 163840\n"
    "v_mfma_f32_16x16x32_bf16 a[56:59], a[216:219], v[184:187], a[56:59]\n"
    "ds_read_b64_tr_b16 a[164:165], v213 offset:10400\n"
    "buffer_load_dwordx4 v[80:83], v226, s[36:39], s69 offen offset:0 sc1\n"
    "v_mfma_f32_16x16x32_bf16 a[56:59], a[220:223], v[188:191], a[56:59]\n"
    "ds_read_b64_tr_b16 a[166:167], v213 offset:15520\n"
    "v_mfma_f32_16x16x32_bf16 a[60:63], a[208:211], v[192:195], a[60:63]\n"
    "ds_read_b64_tr_b16 a[168:169], v213 offset:20640\n"
    "v_mfma_f32_16x16x32_bf16 a[60:63], a[212:215], v[196:199], a[60:63]\n"
    "ds_read_b64_tr_b16 a[170:171], v213 offset:25760\n"
    "v_mfma_f32_16x16x32_bf16 a[60:63], a[216:219], v[200:203], a[60:63]\n"
    "ds_read_b64_tr_b16 a[172:173], v213 offset:30880\n"
    "v_mfma_f32_16x16x32_bf16 a[60:63], a[220:223], v[204:207], a[60:63]\n"
    "ds_read_b64_tr_b16 a[174:175], v213 offset:36000\n"
    "s_waitcnt lgkmcnt(8)\n"
    "v_mfma_f32_16x16x32_bf16 a[64:67], a[224:227], v[144:147], a[64:67]\n"
    "buffer_load_dwordx4 v[84:87], v226, s[36:39], s69 offen offset:1024 sc1\n"
    "v_mfma_f32_16x16x32_bf16 a[64:67], a[228:231], v[148:151], a[64:67]\n"
    "v_mfma_f32_16x16x32_bf16 a[64:67], a[232:235], v[152:155], a[64:67]\n"
    "v_mfma_f32_16x16x32_bf16 a[64:67], a[236:239], v[156:159], a[64:67]\n"
    "v_mfma_f32_16x16x32_bf16 a[68:71], a[224:227], v[160:163], a[68:71]\n"
    "v_mfma_f32_16x16x32_bf16 a[68:71], a[228:231], v[164:167], a[68:71]\n"
    "buffer_load_dwordx4 v[88:91], v226, s[36:39], s69 offen offset:2048 sc1\n"
    "v_mfma_f32_16x16x32_bf16 a[68:71], a[232:235], v[168:171], a[68:71]\n"
    "v_mfma_f32_16x16x32_bf16 a[68:71], a[236:239], v[172:175], a[68:71]\n"
    "v_mfma_f32_16x16x32_bf16 a[72:75], a[224:227], v[176:179], a[72:75]\n"
    "ds_read_b64_tr_b16 a[176:177], v212 offset:192\n"
    "v_mfma_f32_16x16x32_bf16 a[72:75], a[228:231], v[180:183], a[72:75]\n"
    "ds_read_b64_tr_b16 a[178:179], v212 offset:5312\n"
    "v_mfma_f32_16x16x32_bf16 a[72:75], a[232:235], v[184:187], a[72:75]\n"
    "ds_read_b64_tr_b16 a[180:181], v212 offset:10432\n"
    "v_mfma_f32_16x16x32_bf16 a[72:75], a[236:239], v[188:191], a[72:75]\n"
    "ds_read_b64_tr_b16 a[182:183], v212 offset:15552\n"
    "buffer_load_dwordx4 v[92:95], v226, s[36:39], s69 offen offset:3072 sc1\n"
    "v_mfma_f32_16x16x32_bf16 a[76:79], a[224:227], v[192:195], a[76:79]\n"
    "ds_read_b64_tr_b16 a[184:185], v212 offset:20672\n"
    "v_mfma_f32_16x16x32_bf16 a[76:79], a[228:231], v[196:199], a[76:79]\n"
    "ds_read_b64_tr_b16 a[186:187], v212 offset:25792\n"
    "v_mfma_f32_16x16x32_bf16 a[76:79], a[232:235], v[200:203], a[76:79]\n"
    "ds_read_b64_tr_b16 a[188:189], v212 offset:30912\n"
    "v_mfma_f32_16x16x32_bf16 a[76:79], a[236:239], v[204:207], a[76:79]\n"
    "ds_read_b64_tr_b16 a[190:191], v212 offset:36032\n"
    "s_waitcnt lgkmcnt(8)\n"
    "v_mfma_f32_16x16x32_bf16 a[80:83], a[160:163], v[144:147], a[80:83]\n"
    "buffer_load_dwordx4 v[96:99], v226, s[36:39], s70 offen offset:0 sc1\n"
    "v_mfma_f32_16x16x32_bf16 a[80:83], a[164:167], v[148:151], a[80:83]\n"
    "v_mfma_f32_16x16x32_bf16 a[80:83], a[168:171], v[152:155], a[80:83]\n"
    "v_mfma_f32_16x16x32_bf16 a[80:83], a[172:175], v[156:159], a[80:83]\n"
    "v_mfma_f32_16x16x32_bf16 a[84:87], a[160:163], v[160:163], a[84:87]\n"
    "v_mfma_f32_16x16x32_bf16 a[84:87], a[164:167], v[164:167], a[84:87]\n"
    "v_mfma_f32_16x16x32_bf16 a[84:87], a[168:171], v[168:171], a[84:87]\n"
    "buffer_load_dwordx4 v[100:103], v226, s[36:39], s70 offen offset:1024 sc1\n"
    "v_mfma_f32_16x16x32_bf16 a[84:87], a[172:175], v[172:175], a[84:87]\n"
    "v_mfma_f32_16x16x32_bf16 a[88:91], a[160:163], v[176:179], a[88:91]\n"
    "ds_read_b64_tr_b16 a[192:193], v213 offset:224\n"
    "v_mfma_f32_16x16x32_bf16 a[88:91], a[164:167], v[180:183], a[88:91]\n"
    "ds_read_b64_tr_b16 a[194:195], v213 offset:5344\n"
    "v_mfma_f32_16x16x32_bf16 a[88:91], a[168:171], v[184:187], a[88:91]\n"
    "ds_read_b64_tr_b16 a[196:197], v213 offset:10464\n"
    "v_mfma_f32_16x16x32_bf16 a[88:91], a[172:175], v[188:191], a[88:91]\n"
    "ds_read_b64_tr_b16 a[198:199], v213 offset:15584\n"
    "v_mfma_f32_16x16x32_bf16 a[92:95], a[160:163], v[192:195], a[92:95]\n"
    "ds_read_b64_tr_b16 a[200:201], v213 offset:20704\n"
    "v_mfma_f32_16x16x32_bf16 a[92:95], a[164:167], v[196:199], a[92:95]\n"
    "ds_read_b64_tr_b16 a[202:203], v213 offset:25824\n"
    "v_mfma_f32_16x16x32_bf16 a[92:95], a[168:171], v[200:203], a[92:95]\n"
    "ds_read_b64_tr_b16 a[204:205], v213 offset:30944\n"
    "v_mfma_f32_16x16x32_bf16 a[92:95], a[172:175], v[204:207], a[92:95]\n"
    "ds_read_b64_tr_b16 a[206:207], v213 offset:36064\n"
    "s_waitcnt lgkmcnt(8)\n"
    "v_mfma_f32_16x16x32_bf16 a[96:99], a[176:179], v[144:147], a[96:99]\n"
    "buffer_load_dwordx4 v[104:107], v226, s[36:39], s70 offen offset:2048 sc1\n"
    "v_mfma_f32_16x16x32_bf16 a[96:99], a[180:183], v[148:151], a[96:99]\n"
    "v_mfma_f32_16x16x32_bf16 a[96:99], a[184:187], v[152:155], a[96:99]\n"
    "buffer_load_dwordx4 v[108:111], v226, s[36:39], s70 offen offset:3072 sc1\n"
    "v_mfma_f32_16x16x32_bf16 a[96:99], a[188:191], v[156:159], a[96:99]\n"
    "v_mfma_f32_16x16x32_bf16 a[100:103], a[176:179], v[160:163], a[100:103]\n"
    "v_mfma_f32_16x16x32_bf16 a[100:103], a[180:183], v[164:167], a[100:103]\n"
    "v_mfma_f32_16x16x32_bf16 a[100:103], a[184:187], v[168:171], a[100:103]\n"
    "v_mfma_f32_16x16x32_bf16 a[100:103], a[188:191], v[172:175], a[100:103]\n"
    "buffer_load_dwordx4 v[112:115], v226, s[36:39], s71 offen offset:0 sc1\n"
    "v_mfma_f32_16x16x32_bf16 a[104:107], a[176:179], v[176:179], a[104:107]\n"
    "ds_read_b64_tr_b16 a[208:209], v212 offset:256\n"
    "v_mfma_f32_16x16x32_bf16 a[104:107], a[180:183], v[180:183], a[104:107]\n"
    "ds_read_b64_tr_b16 a[210:211], v212 offset:5376\n"
    "v_mfma_f32_16x16x32_bf16 a[104:107], a[184:187], v[184:187], a[104:107]\n"
    "ds_read_b64_tr_b16 a[212:213], v212 offset:10496\n"
    "v_mfma_f32_16x16x32_bf16 a[104:107], a[188:191], v[188:191], a[104:107]\n"
    "ds_read_b64_tr_b16 a[214:215], v212 offset:15616\n"
    "v_mfma_f32_16x16x32_bf16 a[108:111], a[176:179], v[192:195], a[108:111]\n"
    "ds_read_b64_tr_b16 a[216:217], v212 offset:20736\n"
    "v_mfma_f32_16x16x32_bf16 a[108:111], a[180:183], v[196:199], a[108:111]\n"
    "ds_read_b64_tr_b16 a[218:219], v212 offset:25856\n"
    "v_mfma_f32_16x16x32_bf16 a[108:111], a[184:187], v[200:203], a[108:111]\n"
    "ds_read_b64_tr_b16 a[220:221], v212 offset:30976\n"
    "v_mfma_f32_16x16x32_bf16 a[108:111], a[188:191], v[204:207], a[108:111]\n"
    "ds_read_b64_tr_b16 a[222:223], v212 offset:36096\n"
    "s_waitcnt lgkmcnt(8)\n"
    "v_mfma_f32_16x16x32_bf16 a[112:115], a[192:195], v[144:147], a[112:115]\n"
    "buffer_load_dwordx4 v[116:119], v226, s[36:39], s71 offen offset:1024 sc1\n"
    "v_mfma_f32_16x16x32_bf16 a[112:115], a[196:199], v[148:151], a[112:115]\n"
    "v_mfma_f32_16x16x32_bf16 a[112:115], a[200:203], v[152:155], a[112:115]\n"
    "v_mfma_f32_16x16x32_bf16 a[112:115], a[204:207], v[156:159], a[112:115]\n"
    "buffer_load_dwordx4 v[120:123], v226, s[36:39], s71 offen offset:2048 sc1\n"
    "v_mfma_f32_16x16x32_bf16 a[116:119], a[192:195], v[160:163], a[116:119]\n"
    "v_mfma_f32_16x16x32_bf16 a[116:119], a[196:199], v[164:167], a[116:119]\n"
    "v_mfma_f32_16x16x32_bf16 a[116:119], a[200:203], v[168:171], a[116:119]\n"
    "v_mfma_f32_16x16x32_bf16 a[116:119], a[204:207], v[172:175], a[116:119]\n"
    "v_mfma_f32_16x16x32_bf16 a[120:123], a[192:195], v[176:179], a[120:123]\n"
    "ds_read_b64_tr_b16 a[224:225], v213 offset:288\n"
    "v_mfma_f32_16x16x32_bf16 a[120:123], a[196:199], v[180:183], a[120:123]\n"
    "ds_read_b64_tr_b16 a[226:227], v213 offset:5408\n"
    "buffer_load_dwordx4 v[124:127], v226, s[36:39], s71 offen offset:3072 sc1\n"
    "v_mfma_f32_16x16x32_bf16 a[120:123], a[200:203], v[184:187], a[120:123]\n"
    "ds_read_b64_tr_b16 a[228:229], v213 offset:10528\n"
    "v_mfma_f32_16x16x32_bf16 a[120:123], a[204:207], v[188:191], a[120:123]\n"
    "ds_read_b64_tr_b16 a[230:231], v213 offset:15648\n"
    "v_mfma_f32_16x16x32_bf16 a[124:127], a[192:195], v[192:195], a[124:127]\n"
    "ds_read_b64_tr_b16 a[232:233], v213 offset:20768\n"
    "v_mfma_f32_16x16x32_bf16 a[124:127], a[196:199], v[196:199], a[124:127]\n"
    "ds_read_b64_tr_b16 a[234:235], v213 offset:25888\n"
    "v_mfma_f32_16x16x32_bf16 a[124:127], a[200:203], v[200:203], a[124:127]\n"
    "ds_read_b64_tr_b16 a[236:237], v213 offset:31008\n"
    "v_mfma_f32_16x16x32_bf16 a[124:127], a[204:207], v[204:207], a[124:127]\n"
    "ds_read_b64_tr_b16 a[238:239], v213 offset:36128\n"
    "s_waitcnt lgkmcnt(8)\n"
    "v_mfma_f32_16x16x32_bf16 a[128:131], a[208:211], v[144:147], a[128:131]\n"
    "buffer_load_dwordx4 v[128:131], v226, s[36:39], s72 offen offset:0 sc1\n"
    "v_mfma_f32_16x16x32_bf16 a[128:131], a[212:215], v[148:151], a[128:131]\n"
    "v_mfma_f32_16x16x32_bf16 a[128:131], a[216:219], v[152:155], a[128:131]\n"
    "v_mfma_f32_16x16x32_bf16 a[128:131], a[220:223], v[156:159], a[128:131]\n"
    "v_mfma_f32_16x16x32_bf16 a[132:135], a[208:211], v[160:163], a[132:135]\n"
    "buffer_load_dwordx4 v[132:135], v226, s[36:39], s72 offen offset:1024 sc1\n"
    "v_mfma_f32_16x16x32_bf16 a[132:135], a[212:215], v[164:167], a[132:135]\n"
    "v_mfma_f32_16x16x32_bf16 a[132:135], a[216:219], v[168:171], a[132:135]\n"
    "v_mfma_f32_16x16x32_bf16 a[132:135], a[220:223], v[172:175], a[132:135]\n"
    "v_mfma_f32_16x16x32_bf16 a[136:139], a[208:211], v[176:179], a[136:139]\n"
    "v_mfma_f32_16x16x32_bf16 a[136:139], a[212:215], v[180:183], a[136:139]\n"
    "v_mfma_f32_16x16x32_bf16 a[136:139], a[216:219], v[184:187], a[136:139]\n"
    "buffer_load_dwordx4 v[136:139], v226, s[36:39], s72 offen offset:2048 sc1\n"
    "v_mfma_f32_16x16x32_bf16 a[136:139], a[220:223], v[188:191], a[136:139]\n"
    "v_mfma_f32_16x16x32_bf16 a[140:143], a[208:211], v[192:195], a[140:143]\n"
    "v_mfma_f32_16x16x32_bf16 a[140:143], a[212:215], v[196:199], a[140:143]\n"
    "v_mfma_f32_16x16x32_bf16 a[140:143], a[216:219], v[200:203], a[140:143]\n"
    "v_mfma_f32_16x16x32_bf16 a[140:143], a[220:223], v[204:207], a[140:143]\n"
    "s_waitcnt vmcnt(41)\n"
    "s_waitcnt lgkmcnt(0)\n"
    "s_barrier\n"
    "v_mfma_f32_16x16x32_bf16 a[144:147], a[224:227], v[144:147], a[144:147]\n"
    "ds_read_b64_tr_b16 a[160:161], v208 offset:0\n"
    "buffer_load_dwordx4 v[140:143], v226, s[36:39], s72 offen offset:3072 sc1\n"
    "v_mfma_f32_16x16x32_bf16 a[144:147], a[228:231], v[148:151], a[144:147]\n"
    "ds_read_b64_tr_b16 a[162:163], v208 offset:5120\n"
    "v_mfma_f32_16x16x32_bf16 a[144:147], a[232:235], v[152:155], a[144:147]\n"
    "ds_read_b64_tr_b16 a[164:165], v208 offset:10240\n"
    "v_mfma_f32_16x16x32_bf16 a[144:147], a[236:239], v[156:159], a[144:147]\n"
    "ds_read_b64_tr_b16 a[166:167], v208 offset:15360\n"
    "v_mfma_f32_16x16x32_bf16 a[148:151], a[224:227], v[160:163], a[148:151]\n"
    "ds_read_b64_tr_b16 a[168:169], v208 offset:20480\n"
    "v_mfma_f32_16x16x32_bf16 a[148:151], a[228:231], v[164:167], a[148:151]\n"
    "ds_read_b64_tr_b16 a[170:171], v208 offset:25600\n"
    "v_mfma_f32_16x16x32_bf16 a[148:151], a[232:235], v[168:171], a[148:151]\n"
    "ds_read_b64_tr_b16 a[172:173], v208 offset:30720\n"
    "v_mfma_f32_16x16x32_bf16 a[148:151], a[236:239], v[172:175], a[148:151]\n"
    "ds_read_b64_tr_b16 a[174:175], v208 offset:35840\n"
    "v_mfma_f32_16x16x32_bf16 a[152:155], a[224:227], v[176:179], a[152:155]\n"
    "s_add_u32 s69, s69, 4096\n"
    "s_add_u32 s70, s70, 4096\n"
    "s_add_u32 s71, s71, 4096\n"
    "s_add_u32 s72, s72, 4096\n"
    "v_mfma_f32_16x16x32_bf16 a[152:155], a[228:231], v[180:183], a[152:155]\n"
    "v_mfma_f32_16x16x32_bf16 a[152:155], a[232:235], v[184:187], a[152:155]\n"
    "v_mfma_f32_16x16x32_bf16 a[152:155], a[236:239], v[188:191], a[152:155]\n"
    "v_mfma_f32_16x16x32_bf16 a[156:159], a[224:227], v[192:195], a[156:159]\n"
    "s_sub_u32 s74, s74, 1\n"
    "s_cmp_eq_u32 s74, 0\n"
    "s_cselect_b32 s69, s75, s69\n"
    "s_cselect_b32 s70, s75, s70\n"
    "s_cselect_b32 s71, s75, s71\n"
    "s_cselect_b32 s72, s75, s72\n"
    "v_mfma_f32_16x16x32_bf16 a[156:159], a[228:231], v[196:199], a[156:159]\n"
    "v_mfma_f32_16x16x32_bf16 a[156:159], a[232:235], v[200:203], a[156:159]\n"
    "v_mfma_f32_16x16x32_bf16 a[156:159], a[236:239], v[204:207], a[156:159]\n"
    "s_waitcnt lgkmcnt(0)\n"
    "ds_read_b64_tr_b16 a[176:177], v209 offset:32\n"
    "s_waitcnt vmcnt(26)\n"
    "v_mfma_f32_16x16x32_bf16 a[0:3], a[160:163], v[16:19], a[0:3]\n"
    "ds_read_b64_tr_b16 a[178:179], v209 offset:5152\n"
    "s_add_u32 m0, s78, 81920\n"
    "v_mfma_f32_16x16x32_bf16 a[0:3], a[164:167], v[20:23], a[0:3]\n"
    "ds_read_b64_tr_b16 a[180:181], v209 offset:10272\n"
    "buffer_load_dwordx4 v214, s[40:43], s73 offen offset:0 lds\n"
    "v_mfma_f32_16x16x32_bf16 a[0:3], a[168:171], v[24:27], a[0:3]\n"
    "ds_read_b64_tr_b16 a[182:183], v209 offset:15392\n"
    "v_mfma_f32_16x16x32_bf16 a[0:3], a[172:175], v[28:31], a[0:3]\n"
    "ds_read_b64_tr_b16 a[184:185], v209 offset:20512\n"
    "v_mfma_f32_16x16x32_bf16 a[4:7], a[160:163], v[32:35], a[4:7]\n"
    "ds_read_b64_tr_b16 a[186:187], v209 offset:25632\n"
    "v_mfma_f32_16x16x32_bf16 a[4:7], a[164:167], v[36:39], a[4:7]\n"
    "ds_read_b64_tr_b16 a[188:189], v209 offset:30752\n"
    "v_mfma_f32_16x16x32_bf16 a[4:7], a[168:171], v[40:43], a[4:7]\n"
    "ds_read_b64_tr_b16 a[190:191], v209 offset:35872\n"
    "v_mfma_f32_16x16x32_bf16 a[4:7], a[172:175], v[44:47], a[4:7]\n"
    "buffer_load_dwordx4 v215, s[40:43], s73 offen offset:1024 lds\n"
    "v_mfma_f32_16x16x32_bf16 a[8:11], a[160:163], v[48:51], a[8:11]\n"
    "ds_read_b64_tr_b16 a[192:193], v208 offset:64\n"
    "v_mfma_f32_16x16x32_bf16 a[8:11], a[164:167], v[52:55], a[8:11]\n"
    "ds_read_b64_tr_b16 a[194:195], v208 offset:5184\n"
    "v_mfma_f32_16x16x32_bf16 a[8:11], a[168:171], v[56:59], a[8:11]\n"
    "ds_read_b64_tr_b16 a[196:197], v208 offset:10304\n"
    "v_mfma_f32_16x16x32_bf16 a[8:11], a[172:175], v[60:63], a[8:11]\n"
    "ds_read_b64_tr_b16 a[198:199], v208 offset:15424\n"
    "buffer_load_dwordx4 v216, s[40:43], s73 offen offset:2048 lds\n"
    "v_mfma_f32_16x16x32_bf16 a[12:15], a[160:163], v[64:67], a[12:15]\n"
    "ds_read_b64_tr_b16 a[200:201], v208 offset:20544\n"
    "v_mfma_f32_16x16x32_bf16 a[12:15], a[164:167], v[68:71], a[12:15]\n"
    "ds_read_b64_tr_b16 a[202:203], v208 offset:25664\n"
    "v_mfma_f32_16x16x32_bf16 a[12:15], a[168:171], v[72:75], a[12:15]\n"
    "ds_read_b64_tr_b16 a[204:205], v208 offset:30784\n"
    "v_mfma_f32_16x16x32_bf16 a[12:15], a[172:175], v[76:79], a[12:15]\n"
    "ds_read_b64_tr_b16 a[206:207], v208 offset:35904\n"
    "s_waitcnt lgkmcnt(8)\n"
    "v_mfma_f32_16x16x32_bf16 a[16:19], a[176:179], v[16:19], a[16:19]\n"
    "v_mfma_f32_16x16x32_bf16 a[16:19], a[180:183], v[20:23], a[16:19]\n"
    "buffer_load_dwordx4 v217, s[40:43], s73 offen offset:3072 lds\n"
    "v_mfma_f32_16x16x32_bf16 a[16:19], a[184:187], v[24:27], a[16:19]\n"
    "v_mfma_f32_16x16x32_bf16 a[16:19], a[188:191], v[28:31], a[16:19]\n"
    "v_mfma_f32_16x16x32_bf16 a[20:23], a[176:179], v[32:35], a[20:23]\n"
    "v_mfma_f32_16x16x32_bf16 a[20:23], a[180:183], v[36:39], a[20:23]\n"
    "v_mfma_f32_16x16x32_bf16 a[20:23], a[184:187], v[40:43], a[20:23]\n"
    "v_mfma_f32_16x16x32_bf16 a[20:23], a[188:191], v[44:47], a[20:23]\n"
    "s_add_u32 m0, s78, 86016\n"
    "s_nop 0\n"
    "buffer_load_dwordx4 v218, s[40:43], s73 offen offset:0 lds\n"
    "v_mfma_f32_16x16x32_bf16 a[24:27], a[176:179], v[48:51], a[24:27]\n"
    "ds_read_b64_tr_b16 a[208:209], v209 offset:96\n"
    "v_mfma_f32_16x16x32_bf16 a[24:27], a[180:183], v[52:55], a[24:27]\n"
    "ds_read_b64_tr_b16 a[210:211], v209 offset:5216\n"
    "v_mfma_f32_16x16x32_bf16 a[24:27], a[184:187], v[56:59], a[24:27]\n"
    "ds_read_b64_tr_b16 a[212:213], v209 offset:10336\n"
    "v_mfma_f32_16x16x32_bf16 a[24:27], a[188:191], v[60:63], a[24:27]\n"
    "ds_read_b64_tr_b16 a[214:215], v209 offset:15456\n"
    "v_mfma_f32_16x16x32_bf16 a[28:31], a[176:179], v[64:67], a[28:31]\n"
    "ds_read_b64_tr_b16 a[216:217], v209 offset:20576\n"
    "v_mfma_f32_16x16x32_bf16 a[28:31], a[180:183], v[68:71], a[28:31]\n"
    "ds_read_b64_tr_b16 a[218:219], v209 offset:25696\n"
    "v_mfma_f32_16x16x32_bf16 a[28:31], a[184:187], v[72:75], a[28:31]\n"
    "ds_read_b64_tr_b16 a[220:221], v209 offset:30816\n"
    "v_mfma_f32_16x16x32_bf16 a[28:31], a[188:191], v[76:79], a[28:31]\n"
    "ds_read_b64_tr_b16 a[222:223], v209 offset:35936\n"
    "s_waitcnt lgkmcnt(8)\n"
    "v_mfma_f32_16x16x32_bf16 a[32:35], a[192:195], v[16:19], a[32:35]\n"
    "buffer_load_dwordx4 v219, s[40:43], s73 offen offset:1024 lds\n"
    "v_mfma_f32_16x16x32_bf16 a[32:35], a[196:199], v[20:23], a[32:35]\n"
    "v_mfma_f32_16x16x32_bf16 a[32:35], a[200:203], v[24:27], a[32:35]\n"
    "buffer_load_dwordx4 v220, s[40:43], s73 offen offset:2048 lds\n"
    "v_mfma_f32_16x16x32_bf16 a[32:35], a[204:207], v[28:31], a[32:35]\n"
    "v_mfma_f32_16x16x32_bf16 a[36:39], a[192:195], v[32:35], a[36:39]\n"
    "v_mfma_f32_16x16x32_bf16 a[36:39], a[196:199], v[36:39], a[36:39]\n"
    "v_mfma_f32_16x16x32_bf16 a[36:39], a[200:203], v[40:43], a[36:39]\n"
    "v_mfma_f32_16x16x32_bf16 a[36:39], a[204:207], v[44:47], a[36:39]\n"
    "v_mfma_f32_16x16x32_bf16 a[40:43], a[192:195], v[48:51], a[40:43]\n"
    "ds_read_b64_tr_b16 a[224:225], v208 offset:128\n"
    "buffer_load_dwordx4 v221, s[40:43], s73 offen offset:3072 lds\n"
    "v_mfma_f32_16x16x32_bf16 a[40:43], a[196:199], v[52:55], a[40:43]\n"
    "ds_read_b64_tr_b16 a[226:227], v208 offset:5248\n"
    "v_mfma_f32_16x16x32_bf16 a[40:43], a[200:203], v[56:59], a[40:43]\n"
    "ds_read_b64_tr_b16 a[228:229], v208 offset:10368\n"
    "v_mfma_f32_16x16x32_bf16 a[40:43], a[204:207], v[60:63], a[40:43]\n"
    "ds_read_b64_tr_b16 a[230:231], v208 offset:15488\n"
    "v_mfma_f32_16x16x32_bf16 a[44:47], a[192:195], v[64:67], a[44:47]\n"
    "ds_read_b64_tr_b16 a[232:233], v208 offset:20608\n"
    "v_mfma_f32_16x16x32_bf16 a[44:47], a[196:199], v[68:71], a[44:47]\n"
    "ds_read_b64_tr_b16 a[234:235], v208 offset:25728\n"
    "v_mfma_f32_16x16x32_bf16 a[44:47], a[200:203], v[72:75], a[44:47]\n"
    "ds_read_b64_tr_b16 a[236:237], v208 offset:30848\n"
    "s_add_u32 m0, s78, 90112\n"
    "v_mfma_f32_16x16x32_bf16 a[44:47], a[204:207], v[76:79], a[44:47]\n"
    "ds_read_b64_tr_b16 a[238:239], v208 offset:35968\n"
    "s_waitcnt lgkmcnt(8)\n"
    "v_mfma_f32_16x16x32_bf16 a[48:51], a[208:211], v[16:19], a[48:51]\n"
    "buffer_load_dwordx4 v222, s[40:43], s73 offen offset:0 lds\n"
    "v_mfma_f32_16x16x32_bf16 a[48:51], a[212:215], v[20:23], a[48:51]\n"
    "v_mfma_f32_16x16x32_bf16 a[48:51], a[216:219], v[24:27], a[48:51]\n"
    "v_mfma_f32_16x16x32_bf16 a[48:51], a[220:223], v[28:31], a[48:51]\n"
    "buffer_load_dwordx4 v223, s[40:43], s73 offen offset:1024 lds\n"
    "v_mfma_f32_16x16x32_bf16 a[52:55], a[208:211], v[32:35], a[52:55]\n"
    "v_mfma_f32_16x16x32_bf16 a[52:55], a[212:215], v[36:39], a[52:55]\n"
    "v_mfma_f32_16x16x32_bf16 a[52:55], a[216:219], v[40:43], a[52:55]\n"
    "v_mfma_f32_16x16x32_bf16 a[52:55], a[220:223], v[44:47], a[52:55]\n"
    "v_mfma_f32_16x16x32_bf16 a[56:59], a[208:211], v[48:51], a[56:59]\n"
    "ds_read_b64_tr_b16 a[160:161], v209 offset:160\n"
    "v_mfma_f32_16x16x32_bf16 a[56:59], a[212:215], v[52:55], a[56:59]\n"
    "ds_read_b64_tr_b16 a[162:163], v209 offset:5280\n"
    "s_add_u32 s73, s73, 163840\n"
    "v_mfma_f32_16x16x32_bf16 a[56:59], a[216:219], v[56:59], a[56:59]\n"
    "ds_read_b64_tr_b16 a[164:165], v209 offset:10400\n"
    "buffer_load_dwordx4 v[144:147], v226, s[36:39], s69 offen offset:0 sc1\n"
    "v_mfma_f32_16x16x32_bf16 a[56:59], a[220:223], v[60:63], a[56:59]\n"
    "ds_read_b64_tr_b16 a[166:167], v209 offset:15520\n"
    "v_mfma_f32_16x16x32_bf16 a[60:63], a[208:211], v[64:67], a[60:63]\n"
    "ds_read_b64_tr_b16 a[168:169], v209 offset:20640\n"
    "v_mfma_f32_16x16x32_bf16 a[60:63], a[212:215], v[68:71], a[60:63]\n"
    "ds_read_b64_tr_b16 a[170:171], v209 offset:25760\n"
    "v_mfma_f32_16x16x32_bf16 a[60:63], a[216:219], v[72:75], a[60:63]\n"
    "ds_read_b64_tr_b16 a[172:173], v209 offset:30880\n"
    "v_mfma_f32_16x16x32_bf16 a[60:63], a[220:223], v[76:79], a[60:63]\n"
    "ds_read_b64_tr_b16 a[174:175], v209 offset:36000\n"
    "s_waitcnt lgkmcnt(8)\n"
    "v_mfma_f32_16x16x32_bf16 a[64:67], a[224:227], v[16:19], a[64:67]\n"
    "buffer_load_dwordx4 v[148:151], v226, s[36:39], s69 offen offset:1024 sc1\n"
    "v_mfma_f32_16x16x32_bf16 a[64:67], a[228:231], v[20:23], a[64:67]\n"
    "v_mfma_f32_16x16x32_bf16 a[64:67], a[232:235], v[24:27], a[64:67]\n"
    "v_mfma_f32_16x16x32_bf16 a[64:67], a[236:239], v[28:31], a[64:67]\n"
    "v_mfma_f32_16x16x32_bf16 a[68:71], a[224:227], v[32:35], a[68:71]\n"
    "v_mfma_f32_16x16x32_bf16 a[68:71], a[228:231], v[36:39], a[68:71]\n"
    "buffer_load_dwordx4 v[152:155], v226, s[36:39], s69 offen offset:2048 sc1\n"
    "v_mfma_f32_16x16x32_bf16 a[68:71], a[232:235], v[40:43], a[68:71]\n"
    "v_mfma_f32_16x16x32_bf16 a[68:71], a[236:239], v[44:47], a[68:71]\n"
    "v_mfma_f32_16x16x32_bf16 a[72:75], a[224:227], v[48:51], a[72:75]\n"
    "ds_read_b64_tr_b16 a[176:177], v208 offset:192\n"
    "v_mfma_f32_16x16x32_bf16 a[72:75], a[228:231], v[52:55], a[72:75]\n"
    "ds_read_b64_tr_b16 a[178:179], v208 offset:5312\n"
    "v_mfma_f32_16x16x32_bf16 a[72:75], a[232:235], v[56:59], a[72:75]\n"
    "ds_read_b64_tr_b16 a[180:181], v208 offset:10432\n"
    "v_mfma_f32_16x16x32_bf16 a[72:75], a[236:239], v[60:63], a[72:75]\n"
    "ds_read_b64_tr_b16 a[182:183], v208 offset:15552\n"
    "buffer_load_dwordx4 v[156:159], v226, s[36:39], s69 offen offset:3072 sc1\n"
    "v_mfma_f32_16x16x32_bf16 a[76:79], a[224:227], v[64:67], a[76:79]\n"
    "ds_read_b64_tr_b16 a[184:185], v208 offset:20672\n"
    "v_mfma_f32_16x16x32_bf16 a[76:79], a[228:231], v[68:71], a[76:79]\n"
    "ds_read_b64_tr_b16 a[186:187], v208 offset:25792\n"
    "v_mfma_f32_16x16x32_bf16 a[76:79], a[232:235], v[72:75], a[76:79]\n"
    "ds_read_b64_tr_b16 a[188:189], v208 offset:30912\n"
    "v_mfma_f32_16x16x32_bf16 a[76:79], a[236:239], v[76:79], a[76:79]\n"
    "ds_read_b64_tr_b16 a[190:191], v208 offset:36032\n"
    "s_waitcnt lgkmcnt(8)\n"
    "v_mfma_f32_16x16x32_bf16 a[80:83], a[160:163], v[16:19], a[80:83]\n"
    "buffer_load_dwordx4 v[160:163], v226, s[36:39], s70 offen offset:0 sc1\n"
    "v_mfma_f32_16x16x32_bf16 a[80:83], a[164:167], v[20:23], a[80:83]\n"
    "v_mfma_f32_16x16x32_bf16 a[80:83], a[168:171], v[24:27], a[80:83]\n"
    "v_mfma_f32_16x16x32_bf16 a[80:83], a[172:175], v[28:31], a[80:83]\n"
    "v_mfma_f32_16x16x32_bf16 a[84:87], a[160:163], v[32:35], a[84:87]\n"
    "v_mfma_f32_16x16x32_bf16 a[84:87], a[164:167], v[36:39], a[84:87]\n"
    "v_mfma_f32_16x16x32_bf16 a[84:87], a[168:171], v[40:43], a[84:87]\n"
    "buffer_load_dwordx4 v[164:167], v226, s[36:39], s70 offen offset:1024 sc1\n"
    "v_mfma_f32_16x16x32_bf16 a[84:87], a[172:175], v[44:47], a[84:87]\n"
    "v_mfma_f32_16x16x32_bf16 a[88:91], a[160:163], v[48:51], a[88:91]\n"
    "ds_read_b64_tr_b16 a[192:193], v209 offset:224\n"
    "v_mfma_f32_16x16x32_bf16 a[88:91], a[164:167], v[52:55], a[88:91]\n"
    "ds_read_b64_tr_b16 a[194:195], v209 offset:5344\n"
    "v_mfma_f32_16x16x32_bf16 a[88:91], a[168:171], v[56:59], a[88:91]\n"
    "ds_read_b64_tr_b16 a[196:197], v209 offset:10464\n"
    "v_mfma_f32_16x16x32_bf16 a[88:91], a[172:175], v[60:63], a[88:91]\n"
    "ds_read_b64_tr_b16 a[198:199], v209 offset:15584\n"
    "v_mfma_f32_16x16x32_bf16 a[92:95], a[160:163], v[64:67], a[92:95]\n"
    "ds_read_b64_tr_b16 a[200:201], v209 offset:20704\n"
    "v_mfma_f32_16x16x32_bf16 a[92:95], a[164:167], v[68:71], a[92:95]\n"
    "ds_read_b64_tr_b16 a[202:203], v209 offset:25824\n"
    "v_mfma_f32_16x16x32_bf16 a[92:95], a[168:171], v[72:75], a[92:95]\n"
    "ds_read_b64_tr_b16 a[204:205], v209 offset:30944\n"
    "v_mfma_f32_16x16x32_bf16 a[92:95], a[172:175], v[76:79], a[92:95]\n"
    "ds_read_b64_tr_b16 a[206:207], v209 offset:36064\n"
    "s_waitcnt lgkmcnt(8)\n"
    "v_mfma_f32_16x16x32_bf16 a[96:99], a[176:179], v[16:19], a[96:99]\n"
    "buffer_load_dwordx4 v[168:171], v226, s[36:39], s70 offen offset:2048 sc1\n"
    "v_mfma_f32_16x16x32_bf16 a[96:99], a[180:183], v[20:23], a[96:99]\n"
    "v_mfma_f32_16x16x32_bf16 a[96:99], a[184:187], v[24:27], a[96:99]\n"
    "buffer_load_dwordx4 v[172:175], v226, s[36:39], s70 offen offset:3072 sc1\n"
    "v_mfma_f32_16x16x32_bf16 a[96:99], a[188:191], v[28:31], a[96:99]\n"
    "v_mfma_f32_16x16x32_bf16 a[100:103], a[176:179], v[32:35], a[100:103]\n"
    "v_mfma_f32_16x16x32_bf16 a[100:103], a[180:183], v[36:39], a[100:103]\n"
    "v_mfma_f32_16x16x32_bf16 a[100:103], a[184:187], v[40:43], a[100:103]\n"
    "v_mfma_f32_16x16x32_bf16 a[100:103], a[188:191], v[44:47], a[100:103]\n"
    "buffer_load_dwordx4 v[176:179], v226, s[36:39], s71 offen offset:0 sc1\n"
    "v_mfma_f32_16x16x32_bf16 a[104:107], a[176:179], v[48:51], a[104:107]\n"
    "ds_read_b64_tr_b16 a[208:209], v208 offset:256\n"
    "v_mfma_f32_16x16x32_bf16 a[104:107], a[180:183], v[52:55], a[104:107]\n"
    "ds_read_b64_tr_b16 a[210:211], v208 offset:5376\n"
    "v_mfma_f32_16x16x32_bf16 a[104:107], a[184:187], v[56:59], a[104:107]\n"
    "ds_read_b64_tr_b16 a[212:213], v208 offset:10496\n"
    "v_mfma_f32_16x16x32_bf16 a[104:107], a[188:191], v[60:63], a[104:107]\n"
    "ds_read_b64_tr_b16 a[214:215], v208 offset:15616\n"
    "v_mfma_f32_16x16x32_bf16 a[108:111], a[176:179], v[64:67], a[108:111]\n"
    "ds_read_b64_tr_b16 a[216:217], v208 offset:20736\n"
    "v_mfma_f32_16x16x32_bf16 a[108:111], a[180:183], v[68:71], a[108:111]\n"
    "ds_read_b64_tr_b16 a[218:219], v208 offset:25856\n"
    "v_mfma_f32_16x16x32_bf16 a[108:111], a[184:187], v[72:75], a[108:111]\n"
    "ds_read_b64_tr_b16 a[220:221], v208 offset:30976\n"
    "v_mfma_f32_16x16x32_bf16 a[108:111], a[188:191], v[76:79], a[108:111]\n"
    "ds_read_b64_tr_b16 a[222:223], v208 offset:36096\n"
    "s_waitcnt lgkmcnt(8)\n"
    "v_mfma_f32_16x16x32_bf16 a[112:115], a[192:195], v[16:19], a[112:115]\n"
    "buffer_load_dwordx4 v[180:183], v226, s[36:39], s71 offen offset:1024 sc1\n"
    "v_mfma_f32_16x16x32_bf16 a[112:115], a[196:199], v[20:23], a[112:115]\n"
    "v_mfma_f32_16x16x32_bf16 a[112:115], a[200:203], v[24:27], a[112:115]\n"
    "v_mfma_f32_16x16x32_bf16 a[112:115], a[204:207], v[28:31], a[112:115]\n"
    "buffer_load_dwordx4 v[184:187], v226, s[36:39], s71 offen offset:2048 sc1\n"
    "v_mfma_f32_16x16x32_bf16 a[116:119], a[192:195], v[32:35], a[116:119]\n"
    "v_mfma_f32_16x16x32_bf16 a[116:119], a[196:199], v[36:39], a[116:119]\n"
    "v_mfma_f32_16x16x32_bf16 a[116:119], a[200:203], v[40:43], a[116:119]\n"
    "v_mfma_f32_16x16x32_bf16 a[116:119], a[204:207], v[44:47], a[116:119]\n"
    "v_mfma_f32_16x16x32_bf16 a[120:123], a[192:195], v[48:51], a[120:123]\n"
    "ds_read_b64_tr_b16 a[224:225], v209 offset:288\n"
    "v_mfma_f32_16x16x32_bf16 a[120:123], a[196:199], v[52:55], a[120:123]\n"
    "ds_read_b64_tr_b16 a[226:227], v209 offset:5408\n"
    "buffer_load_dwordx4 v[188:191], v226, s[36:39], s71 offen offset:3072 sc1\n"
    "v_mfma_f32_16x16x32_bf16 a[120:123], a[200:203], v[56:59], a[120:123]\n"
    "ds_read_b64_tr_b16 a[228:229], v209 offset:10528\n"
    "v_mfma_f32_16x16x32_bf16 a[120:123], a[204:207], v[60:63], a[120:123]\n"
    "ds_read_b64_tr_b16 a[230:231], v209 offset:15648\n"
    "v_mfma_f32_16x16x32_bf16 a[124:127], a[192:195], v[64:67], a[124:127]\n"
    "ds_read_b64_tr_b16 a[232:233], v209 offset:20768\n"
    "v_mfma_f32_16x16x32_bf16 a[124:127], a[196:199], v[68:71], a[124:127]\n"
    "ds_read_b64_tr_b16 a[234:235], v209 offset:25888\n"
    "v_mfma_f32_16x16x32_bf16 a[124:127], a[200:203], v[72:75], a[124:127]\n"
    "ds_read_b64_tr_b16 a[236:237], v209 offset:31008\n"
    "v_mfma_f32_16x16x32_bf16 a[124:127], a[204:207], v[76:79], a[124:127]\n"
    "ds_read_b64_tr_b16 a[238:239], v209 offset:36128\n"
    "s_waitcnt lgkmcnt(8)\n"
    "v_mfma_f32_16x16x32_bf16 a[128:131], a[208:211], v[16:19], a[128:131]\n"
    "buffer_load_dwordx4 v[192:195], v226, s[36:39], s72 offen offset:0 sc1\n"
    "v_mfma_f32_16x16x32_bf16 a[128:131], a[212:215], v[20:23], a[128:131]\n"
    "v_mfma_f32_16x16x32_bf16 a[128:131], a[216:219], v[24:27], a[128:131]\n"
    "v_mfma_f32_16x16x32_bf16 a[128:131], a[220:223], v[28:31], a[128:131]\n"
    "v_mfma_f32_16x16x32_bf16 a[132:135], a[208:211], v[32:35], a[132:135]\n"
    "buffer_load_dwordx4 v[196:199], v226, s[36:39], s72 offen offset:1024 sc1\n"
    "v_mfma_f32_16x16x32_bf16 a[132:135], a[212:215], v[36:39], a[132:135]\n"
    "v_mfma_f32_16x16x32_bf16 a[132:135], a[216:219], v[40:43], a[132:135]\n"
    "v_mfma_f32_16x16x32_bf16 a[132:135], a[220:223], v[44:47], a[132:135]\n"
    "v_mfma_f32_16x16x32_bf16 a[136:139], a[208:211], v[48:51], a[136:139]\n"
    "v_mfma_f32_16x16x32_bf16 a[136:139], a[212:215], v[52:55], a[136:139]\n"
    "v_mfma_f32_16x16x32_bf16 a[136:139], a[216:219], v[56:59], a[136:139]\n"
    "buffer_load_dwordx4 v[200:203], v226, s[36:39], s72 offen offset:2048 sc1\n"
    "v_mfma_f32_16x16x32_bf16 a[136:139], a[220:223], v[60:63], a[136:139]\n"
    "v_mfma_f32_16x16x32_bf16 a[140:143], a[208:211], v[64:67], a[140:143]\n"
    "v_mfma_f32_16x16x32_bf16 a[140:143], a[212:215], v[68:71], a[140:143]\n"
    "v_mfma_f32_16x16x32_bf16 a[140:143], a[216:219], v[72:75], a[140:143]\n"
    "v_mfma_f32_16x16x32_bf16 a[140:143], a[220:223], v[76:79], a[140:143]\n"
    "s_waitcnt vmcnt(41)\n"
    "s_waitcnt lgkmcnt(0)\n"
    "s_barrier\n"
    "v_mfma_f32_16x16x32_bf16 a[144:147], a[224:227], v[16:19], a[144:147]\n"
    "ds_read_b64_tr_b16 a[160:161], v210 offset:0\n"
    "buffer_load_dwordx4 v[204:207], v226, s[36:39], s72 offen offset:3072 sc1\n"
    "v_mfma_f32_16x16x32_bf16 a[144:147], a[228:231], v[20:23], a[144:147]\n"
    "ds_read_b64_tr_b16 a[162:163], v210 offset:5120\n"
    "v_mfma_f32_16x16x32_bf16 a[144:147], a[232:235], v[24:27], a[144:147]\n"
    "ds_read_b64_tr_b16 a[164:165], v210 offset:10240\n"
    "v_mfma_f32_16x16x32_bf16 a[144:147], a[236:239], v[28:31], a[144:147]\n"
    "ds_read_b64_tr_b16 a[166:167], v210 offset:15360\n"
    "v_mfma_f32_16x16x32_bf16 a[148:151], a[224:227], v[32:35], a[148:151]\n"
    "ds_read_b64_tr_b16 a[168:169], v210 offset:20480\n"
    "v_mfma_f32_16x16x32_bf16 a[148:151], a[228:231], v[36:39], a[148:151]\n"
    "ds_read_b64_tr_b16 a[170:171], v210 offset:25600\n"
    "v_mfma_f32_16x16x32_bf16 a[148:151], a[232:235], v[40:43], a[148:151]\n"
    "ds_read_b64_tr_b16 a[172:173], v210 offset:30720\n"
    "v_mfma_f32_16x16x32_bf16 a[148:151], a[236:239], v[44:47], a[148:151]\n"
    "ds_read_b64_tr_b16 a[174:175], v210 offset:35840\n"
    "v_mfma_f32_16x16x32_bf16 a[152:155], a[224:227], v[48:51], a[152:155]\n"
    "s_add_u32 s69, s69, 4096\n"
    "s_add_u32 s70, s70, 4096\n"
    "s_add_u32 s71, s71, 4096\n"
    "s_add_u32 s72, s72, 4096\n"
    "v_mfma_f32_16x16x32_bf16 a[152:155], a[228:231], v[52:55], a[152:155]\n"
    "v_mfma_f32_16x16x32_bf16 a[152:155], a[232:235], v[56:59], a[152:155]\n"
    "v_mfma_f32_16x16x32_bf16 a[152:155], a[236:239], v[60:63], a[152:155]\n"
    "v_mfma_f32_16x16x32_bf16 a[156:159], a[224:227], v[64:67], a[156:159]\n"
    "s_sub_u32 s74, s74, 1\n"
    "s_cmp_eq_u32 s74, 0\n"
    "s_cselect_b32 s69, s75, s69\n"
    "s_cselect_b32 s70, s75, s70\n"
    "s_cselect_b32 s71, s75, s71\n"
    "s_cselect_b32 s72, s75, s72\n"
    "v_mfma_f32_16x16x32_bf16 a[156:159], a[228:231], v[68:71], a[156:159]\n"
    "v_mfma_f32_16x16x32_bf16 a[156:159], a[232:235], v[72:75], a[156:159]\n"
    "v_mfma_f32_16x16x32_bf16 a[156:159], a[236:239], v[76:79], a[156:159]\n"
    "s_waitcnt lgkmcnt(0)\n"
    "ds_read_b64_tr_b16 a[176:177], v211 offset:32\n"
    "s_waitcnt vmcnt(26)\n"
    "v_mfma_f32_16x16x32_bf16 a[0:3], a[160:163], v[80:83], a[0:3]\n"
    "ds_read_b64_tr_b16 a[178:179], v211 offset:5152\n"
    "s_add_u32 m0, s78, 0\n"
    "v_mfma_f32_16x16x32_bf16 a[0:3], a[164:167], v[84:87], a[0:3]\n"
    "ds_read_b64_tr_b16 a[180:181], v211 offset:10272\n"
    "buffer_load_dwordx4 v214, s[40:43], s73 offen offset:0 lds\n"
    "v_mfma_f32_16x16x32_bf16 a[0:3], a[168:171], v[88:91], a[0:3]\n"
    "ds_read_b64_tr_b16 a[182:183], v211 offset:15392\n"
    "v_mfma_f32_16x16x32_bf16 a[0:3], a[172:175], v[92:95], a[0:3]\n"
    "ds_read_b64_tr_b16 a[184:185], v211 offset:20512\n"
    "v_mfma_f32_16x16x32_bf16 a[4:7], a[160:163], v[96:99], a[4:7]\n"
    "ds_read_b64_tr_b16 a[186:187], v211 offset:25632\n"
    "v_mfma_f32_16x16x32_bf16 a[4:7], a[164:167], v[100:103], a[4:7]\n"
    "ds_read_b64_tr_b16 a[188:189], v211 offset:30752\n"
    "v_mfma_f32_16x16x32_bf16 a[4:7], a[168:171], v[104:107], a[4:7]\n"
    "ds_read_b64_tr_b16 a[190:191], v211 offset:35872\n"
    "v_mfma_f32_16x16x32_bf16 a[4:7], a[172:175], v[108:111], a[4:7]\n"
    "buffer_load_dwordx4 v215, s[40:43], s73 offen offset:1024 lds\n"
    "v_mfma_f32_16x16x32_bf16 a[8:11], a[160:163], v[112:115], a[8:11]\n"
    "ds_read_b64_tr_b16 a[192:193], v210 offset:64\n"
    "v_mfma_f32_16x16x32_bf16 a[8:11], a[164:167], v[116:119], a[8:11]\n"
    "ds_read_b64_tr_b16 a[194:195], v210 offset:5184\n"
    "v_mfma_f32_16x16x32_bf16 a[8:11], a[168:171], v[120:123], a[8:11]\n"
    "ds_read_b64_tr_b16 a[196:197], v210 offset:10304\n"
    "v_mfma_f32_16x16x32_bf16 a[8:11], a[172:175], v[124:127], a[8:11]\n"
    "ds_read_b64_tr_b16 a[198:199], v210 offset:15424\n"
    "buffer_load_dwordx4 v216, s[40:43], s73 offen offset:2048 lds\n"
    "v_mfma_f32_16x16x32_bf16 a[12:15], a[160:163], v[128:131], a[12:15]\n"
    "ds_read_b64_tr_b16 a[200:201], v210 offset:20544\n"
    "v_mfma_f32_16x16x32_bf16 a[12:15], a[164:167], v[132:135], a[12:15]\n"
    "ds_read_b64_tr_b16 a[202:203], v210 offset:25664\n"
    "v_mfma_f32_16x16x32_bf16 a[12:15], a[168:171], v[136:139], a[12:15]\n"
    "ds_read_b64_tr_b16 a[204:205], v210 offset:30784\n"
    "v_mfma_f32_16x16x32_bf16 a[12:15], a[172:175], v[140:143], a[12:15]\n"
    "ds_read_b64_tr_b16 a[206:207], v210 offset:35904\n"
    "s_waitcnt lgkmcnt(8)\n"
    "v_mfma_f32_16x16x32_bf16 a[16:19], a[176:179], v[80:83], a[16:19]\n"
    "v_mfma_f32_16x16x32_bf16 a[16:19], a[180:183], v[84:87], a[16:19]\n"
    "buffer_load_dwordx4 v217, s[40:43], s73 offen offset:3072 lds\n"
    "v_mfma_f32_16x16x32_bf16 a[16:19], a[184:187], v[88:91], a[16:19]\n"
    "v_mfma_f32_16x16x32_bf16 a[16:19], a[188:191], v[92:95], a[16:19]\n"
    "v_mfma_f32_16x16x32_bf16 a[20:23], a[176:179], v[96:99], a[20:23]\n"
    "v_mfma_f32_16x16x32_bf16 a[20:23], a[180:183], v[100:103], a[20:23]\n"
    "v_mfma_f32_16x16x32_bf16 a[20:23], a[184:187], v[104:107], a[20:23]\n"
    "v_mfma_f32_16x16x32_bf16 a[20:23], a[188:191], v[108:111], a[20:23]\n"
    "s_add_u32 m0, s78, 4096\n"
    "s_nop 0\n"
    "buffer_load_dwordx4 v218, s[40:43], s73 offen offset:0 lds\n"
    "v_mfma_f32_16x16x32_bf16 a[24:27], a[176:179], v[112:115], a[24:27]\n"
    "ds_read_b64_tr_b16 a[208:209], v211 offset:96\n"
    "v_mfma_f32_16x16x32_bf16 a[24:27], a[180:183], v[116:119], a[24:27]\n"
    "ds_read_b64_tr_b16 a[210:211], v211 offset:5216\n"
    "v_mfma_f32_16x16x32_bf16 a[24:27], a[184:187], v[120:123], a[24:27]\n"
    "ds_read_b64_tr_b16 a[212:213], v211 offset:10336\n"
    "v_mfma_f32_16x16x32_bf16 a[24:27], a[188:191], v[124:127], a[24:27]\n"
    "ds_read_b64_tr_b16 a[214:215], v211 offset:15456\n"
    "v_mfma_f32_16x16x32_bf16 a[28:31], a[176:179], v[128:131], a[28:31]\n"
    "ds_read_b64_tr_b16 a[216:217], v211 offset:20576\n"
    "v_mfma_f32_16x16x32_bf16 a[28:31], a[180:183], v[132:135], a[28:31]\n"
    "ds_read_b64_tr_b16 a[218:219], v211 offset:25696\n"
    "v_mfma_f32_16x16x32_bf16 a[28:31], a[184:187], v[136:139], a[28:31]\n"
    "ds_read_b64_tr_b16 a[220:221], v211 offset:30816\n"
    "v_mfma_f32_16x16x32_bf16 a[28:31], a[188:191], v[140:143], a[28:31]\n"
    "ds_read_b64_tr_b16 a[222:223], v211 offset:35936\n"
    "s_waitcnt lgkmcnt(8)\n"
    "v_mfma_f32_16x16x32_bf16 a[32:35], a[192:195], v[80:83], a[32:35]\n"
    "buffer_load_dwordx4 v219, s[40:43], s73 offen offset:1024 lds\n"
    "v_mfma_f32_16x16x32_bf16 a[32:35], a[196:199], v[84:87], a[32:35]\n"
    "v_mfma_f32_16x16x32_bf16 a[32:35], a[200:203], v[88:91], a[32:35]\n"
    "buffer_load_dwordx4 v220, s[40:43], s73 offen offset:2048 lds\n"
    "v_mfma_f32_16x16x32_bf16 a[32:35], a[204:207], v[92:95], a[32:35]\n"
    "v_mfma_f32_16x16x32_bf16 a[36:39], a[192:195], v[96:99], a[36:39]\n"
    "v_mfma_f32_16x16x32_bf16 a[36:39], a[196:199], v[100:103], a[36:39]\n"
    "v_mfma_f32_16x16x32_bf16 a[36:39], a[200:203], v[104:107], a[36:39]\n"
    "v_mfma_f32_16x16x32_bf16 a[36:39], a[204:207], v[108:111], a[36:39]\n"
    "v_mfma_f32_16x16x32_bf16 a[40:43], a[192:195], v[112:115], a[40:43]\n"
    "ds_read_b64_tr_b16 a[224:225], v210 offset:128\n"
    "buffer_load_dwordx4 v221, s[40:43], s73 offen offset:3072 lds\n"
    "v_mfma_f32_16x16x32_bf16 a[40:43], a[196:199], v[116:119], a[40:43]\n"
    "ds_read_b64_tr_b16 a[226:227], v210 offset:5248\n"
    "v_mfma_f32_16x16x32_bf16 a[40:43], a[200:203], v[120:123], a[40:43]\n"
    "ds_read_b64_tr_b16 a[228:229], v210 offset:10368\n"
    "v_mfma_f32_16x16x32_bf16 a[40:43], a[204:207], v[124:127], a[40:43]\n"
    "ds_read_b64_tr_b16 a[230:231], v210 offset:15488\n"
    "v_mfma_f32_16x16x32_bf16 a[44:47], a[192:195], v[128:131], a[44:47]\n"
    "ds_read_b64_tr_b16 a[232:233], v210 offset:20608\n"
    "v_mfma_f32_16x16x32_bf16 a[44:47], a[196:199], v[132:135], a[44:47]\n"
    "ds_read_b64_tr_b16 a[234:235], v210 offset:25728\n"
    "v_mfma_f32_16x16x32_bf16 a[44:47], a[200:203], v[136:139], a[44:47]\n"
    "ds_read_b64_tr_b16 a[236:237], v210 offset:30848\n"
    "s_add_u32 m0, s78, 8192\n"
    "v_mfma_f32_16x16x32_bf16 a[44:47], a[204:207], v[140:143], a[44:47]\n"
    "ds_read_b64_tr_b16 a[238:239], v210 offset:35968\n"
    "s_waitcnt lgkmcnt(8)\n"
    "v_mfma_f32_16x16x32_bf16 a[48:51], a[208:211], v[80:83], a[48:51]\n"
    "buffer_load_dwordx4 v222, s[40:43], s73 offen offset:0 lds\n"
    "v_mfma_f32_16x16x32_bf16 a[48:51], a[212:215], v[84:87], a[48:51]\n"
    "v_mfma_f32_16x16x32_bf16 a[48:51], a[216:219], v[88:91], a[48:51]\n"
    "v_mfma_f32_16x16x32_bf16 a[48:51], a[220:223], v[92:95], a[48:51]\n"
    "buffer_load_dwordx4 v223, s[40:43], s73 offen offset:1024 lds\n"
    "v_mfma_f32_16x16x32_bf16 a[52:55], a[208:211], v[96:99], a[52:55]\n"
    "v_mfma_f32_16x16x32_bf16 a[52:55], a[212:215], v[100:103], a[52:55]\n"
    "v_mfma_f32_16x16x32_bf16 a[52:55], a[216:219], v[104:107], a[52:55]\n"
    "v_mfma_f32_16x16x32_bf16 a[52:55], a[220:223], v[108:111], a[52:55]\n"
    "v_mfma_f32_16x16x32_bf16 a[56:59], a[208:211], v[112:115], a[56:59]\n"
    "ds_read_b64_tr_b16 a[160:161], v211 offset:160\n"
    "v_mfma_f32_16x16x32_bf16 a[56:59], a[212:215], v[116:119], a[56:59]\n"
    "ds_read_b64_tr_b16 a[162:163], v211 offset:5280\n"
    "s_add_u32 s73, s73, 163840\n"
    "v_mfma_f32_16x16x32_bf16 a[56:59], a[216:219], v[120:123], a[56:59]\n"
    "ds_read_b64_tr_b16 a[164:165], v211 offset:10400\n"
    "buffer_load_dwordx4 v[16:19], v226, s[36:39], s69 offen offset:0 sc1\n"
    "v_mfma_f32_16x16x32_bf16 a[56:59], a[220:223], v[124:127], a[56:59]\n"
    "ds_read_b64_tr_b16 a[166:167], v211 offset:15520\n"
    "v_mfma_f32_16x16x32_bf16 a[60:63], a[208:211], v[128:131], a[60:63]\n"
    "ds_read_b64_tr_b16 a[168:169], v211 offset:20640\n"
    "v_mfma_f32_16x16x32_bf16 a[60:63], a[212:215], v[132:135], a[60:63]\n"
    "ds_read_b64_tr_b16 a[170:171], v211 offset:25760\n"
    "v_mfma_f32_16x16x32_bf16 a[60:63], a[216:219], v[136:139], a[60:63]\n"
    "ds_read_b64_tr_b16 a[172:173], v211 offset:30880\n"
    "v_mfma_f32_16x16x32_bf16 a[60:63], a[220:223], v[140:143], a[60:63]\n"
    "ds_read_b64_tr_b16 a[174:175], v211 offset:36000\n"
    "s_waitcnt lgkmcnt(8)\n"
    "v_mfma_f32_16x16x32_bf16 a[64:67], a[224:227], v[80:83], a[64:67]\n"
    "buffer_load_dwordx4 v[20:23], v226, s[36:39], s69 offen offset:1024 sc1\n"
    "v_mfma_f32_16x16x32_bf16 a[64:67], a[228:231], v[84:87], a[64:67]\n"
    "v_mfma_f32_16x16x32_bf16 a[64:67], a[232:235], v[88:91], a[64:67]\n"
    "v_mfma_f32_16x16x32_bf16 a[64:67], a[236:239], v[92:95], a[64:67]\n"
    "v_mfma_f32_16x16x32_bf16 a[68:71], a[224:227], v[96:99], a[68:71]\n"
    "v_mfma_f32_16x16x32_bf16 a[68:71], a[228:231], v[100:103], a[68:71]\n"
    "buffer_load_dwordx4 v[24:27], v226, s[36:39], s69 offen offset:2048 sc1\n"
    "v_mfma_f32_16x16x32_bf16 a[68:71], a[232:235], v[104:107], a[68:71]\n"
    "v_mfma_f32_16x16x32_bf16 a[68:71], a[236:239], v[108:111], a[68:71]\n"
    "v_mfma_f32_16x16x32_bf16 a[72:75], a[224:227], v[112:115], a[72:75]\n"
    "ds_read_b64_tr_b16 a[176:177], v210 offset:192\n"
    "v_mfma_f32_16x16x32_bf16 a[72:75], a[228:231], v[116:119], a[72:75]\n"
    "ds_read_b64_tr_b16 a[178:179], v210 offset:5312\n"
    "v_mfma_f32_16x16x32_bf16 a[72:75], a[232:235], v[120:123], a[72:75]\n"
    "ds_read_b64_tr_b16 a[180:181], v210 offset:10432\n"
    "v_mfma_f32_16x16x32_bf16 a[72:75], a[236:239], v[124:127], a[72:75]\n"
    "ds_read_b64_tr_b16 a[182:183], v210 offset:15552\n"
    "buffer_load_dwordx4 v[28:31], v226, s[36:39], s69 offen offset:3072 sc1\n"
    "v_mfma_f32_16x16x32_bf16 a[76:79], a[224:227], v[128:131], a[76:79]\n"
    "ds_read_b64_tr_b16 a[184:185], v210 offset:20672\n"
    "v_mfma_f32_16x16x32_bf16 a[76:79], a[228:231], v[132:135], a[76:79]\n"
    "ds_read_b64_tr_b16 a[186:187], v210 offset:25792\n"
    "v_mfma_f32_16x16x32_bf16 a[76:79], a[232:235], v[136:139], a[76:79]\n"
    "ds_read_b64_tr_b16 a[188:189], v210 offset:30912\n"
    "v_mfma_f32_16x16x32_bf16 a[76:79], a[236:239], v[140:143], a[76:79]\n"
    "ds_read_b64_tr_b16 a[190:191], v210 offset:36032\n"
    "s_waitcnt lgkmcnt(8)\n"
    "v_mfma_f32_16x16x32_bf16 a[80:83], a[160:163], v[80:83], a[80:83]\n"
    "buffer_load_dwordx4 v[32:35], v226, s[36:39], s70 offen offset:0 sc1\n"
    "v_mfma_f32_16x16x32_bf16 a[80:83], a[164:167], v[84:87], a[80:83]\n"
    "v_mfma_f32_16x16x32_bf16 a[80:83], a[168:171], v[88:91], a[80:83]\n"
    "v_mfma_f32_16x16x32_bf16 a[80:83], a[172:175], v[92:95], a[80:83]\n"
    "v_mfma_f32_16x16x32_bf16 a[84:87], a[160:163], v[96:99], a[84:87]\n"
    "v_mfma_f32_16x16x32_bf16 a[84:87], a[164:167], v[100:103], a[84:87]\n"
    "v_mfma_f32_16x16x32_bf16 a[84:87], a[168:171], v[104:107], a[84:87]\n"
    "buffer_load_dwordx4 v[36:39], v226, s[36:39], s70 offen offset:1024 sc1\n"
    "v_mfma_f32_16x16x32_bf16 a[84:87], a[172:175], v[108:111], a[84:87]\n"
    "v_mfma_f32_16x16x32_bf16 a[88:91], a[160:163], v[112:115], a[88:91]\n"
    "ds_read_b64_tr_b16 a[192:193], v211 offset:224\n"
    "v_mfma_f32_16x16x32_bf16 a[88:91], a[164:167], v[116:119], a[88:91]\n"
    "ds_read_b64_tr_b16 a[194:195], v211 offset:5344\n"
    "v_mfma_f32_16x16x32_bf16 a[88:91], a[168:171], v[120:123], a[88:91]\n"
    "ds_read_b64_tr_b16 a[196:197], v211 offset:10464\n"
    "v_mfma_f32_16x16x32_bf16 a[88:91], a[172:175], v[124:127], a[88:91]\n"
    "ds_read_b64_tr_b16 a[198:199], v211 offset:15584\n"
    "v_mfma_f32_16x16x32_bf16 a[92:95], a[160:163], v[128:131], a[92:95]\n"
    "ds_read_b64_tr_b16 a[200:201], v211 offset:20704\n"
    "v_mfma_f32_16x16x32_bf16 a[92:95], a[164:167], v[132:135], a[92:95]\n"
    "ds_read_b64_tr_b16 a[202:203], v211 offset:25824\n"
    "v_mfma_f32_16x16x32_bf16 a[92:95], a[168:171], v[136:139], a[92:95]\n"
    "ds_read_b64_tr_b16 a[204:205], v211 offset:30944\n"
    "v_mfma_f32_16x16x32_bf16 a[92:95], a[172:175], v[140:143], a[92:95]\n"
    "ds_read_b64_tr_b16 a[206:207], v211 offset:36064\n"
    "s_waitcnt lgkmcnt(8)\n"
    "v_mfma_f32_16x16x32_bf16 a[96:99], a[176:179], v[80:83], a[96:99]\n"
    "buffer_load_dwordx4 v[40:43], v226, s[36:39], s70 offen offset:2048 sc1\n"
    "v_mfma_f32_16x16x32_bf16 a[96:99], a[180:183], v[84:87], a[96:99]\n"
    "v_mfma_f32_16x16x32_bf16 a[96:99], a[184:187], v[88:91], a[96:99]\n"
    "buffer_load_dwordx4 v[44:47], v226, s[36:39], s70 offen offset:3072 sc1\n"
    "v_mfma_f32_16x16x32_bf16 a[96:99], a[188:191], v[92:95], a[96:99]\n"
    "v_mfma_f32_16x16x32_bf16 a[100:103], a[176:179], v[96:99], a[100:103]\n"
    "v_mfma_f32_16x16x32_bf16 a[100:103], a[180:183], v[100:103], a[100:103]\n"
    "v_mfma_f32_16x16x32_bf16 a[100:103], a[184:187], v[104:107], a[100:103]\n"
    "v_mfma_f32_16x16x32_bf16 a[100:103], a[188:191], v[108:111], a[100:103]\n"
    "buffer_load_dwordx4 v[48:51], v226, s[36:39], s71 offen offset:0 sc1\n"
    "v_mfma_f32_16x16x32_bf16 a[104:107], a[176:179], v[112:115], a[104:107]\n"
    "ds_read_b64_tr_b16 a[208:209], v210 offset:256\n"
    "v_mfma_f32_16x16x32_bf16 a[104:107], a[180:183], v[116:119], a[104:107]\n"
    "ds_read_b64_tr_b16 a[210:211], v210 offset:5376\n"
    "v_mfma_f32_16x16x32_bf16 a[104:107], a[184:187], v[120:123], a[104:107]\n"
    "ds_read_b64_tr_b16 a[212:213], v210 offset:10496\n"
    "v_mfma_f32_16x16x32_bf16 a[104:107], a[188:191], v[124:127], a[104:107]\n"
    "ds_read_b64_tr_b16 a[214:215], v210 offset:15616\n"
    "v_mfma_f32_16x16x32_bf16 a[108:111], a[176:179], v[128:131], a[108:111]\n"
    "ds_read_b64_tr_b16 a[216:217], v210 offset:20736\n"
    "v_mfma_f32_16x16x32_bf16 a[108:111], a[180:183], v[132:135], a[108:111]\n"
    "ds_read_b64_tr_b16 a[218:219], v210 offset:25856\n"
    "v_mfma_f32_16x16x32_bf16 a[108:111], a[184:187], v[136:139], a[108:111]\n"
    "ds_read_b64_tr_b16 a[220:221], v210 offset:30976\n"
    "v_mfma_f32_16x16x32_bf16 a[108:111], a[188:191], v[140:143], a[108:111]\n"
    "ds_read_b64_tr_b16 a[222:223], v210 offset:36096\n"
    "s_waitcnt lgkmcnt(8)\n"
    "v_mfma_f32_16x16x32_bf16 a[112:115], a[192:195], v[80:83], a[112:115]\n"
    "buffer_load_dwordx4 v[52:55], v226, s[36:39], s71 offen offset:1024 sc1\n"
    "v_mfma_f32_16x16x32_bf16 a[112:115], a[196:199], v[84:87], a[112:115]\n"
    "v_mfma_f32_16x16x32_bf16 a[112:115], a[200:203], v[88:91], a[112:115]\n"
    "v_mfma_f32_16x16x32_bf16 a[112:115], a[204:207], v[92:95], a[112:115]\n"
    "buffer_load_dwordx4 v[56:59], v226, s[36:39], s71 offen offset:2048 sc1\n"
    "v_mfma_f32_16x16x32_bf16 a[116:119], a[192:195], v[96:99], a[116:119]\n"
    "v_mfma_f32_16x16x32_bf16 a[116:119], a[196:199], v[100:103], a[116:119]\n"
    "v_mfma_f32_16x16x32_bf16 a[116:119], a[200:203], v[104:107], a[116:119]\n"
    "v_mfma_f32_16x16x32_bf16 a[116:119], a[204:207], v[108:111], a[116:119]\n"
    "v_mfma_f32_16x16x32_bf16 a[120:123], a[192:195], v[112:115], a[120:123]\n"
    "ds_read_b64_tr_b16 a[224:225], v211 offset:288\n"
    "v_mfma_f32_16x16x32_bf16 a[120:123], a[196:199], v[116:119], a[120:123]\n"
    "ds_read_b64_tr_b16 a[226:227], v211 offset:5408\n"
    "buffer_load_dwordx4 v[60:63], v226, s[36:39], s71 offen offset:3072 sc1\n"
    "v_mfma_f32_16x16x32_bf16 a[120:123], a[200:203], v[120:123], a[120:123]\n"
    "ds_read_b64_tr_b16 a[228:229], v211 offset:10528\n"
    "v_mfma_f32_16x16x32_bf16 a[120:123], a[204:207], v[124:127], a[120:123]\n"
    "ds_read_b64_tr_b16 a[230:231], v211 offset:15648\n"
    "v_mfma_f32_16x16x32_bf16 a[124:127], a[192:195], v[128:131], a[124:127]\n"
    "ds_read_b64_tr_b16 a[232:233], v211 offset:20768\n"
    "v_mfma_f32_16x16x32_bf16 a[124:127], a[196:199], v[132:135], a[124:127]\n"
    "ds_read_b64_tr_b16 a[234:235], v211 offset:25888\n"
    "v_mfma_f32_16x16x32_bf16 a[124:127], a[200:203], v[136:139], a[124:127]\n"
    "ds_read_b64_tr_b16 a[236:237], v211 offset:31008\n"
    "v_mfma_f32_16x16x32_bf16 a[124:127], a[204:207], v[140:143], a[124:127]\n"
    "ds_read_b64_tr_b16 a[238:239], v211 offset:36128\n"
    "s_waitcnt lgkmcnt(8)\n"
    "v_mfma_f32_16x16x32_bf16 a[128:131], a[208:211], v[80:83], a[128:131]\n"
    "buffer_load_dwordx4 v[64:67], v226, s[36:39], s72 offen offset:0 sc1\n"
    "v_mfma_f32_16x16x32_bf16 a[128:131], a[212:215], v[84:87], a[128:131]\n"
    "v_mfma_f32_16x16x32_bf16 a[128:131], a[216:219], v[88:91], a[128:131]\n"
    "v_mfma_f32_16x16x32_bf16 a[128:131], a[220:223], v[92:95], a[128:131]\n"
    "v_mfma_f32_16x16x32_bf16 a[132:135], a[208:211], v[96:99], a[132:135]\n"
    "buffer_load_dwordx4 v[68:71], v226, s[36:39], s72 offen offset:1024 sc1\n"
    "v_mfma_f32_16x16x32_bf16 a[132:135], a[212:215], v[100:103], a[132:135]\n"
    "v_mfma_f32_16x16x32_bf16 a[132:135], a[216:219], v[104:107], a[132:135]\n"
    "v_mfma_f32_16x16x32_bf16 a[132:135], a[220:223], v[108:111], a[132:135]\n"
    "v_mfma_f32_16x16x32_bf16 a[136:139], a[208:211], v[112:115], a[136:139]\n"
    "v_mfma_f32_16x16x32_bf16 a[136:139], a[212:215], v[116:119], a[136:139]\n"
    "v_mfma_f32_16x16x32_bf16 a[136:139], a[216:219], v[120:123], a[136:139]\n"
    "buffer_load_dwordx4 v[72:75], v226, s[36:39], s72 offen offset:2048 sc1\n"
    "v_mfma_f32_16x16x32_bf16 a[136:139], a[220:223], v[124:127], a[136:139]\n"
    "v_mfma_f32_16x16x32_bf16 a[140:143], a[208:211], v[128:131], a[140:143]\n"
    "v_mfma_f32_16x16x32_bf16 a[140:143], a[212:215], v[132:135], a[140:143]\n"
    "v_mfma_f32_16x16x32_bf16 a[140:143], a[216:219], v[136:139], a[140:143]\n"
    "v_mfma_f32_16x16x32_bf16 a[140:143], a[220:223], v[140:143], a[140:143]\n"
    "s_waitcnt vmcnt(41)\n"
    "s_waitcnt lgkmcnt(0)\n"
    "s_barrier\n"
    "v_mfma_f32_16x16x32_bf16 a[144:147], a[224:227], v[80:83], a[144:147]\n"
    "ds_read_b64_tr_b16 a[160:161], v212 offset:0\n"
    "buffer_load_dwordx4 v[76:79], v226, s[36:39], s72 offen offset:3072 sc1\n"
    "v_mfma_f32_16x16x32_bf16 a[144:147], a[228:231], v[84:87], a[144:147]\n"
    "ds_read_b64_tr_b16 a[162:163], v212 offset:5120\n"
    "v_mfma_f32_16x16x32_bf16 a[144:147], a[232:235], v[88:91], a[144:147]\n"
    "ds_read_b64_tr_b16 a[164:165], v212 offset:10240\n"
    "v_mfma_f32_16x16x32_bf16 a[144:147], a[236:239], v[92:95], a[144:147]\n"
    "ds_read_b64_tr_b16 a[166:167], v212 offset:15360\n"
    "v_mfma_f32_16x16x32_bf16 a[148:151], a[224:227], v[96:99], a[148:151]\n"
    "ds_read_b64_tr_b16 a[168:169], v212 offset:20480\n"
    "v_mfma_f32_16x16x32_bf16 a[148:151], a[228:231], v[100:103], a[148:151]\n"
    "ds_read_b64_tr_b16 a[170:171], v212 offset:25600\n"
    "v_mfma_f32_16x16x32_bf16 a[148:151], a[232:235], v[104:107], a[148:151]\n"
    "ds_read_b64_tr_b16 a[172:173], v212 offset:30720\n"
    "v_mfma_f32_16x16x32_bf16 a[148:151], a[236:239], v[108:111], a[148:151]\n"
    "ds_read_b64_tr_b16 a[174:175], v212 offset:35840\n"
    "v_mfma_f32_16x16x32_bf16 a[152:155], a[224:227], v[112:115], a[152:155]\n"
    "s_add_u32 s69, s69, 4096\n"
    "s_add_u32 s70, s70, 4096\n"
    "s_add_u32 s71, s71, 4096\n"
    "s_add_u32 s72, s72, 4096\n"
    "v_mfma_f32_16x16x32_bf16 a[152:155], a[228:231], v[116:119], a[152:155]\n"
    "v_mfma_f32_16x16x32_bf16 a[152:155], a[232:235], v[120:123], a[152:155]\n"
    "v_mfma_f32_16x16x32_bf16 a[152:155], a[236:239], v[124:127], a[152:155]\n"
    "v_mfma_f32_16x16x32_bf16 a[156:159], a[224:227], v[128:131], a[156:159]\n"
    "s_sub_u32 s74, s74, 1\n"
    "s_cmp_eq_u32 s74, 0\n"
    "s_cselect_b32 s69, s75, s69\n"
    "s_cselect_b32 s70, s75, s70\n"
    "s_cselect_b32 s71, s75, s71\n"
    "s_cselect_b32 s72, s75, s72\n"
    "v_mfma_f32_16x16x32_bf16 a[156:159], a[228:231], v[132:135], a[156:159]\n"
    "v_mfma_f32_16x16x32_bf16 a[156:159], a[232:235], v[136:139], a[156:159]\n"
    "v_mfma_f32_16x16x32_bf16 a[156:159], a[236:239], v[140:143], a[156:159]\n"
    "s_waitcnt lgkmcnt(0)\n"
    "ds_read_b64_tr_b16 a[176:177], v213 offset:32\n"
    "s_waitcnt vmcnt(26)\n"
    "v_mfma_f32_16x16x32_bf16 a[0:3], a[160:163], v[144:147], a[0:3]\n"
    "ds_read_b64_tr_b16 a[178:179], v213 offset:5152\n"
    "s_add_u32 m0, s78, 40960\n"
    "v_mfma_f32_16x16x32_bf16 a[0:3], a[164:167], v[148:151], a[0:3]\n"
    "ds_read_b64_tr_b16 a[180:181], v213 offset:10272\n"
    "buffer_load_dwordx4 v214, s[40:43], s73 offen offset:0 lds\n"
    "v_mfma_f32_16x16x32_bf16 a[0:3], a[168:171], v[152:155], a[0:3]\n"
    "ds_read_b64_tr_b16 a[182:183], v213 offset:15392\n"
    "v_mfma_f32_16x16x32_bf16 a[0:3], a[172:175], v[156:159], a[0:3]\n"
    "ds_read_b64_tr_b16 a[184:185], v213 offset:20512\n"
    "v_mfma_f32_16x16x32_bf16 a[4:7], a[160:163], v[160:163], a[4:7]\n"
    "ds_read_b64_tr_b16 a[186:187], v213 offset:25632\n"
    "v_mfma_f32_16x16x32_bf16 a[4:7], a[164:167], v[164:167], a[4:7]\n"
    "ds_read_b64_tr_b16 a[188:189], v213 offset:30752\n"
    "v_mfma_f32_16x16x32_bf16 a[4:7], a[168:171], v[168:171], a[4:7]\n"
    "ds_read_b64_tr_b16 a[190:191], v213 offset:35872\n"
    "v_mfma_f32_16x16x32_bf16 a[4:7], a[172:175], v[172:175], a[4:7]\n"
    "buffer_load_dwordx4 v215, s[40:43], s73 offen offset:1024 lds\n"
    "v_mfma_f32_16x16x32_bf16 a[8:11], a[160:163], v[176:179], a[8:11]\n"
    "ds_read_b64_tr_b16 a[192:193], v212 offset:64\n"
    "v_mfma_f32_16x16x32_bf16 a[8:11], a[164:167], v[180:183], a[8:11]\n"
    "ds_read_b64_tr_b16 a[194:195], v212 offset:5184\n"
    "v_mfma_f32_16x16x32_bf16 a[8:11], a[168:171], v[184:187], a[8:11]\n"
    "ds_read_b64_tr_b16 a[196:197], v212 offset:10304\n"
    "v_mfma_f32_16x16x32_bf16 a[8:11], a[172:175], v[188:191], a[8:11]\n"
    "ds_read_b64_tr_b16 a[198:199], v212 offset:15424\n"
    "buffer_load_dwordx4 v216, s[40:43], s73 offen offset:2048 lds\n"
    "v_mfma_f32_16x16x32_bf16 a[12:15], a[160:163], v[192:195], a[12:15]\n"
    "ds_read_b64_tr_b16 a[200:201], v212 offset:20544\n"
    "v_mfma_f32_16x16x32_bf16 a[12:15], a[164:167], v[196:199], a[12:15]\n"
    "ds_read_b64_tr_b16 a[202:203], v212 offset:25664\n"
    "v_mfma_f32_16x16x32_bf16 a[12:15], a[168:171], v[200:203], a[12:15]\n"
    "ds_read_b64_tr_b16 a[204:205], v212 offset:30784\n"
    "v_mfma_f32_16x16x32_bf16 a[12:15], a[172:175], v[204:207], a[12:15]\n"
    "ds_read_b64_tr_b16 a[206:207], v212 offset:35904\n"
    "s_waitcnt lgkmcnt(8)\n"
    "v_mfma_f32_16x16x32_bf16 a[16:19], a[176:179], v[144:147], a[16:19]\n"
    "v_mfma_f32_16x16x32_bf16 a[16:19], a[180:183], v[148:151], a[16:19]\n"
    "buffer_load_dwordx4 v217, s[40:43], s73 offen offset:3072 lds\n"
    "v_mfma_f32_16x16x32_bf16 a[16:19], a[184:187], v[152:155], a[16:19]\n"
    "v_mfma_f32_16x16x32_bf16 a[16:19], a[188:191], v[156:159], a[16:19]\n"
    "v_mfma_f32_16x16x32_bf16 a[20:23], a[176:179], v[160:163], a[20:23]\n"
    "v_mfma_f32_16x16x32_bf16 a[20:23], a[180:183], v[164:167], a[20:23]\n"
    "v_mfma_f32_16x16x32_bf16 a[20:23], a[184:187], v[168:171], a[20:23]\n"
    "v_mfma_f32_16x16x32_bf16 a[20:23], a[188:191], v[172:175], a[20:23]\n"
    "s_add_u32 m0, s78, 45056\n"
    "s_nop 0\n"
    "buffer_load_dwordx4 v218, s[40:43], s73 offen offset:0 lds\n"
    "v_mfma_f32_16x16x32_bf16 a[24:27], a[176:179], v[176:179], a[24:27]\n"
    "ds_read_b64_tr_b16 a[208:209], v213 offset:96\n"
    "v_mfma_f32_16x16x32_bf16 a[24:27], a[180:183], v[180:183], a[24:27]\n"
    "ds_read_b64_tr_b16 a[210:211], v213 offset:5216\n"
    "v_mfma_f32_16x16x32_bf16 a[24:27], a[184:187], v[184:187], a[24:27]\n"
    "ds_read_b64_tr_b16 a[212:213], v213 offset:10336\n"
    "v_mfma_f32_16x16x32_bf16 a[24:27], a[188:191], v[188:191], a[24:27]\n"
    "ds_read_b64_tr_b16 a[214:215], v213 offset:15456\n"
    "v_mfma_f32_16x16x32_bf16 a[28:31], a[176:179], v[192:195], a[28:31]\n"
    "ds_read_b64_tr_b16 a[216:217], v213 offset:20576\n"
    "v_mfma_f32_16x16x32_bf16 a[28:31], a[180:183], v[196:199], a[28:31]\n"
    "ds_read_b64_tr_b16 a[218:219], v213 offset:25696\n"
    "v_mfma_f32_16x16x32_bf16 a[28:31], a[184:187], v[200:203], a[28:31]\n"
    "ds_read_b64_tr_b16 a[220:221], v213 offset:30816\n"
    "v_mfma_f32_16x16x32_bf16 a[28:31], a[188:191], v[204:207], a[28:31]\n"
    "ds_read_b64_tr_b16 a[222:223], v213 offset:35936\n"
    "s_waitcnt lgkmcnt(8)\n"
    "v_mfma_f32_16x16x32_bf16 a[32:35], a[192:195], v[144:147], a[32:35]\n"
    "buffer_load_dwordx4 v219, s[40:43], s73 offen offset:1024 lds\n"
    "v_mfma_f32_16x16x32_bf16 a[32:35], a[196:199], v[148:151], a[32:35]\n"
    "v_mfma_f32_16x16x32_bf16 a[32:35], a[200:203], v[152:155], a[32:35]\n"
    "buffer_load_dwordx4 v220, s[40:43], s73 offen offset:2048 lds\n"
    "v_mfma_f32_16x16x32_bf16 a[32:35], a[204:207], v[156:159], a[32:35]\n"
    "v_mfma_f32_16x16x32_bf16 a[36:39], a[192:195], v[160:163], a[36:39]\n"
    "v_mfma_f32_16x16x32_bf16 a[36:39], a[196:199], v[164:167], a[36:39]\n"
    "v_mfma_f32_16x16x32_bf16 a[36:39], a[200:203], v[168:171], a[36:39]\n"
    "v_mfma_f32_16x16x32_bf16 a[36:39], a[204:207], v[172:175], a[36:39]\n"
    "v_mfma_f32_16x16x32_bf16 a[40:43], a[192:195], v[176:179], a[40:43]\n"
    "ds_read_b64_tr_b16 a[224:225], v212 offset:128\n"
    "buffer_load_dwordx4 v221, s[40:43], s73 offen offset:3072 lds\n"
    "v_mfma_f32_16x16x32_bf16 a[40:43], a[196:199], v[180:183], a[40:43]\n"
    "ds_read_b64_tr_b16 a[226:227], v212 offset:5248\n"
    "v_mfma_f32_16x16x32_bf16 a[40:43], a[200:203], v[184:187], a[40:43]\n"
    "ds_read_b64_tr_b16 a[228:229], v212 offset:10368\n"
    "v_mfma_f32_16x16x32_bf16 a[40:43], a[204:207], v[188:191], a[40:43]\n"
    "ds_read_b64_tr_b16 a[230:231], v212 offset:15488\n"
    "v_mfma_f32_16x16x32_bf16 a[44:47], a[192:195], v[192:195], a[44:47]\n"
    "ds_read_b64_tr_b16 a[232:233], v212 offset:20608\n"
    "v_mfma_f32_16x16x32_bf16 a[44:47], a[196:199], v[196:199], a[44:47]\n"
    "ds_read_b64_tr_b16 a[234:235], v212 offset:25728\n"
    "v_mfma_f32_16x16x32_bf16 a[44:47], a[200:203], v[200:203], a[44:47]\n"
    "ds_read_b64_tr_b16 a[236:237], v212 offset:30848\n"
    "s_add_u32 m0, s78, 49152\n"
    "v_mfma_f32_16x16x32_bf16 a[44:47], a[204:207], v[204:207], a[44:47]\n"
    "ds_read_b64_tr_b16 a[238:239], v212 offset:35968\n"
    "s_waitcnt lgkmcnt(8)\n"
    "v_mfma_f32_16x16x32_bf16 a[48:51], a[208:211], v[144:147], a[48:51]\n"
    "buffer_load_dwordx4 v222, s[40:43], s73 offen offset:0 lds\n"
    "v_mfma_f32_16x16x32_bf16 a[48:51], a[212:215], v[148:151], a[48:51]\n"
    "v_mfma_f32_16x16x32_bf16 a[48:51], a[216:219], v[152:155], a[48:51]\n"
    "v_mfma_f32_16x16x32_bf16 a[48:51], a[220:223], v[156:159], a[48:51]\n"
    "buffer_load_dwordx4 v223, s[40:43], s73 offen offset:1024 lds\n"
    "v_mfma_f32_16x16x32_bf16 a[52:55], a[208:211], v[160:163], a[52:55]\n"
    "v_mfma_f32_16x16x32_bf16 a[52:55], a[212:215], v[164:167], a[52:55]\n"
    "v_mfma_f32_16x16x32_bf16 a[52:55], a[216:219], v[168:171], a[52:55]\n"
    "v_mfma_f32_16x16x32_bf16 a[52:55], a[220:223], v[172:175], a[52:55]\n"
    "v_mfma_f32_16x16x32_bf16 a[56:59], a[208:211], v[176:179], a[56:59]\n"
    "ds_read_b64_tr_b16 a[160:161], v213 offset:160\n"
    "v_mfma_f32_16x16x32_bf16 a[56:59], a[212:215], v[180:183], a[56:59]\n"
    "ds_read_b64_tr_b16 a[162:163], v213 offset:5280\n"
    "s_add_u32 s73, s73, 163840\n"
    "v_mfma_f32_16x16x32_bf16 a[56:59], a[216:219], v[184:187], a[56:59]\n"
    "ds_read_b64_tr_b16 a[164:165], v213 offset:10400\n"
    "buffer_load_dwordx4 v[80:83], v226, s[36:39], s69 offen offset:0 sc1\n"
    "v_mfma_f32_16x16x32_bf16 a[56:59], a[220:223], v[188:191], a[56:59]\n"
    "ds_read_b64_tr_b16 a[166:167], v213 offset:15520\n"
    "v_mfma_f32_16x16x32_bf16 a[60:63], a[208:211], v[192:195], a[60:63]\n"
    "ds_read_b64_tr_b16 a[168:169], v213 offset:20640\n"
    "v_mfma_f32_16x16x32_bf16 a[60:63], a[212:215], v[196:199], a[60:63]\n"
    "ds_read_b64_tr_b16 a[170:171], v213 offset:25760\n"
    "v_mfma_f32_16x16x32_bf16 a[60:63], a[216:219], v[200:203], a[60:63]\n"
    "ds_read_b64_tr_b16 a[172:173], v213 offset:30880\n"
    "v_mfma_f32_16x16x32_bf16 a[60:63], a[220:223], v[204:207], a[60:63]\n"
    "ds_read_b64_tr_b16 a[174:175], v213 offset:36000\n"
    "s_waitcnt lgkmcnt(8)\n"
    "v_mfma_f32_16x16x32_bf16 a[64:67], a[224:227], v[144:147], a[64:67]\n"
    "buffer_load_dwordx4 v[84:87], v226, s[36:39], s69 offen offset:1024 sc1\n"
    "v_mfma_f32_16x16x32_bf16 a[64:67], a[228:231], v[148:151], a[64:67]\n"
    "v_mfma_f32_16x16x32_bf16 a[64:67], a[232:235], v[152:155], a[64:67]\n"
    "v_mfma_f32_16x16x32_bf16 a[64:67], a[236:239], v[156:159], a[64:67]\n"
    "v_mfma_f32_16x16x32_bf16 a[68:71], a[224:227], v[160:163], a[68:71]\n"
    "v_mfma_f32_16x16x32_bf16 a[68:71], a[228:231], v[164:167], a[68:71]\n"
    "buffer_load_dwordx4 v[88:91], v226, s[36:39], s69 offen offset:2048 sc1\n"
    "v_mfma_f32_16x16x32_bf16 a[68:71], a[232:235], v[168:171], a[68:71]\n"
    "v_mfma_f32_16x16x32_bf16 a[68:71], a[236:239], v[172:175], a[68:71]\n"
    "v_mfma_f32_16x16x32_bf16 a[72:75], a[224:227], v[176:179], a[72:75]\n"
    "ds_read_b64_tr_b16 a[176:177], v212 offset:192\n"
    "v_mfma_f32_16x16x32_bf16 a[72:75], a[228:231], v[180:183], a[72:75]\n"
    "ds_read_b64_tr_b16 a[178:179], v212 offset:5312\n"
    "v_mfma_f32_16x16x32_bf16 a[72:75], a[232:235], v[184:187], a[72:75]\n"
    "ds_read_b64_tr_b16 a[180:181], v212 offset:10432\n"
    "v_mfma_f32_16x16x32_bf16 a[72:75], a[236:239], v[188:191], a[72:75]\n"
    "ds_read_b64_tr_b16 a[182:183], v212 offset:15552\n"
    "buffer_load_dwordx4 v[92:95], v226, s[36:39], s69 offen offset:3072 sc1\n"
    "v_mfma_f32_16x16x32_bf16 a[76:79], a[224:227], v[192:195], a[76:79]\n"
    "ds_read_b64_tr_b16 a[184:185], v212 offset:20672\n"
    "v_mfma_f32_16x16x32_bf16 a[76:79], a[228:231], v[196:199], a[76:79]\n"
    "ds_read_b64_tr_b16 a[186:187], v212 offset:25792\n"
    "v_mfma_f32_16x16x32_bf16 a[76:79], a[232:235], v[200:203], a[76:79]\n"
    "ds_read_b64_tr_b16 a[188:189], v212 offset:30912\n"
    "v_mfma_f32_16x16x32_bf16 a[76:79], a[236:239], v[204:207], a[76:79]\n"
    "ds_read_b64_tr_b16 a[190:191], v212 offset:36032\n"
    "s_waitcnt lgkmcnt(8)\n"
    "v_mfma_f32_16x16x32_bf16 a[80:83], a[160:163], v[144:147], a[80:83]\n"
    "buffer_load_dwordx4 v[96:99], v226, s[36:39], s70 offen offset:0 sc1\n"
    "v_mfma_f32_16x16x32_bf16 a[80:83], a[164:167], v[148:151], a[80:83]\n"
    "v_mfma_f32_16x16x32_bf16 a[80:83], a[168:171], v[152:155], a[80:83]\n"
    "v_mfma_f32_16x16x32_bf16 a[80:83], a[172:175], v[156:159], a[80:83]\n"
    "v_mfma_f32_16x16x32_bf16 a[84:87], a[160:163], v[160:163], a[84:87]\n"
    "v_mfma_f32_16x16x32_bf16 a[84:87], a[164:167], v[164:167], a[84:87]\n"
    "v_mfma_f32_16x16x32_bf16 a[84:87], a[168:171], v[168:171], a[84:87]\n"
    "buffer_load_dwordx4 v[100:103], v226, s[36:39], s70 offen offset:1024 sc1\n"
    "v_mfma_f32_16x16x32_bf16 a[84:87], a[172:175], v[172:175], a[84:87]\n"
    "v_mfma_f32_16x16x32_bf16 a[88:91], a[160:163], v[176:179], a[88:91]\n"
    "ds_read_b64_tr_b16 a[192:193], v213 offset:224\n"
    "v_mfma_f32_16x16x32_bf16 a[88:91], a[164:167], v[180:183], a[88:91]\n"
    "ds_read_b64_tr_b16 a[194:195], v213 offset:5344\n"
    "v_mfma_f32_16x16x32_bf16 a[88:91], a[168:171], v[184:187], a[88:91]\n"
    "ds_read_b64_tr_b16 a[196:197], v213 offset:10464\n"
    "v_mfma_f32_16x16x32_bf16 a[88:91], a[172:175], v[188:191], a[88:91]\n"
    "ds_read_b64_tr_b16 a[198:199], v213 offset:15584\n"
    "v_mfma_f32_16x16x32_bf16 a[92:95], a[160:163], v[192:195], a[92:95]\n"
    "ds_read_b64_tr_b16 a[200:201], v213 offset:20704\n"
    "v_mfma_f32_16x16x32_bf16 a[92:95], a[164:167], v[196:199], a[92:95]\n"
    "ds_read_b64_tr_b16 a[202:203], v213 offset:25824\n"
    "v_mfma_f32_16x16x32_bf16 a[92:95], a[168:171], v[200:203], a[92:95]\n"
    "ds_read_b64_tr_b16 a[204:205], v213 offset:30944\n"
    "v_mfma_f32_16x16x32_bf16 a[92:95], a[172:175], v[204:207], a[92:95]\n"
    "ds_read_b64_tr_b16 a[206:207], v213 offset:36064\n"
    "s_waitcnt lgkmcnt(8)\n"
    "v_mfma_f32_16x16x32_bf16 a[96:99], a[176:179], v[144:147], a[96:99]\n"
    "buffer_load_dwordx4 v[104:107], v226, s[36:39], s70 offen offset:2048 sc1\n"
    "v_mfma_f32_16x16x32_bf16 a[96:99], a[180:183], v[148:151], a[96:99]\n"
    "v_mfma_f32_16x16x32_bf16 a[96:99], a[184:187], v[152:155], a[96:99]\n"
    "buffer_load_dwordx4 v[108:111], v226, s[36:39], s70 offen offset:3072 sc1\n"
    "v_mfma_f32_16x16x32_bf16 a[96:99], a[188:191], v[156:159], a[96:99]\n"
    "v_mfma_f32_16x16x32_bf16 a[100:103], a[176:179], v[160:163], a[100:103]\n"
    "v_mfma_f32_16x16x32_bf16 a[100:103], a[180:183], v[164:167], a[100:103]\n"
    "v_mfma_f32_16x16x32_bf16 a[100:103], a[184:187], v[168:171], a[100:103]\n"
    "v_mfma_f32_16x16x32_bf16 a[100:103], a[188:191], v[172:175], a[100:103]\n"
    "buffer_load_dwordx4 v[112:115], v226, s[36:39], s71 offen offset:0 sc1\n"
    "v_mfma_f32_16x16x32_bf16 a[104:107], a[176:179], v[176:179], a[104:107]\n"
    "ds_read_b64_tr_b16 a[208:209], v212 offset:256\n"
    "v_mfma_f32_16x16x32_bf16 a[104:107], a[180:183], v[180:183], a[104:107]\n"
    "ds_read_b64_tr_b16 a[210:211], v212 offset:5376\n"
    "v_mfma_f32_16x16x32_bf16 a[104:107], a[184:187], v[184:187], a[104:107]\n"
    "ds_read_b64_tr_b16 a[212:213], v212 offset:10496\n"
    "v_mfma_f32_16x16x32_bf16 a[104:107], a[188:191], v[188:191], a[104:107]\n"
    "ds_read_b64_tr_b16 a[214:215], v212 offset:15616\n"
    "v_mfma_f32_16x16x32_bf16 a[108:111], a[176:179], v[192:195], a[108:111]\n"
    "ds_read_b64_tr_b16 a[216:217], v212 offset:20736\n"
    "v_mfma_f32_16x16x32_bf16 a[108:111], a[180:183], v[196:199], a[108:111]\n"
    "ds_read_b64_tr_b16 a[218:219], v212 offset:25856\n"
    "v_mfma_f32_16x16x32_bf16 a[108:111], a[184:187], v[200:203], a[108:111]\n"
    "ds_read_b64_tr_b16 a[220:221], v212 offset:30976\n"
    "v_mfma_f32_16x16x32_bf16 a[108:111], a[188:191], v[204:207], a[108:111]\n"
    "ds_read_b64_tr_b16 a[222:223], v212 offset:36096\n"
    "s_waitcnt lgkmcnt(8)\n"
    "v_mfma_f32_16x16x32_bf16 a[112:115], a[192:195], v[144:147], a[112:115]\n"
    "buffer_load_dwordx4 v[116:119], v226, s[36:39], s71 offen offset:1024 sc1\n"
    "v_mfma_f32_16x16x32_bf16 a[112:115], a[196:199], v[148:151], a[112:115]\n"
    "v_mfma_f32_16x16x32_bf16 a[112:115], a[200:203], v[152:155], a[112:115]\n"
    "v_mfma_f32_16x16x32_bf16 a[112:115], a[204:207], v[156:159], a[112:115]\n"
    "buffer_load_dwordx4 v[120:123], v226, s[36:39], s71 offen offset:2048 sc1\n"
    "v_mfma_f32_16x16x32_bf16 a[116:119], a[192:195], v[160:163], a[116:119]\n"
    "v_mfma_f32_16x16x32_bf16 a[116:119], a[196:199], v[164:167], a[116:119]\n"
    "v_mfma_f32_16x16x32_bf16 a[116:119], a[200:203], v[168:171], a[116:119]\n"
    "v_mfma_f32_16x16x32_bf16 a[116:119], a[204:207], v[172:175], a[116:119]\n"
    "v_mfma_f32_16x16x32_bf16 a[120:123], a[192:195], v[176:179], a[120:123]\n"
    "ds_read_b64_tr_b16 a[224:225], v213 offset:288\n"
    "v_mfma_f32_16x16x32_bf16 a[120:123], a[196:199], v[180:183], a[120:123]\n"
    "ds_read_b64_tr_b16 a[226:227], v213 offset:5408\n"
    "buffer_load_dwordx4 v[124:127], v226, s[36:39], s71 offen offset:3072 sc1\n"
    "v_mfma_f32_16x16x32_bf16 a[120:123], a[200:203], v[184:187], a[120:123]\n"
    "ds_read_b64_tr_b16 a[228:229], v213 offset:10528\n"
    "v_mfma_f32_16x16x32_bf16 a[120:123], a[204:207], v[188:191], a[120:123]\n"
    "ds_read_b64_tr_b16 a[230:231], v213 offset:15648\n"
    "v_mfma_f32_16x16x32_bf16 a[124:127], a[192:195], v[192:195], a[124:127]\n"
    "ds_read_b64_tr_b16 a[232:233], v213 offset:20768\n"
    "v_mfma_f32_16x16x32_bf16 a[124:127], a[196:199], v[196:199], a[124:127]\n"
    "ds_read_b64_tr_b16 a[234:235], v213 offset:25888\n"
    "v_mfma_f32_16x16x32_bf16 a[124:127], a[200:203], v[200:203], a[124:127]\n"
    "ds_read_b64_tr_b16 a[236:237], v213 offset:31008\n"
    "v_mfma_f32_16x16x32_bf16 a[124:127], a[204:207], v[204:207], a[124:127]\n"
    "ds_read_b64_tr_b16 a[238:239], v213 offset:36128\n"
    "s_waitcnt lgkmcnt(8)\n"
    "v_mfma_f32_16x16x32_bf16 a[128:131], a[208:211], v[144:147], a[128:131]\n"
    "buffer_load_dwordx4 v[128:131], v226, s[36:39], s72 offen offset:0 sc1\n"
    "v_mfma_f32_16x16x32_bf16 a[128:131], a[212:215], v[148:151], a[128:131]\n"
    "v_mfma_f32_16x16x32_bf16 a[128:131], a[216:219], v[152:155], a[128:131]\n"
    "v_mfma_f32_16x16x32_bf16 a[128:131], a[220:223], v[156:159], a[128:131]\n"
    "v_mfma_f32_16x16x32_bf16 a[132:135], a[208:211], v[160:163], a[132:135]\n"
    "buffer_load_dwordx4 v[132:135], v226, s[36:39], s72 offen offset:1024 sc1\n"
    "v_mfma_f32_16x16x32_bf16 a[132:135], a[212:215], v[164:167], a[132:135]\n"
    "v_mfma_f32_16x16x32_bf16 a[132:135], a[216:219], v[168:171], a[132:135]\n"
    "v_mfma_f32_16x16x32_bf16 a[132:135], a[220:223], v[172:175], a[132:135]\n"
    "v_mfma_f32_16x16x32_bf16 a[136:139], a[208:211], v[176:179], a[136:139]\n"
    "v_mfma_f32_16x16x32_bf16 a[136:139], a[212:215], v[180:183], a[136:139]\n"
    "v_mfma_f32_16x16x32_bf16 a[136:139], a[216:219], v[184:187], a[136:139]\n"
    "buffer_load_dwordx4 v[136:139], v226, s[36:39], s72 offen offset:2048 sc1\n"
    "v_mfma_f32_16x16x32_bf16 a[136:139], a[220:223], v[188:191], a[136:139]\n"
    "v_mfma_f32_16x16x32_bf16 a[140:143], a[208:211], v[192:195], a[140:143]\n"
    "v_mfma_f32_16x16x32_bf16 a[140:143], a[212:215], v[196:199], a[140:143]\n"
    "v_mfma_f32_16x16x32_bf16 a[140:143], a[216:219], v[200:203], a[140:143]\n"
    "v_mfma_f32_16x16x32_bf16 a[140:143], a[220:223], v[204:207], a[140:143]\n"
    "s_waitcnt vmcnt(41)\n"
    "s_waitcnt lgkmcnt(0)\n"
    "s_barrier\n"
    "v_mfma_f32_16x16x32_bf16 a[144:147], a[224:227], v[144:147], a[144:147]\n"
    "ds_read_b64_tr_b16 a[160:161], v208 offset:0\n"
    "buffer_load_dwordx4 v[140:143], v226, s[36:39], s72 offen offset:3072 sc1\n"
    "v_mfma_f32_16x16x32_bf16 a[144:147], a[228:231], v[148:151], a[144:147]\n"
    "ds_read_b64_tr_b16 a[162:163], v208 offset:5120\n"
    "v_mfma_f32_16x16x32_bf16 a[144:147], a[232:235], v[152:155], a[144:147]\n"
    "ds_read_b64_tr_b16 a[164:165], v208 offset:10240\n"
    "v_mfma_f32_16x16x32_bf16 a[144:147], a[236:239], v[156:159], a[144:147]\n"
    "ds_read_b64_tr_b16 a[166:167], v208 offset:15360\n"
    "v_mfma_f32_16x16x32_bf16 a[148:151], a[224:227], v[160:163], a[148:151]\n"
    "ds_read_b64_tr_b16 a[168:169], v208 offset:20480\n"
    "v_mfma_f32_16x16x32_bf16 a[148:151], a[228:231], v[164:167], a[148:151]\n"
    "ds_read_b64_tr_b16 a[170:171], v208 offset:25600\n"
    "v_mfma_f32_16x16x32_bf16 a[148:151], a[232:235], v[168:171], a[148:151]\n"
    "ds_read_b64_tr_b16 a[172:173], v208 offset:30720\n"
    "v_mfma_f32_16x16x32_bf16 a[148:151], a[236:239], v[172:175], a[148:151]\n"
    "ds_read_b64_tr_b16 a[174:175], v208 offset:35840\n"
    "v_mfma_f32_16x16x32_bf16 a[152:155], a[224:227], v[176:179], a[152:155]\n"
    "s_add_u32 s69, s69, 4096\n"
    "s_add_u32 s70, s70, 4096\n"
    "s_add_u32 s71, s71, 4096\n"
    "s_add_u32 s72, s72, 4096\n"
    "v_mfma_f32_16x16x32_bf16 a[152:155], a[228:231], v[180:183], a[152:155]\n"
    "v_mfma_f32_16x16x32_bf16 a[152:155], a[232:235], v[184:187], a[152:155]\n"
    "v_mfma_f32_16x16x32_bf16 a[152:155], a[236:239], v[188:191], a[152:155]\n"
    "v_mfma_f32_16x16x32_bf16 a[156:159], a[224:227], v[192:195], a[156:159]\n"
    "s_sub_u32 s74, s74, 1\n"
    "s_cmp_eq_u32 s74, 0\n"
    "s_cselect_b32 s69, s75, s69\n"
    "s_cselect_b32 s70, s75, s70\n"
    "s_cselect_b32 s71, s75, s71\n"
    "s_cselect_b32 s72, s75, s72\n"
    "v_mfma_f32_16x16x32_bf16 a[156:159], a[228:231], v[196:199], a[156:159]\n"
    "v_mfma_f32_16x16x32_bf16 a[156:159], a[232:235], v[200:203], a[156:159]\n"
    "v_mfma_f32_16x16x32_bf16 a[156:159], a[236:239], v[204:207], a[156:159]\n"
    "s_cmp_ge_u32 s65, 6\n"
    "s_cbranch_scc1 L_ffn\n"
    "s_waitcnt vmcnt(16)\n"
    "s_barrier\n"
    "s_waitcnt lgkmcnt(0)\n"
    "ds_read_b64_tr_b16 a[176:177], v210 offset:0\n"
    "v_mfma_f32_16x16x32_bf16 a[0:3], a[160:163], v[16:19], a[0:3]\n"
    "ds_read_b64_tr_b16 a[178:179], v210 offset:5120\n"
    "s_mov_b32 s86, s22\n"
    "s_mov_b32 s87, s23\n"
    "v_mfma_f32_16x16x32_bf16 a[0:3], a[164:167], v[20:23], a[0:3]\n"
    "ds_read_b64_tr_b16 a[180:181], v210 offset:10240\n"
    "v_mfma_f32_16x16x32_bf16 a[0:3], a[168:171], v[24:27], a[0:3]\n"
    "ds_read_b64_tr_b16 a[182:183], v210 offset:15360\n"
    "v_mfma_f32_16x16x32_bf16 a[0:3], a[172:175], v[28:31], a[0:3]\n"
    "ds_read_b64_tr_b16 a[184:185], v210 offset:20480\n"
    "v_mfma_f32_16x16x32_bf16 a[4:7], a[160:163], v[32:35], a[4:7]\n"
    "ds_read_b64_tr_b16 a[186:187], v210 offset:25600\n"
    "v_mfma_f32_16x16x32_bf16 a[4:7], a[164:167], v[36:39], a[4:7]\n"
    "ds_read_b64_tr_b16 a[188:189], v210 offset:30720\n"
    "v_mfma_f32_16x16x32_bf16 a[4:7], a[168:171], v[40:43], a[4:7]\n"
    "ds_read_b64_tr_b16 a[190:191], v210 offset:35840\n"
    "v_mfma_f32_16x16x32_bf16 a[4:7], a[172:175], v[44:47], a[4:7]\n"
    "ds_read_b64_tr_b16 a[192:193], v209 offset:32\n"
    "v_mfma_f32_16x16x32_bf16 a[8:11], a[160:163], v[48:51], a[8:11]\n"
    "ds_read_b64_tr_b16 a[194:195], v209 offset:5152\n"
    "v_mfma_f32_16x16x32_bf16 a[8:11], a[164:167], v[52:55], a[8:11]\n"
    "ds_read_b64_tr_b16 a[196:197], v209 offset:10272\n"
    "v_mfma_f32_16x16x32_bf16 a[8:11], a[168:171], v[56:59], a[8:11]\n"
    "ds_read_b64_tr_b16 a[198:199], v209 offset:15392\n"
    "v_mfma_f32_16x16x32_bf16 a[8:11], a[172:175], v[60:63], a[8:11]\n"
    "ds_read_b64_tr_b16 a[200:201], v209 offset:20512\n"
    "v_mfma_f32_16x16x32_bf16 a[12:15], a[160:163], v[64:67], a[12:15]\n"
    "ds_read_b64_tr_b16 a[208:209], v211 offset:32\n"
    "v_mfma_f32_16x16x32_bf16 a[12:15], a[164:167], v[68:71], a[12:15]\n"
    "ds_read_b64_tr_b16 a[202:203], v209 offset:25632\n"
    "v_mfma_f32_16x16x32_bf16 a[12:15], a[168:171], v[72:75], a[12:15]\n"
    "ds_read_b64_tr_b16 a[210:211], v211 offset:5152\n"
    "v_mfma_f32_16x16x32_bf16 a[12:15], a[172:175], v[76:79], a[12:15]\n"
    "ds_read_b64_tr_b16 a[204:205], v209 offset:30752\n"
    "s_waitcnt lgkmcnt(9)\n"
    "s_waitcnt vmcnt(0)\n"
    "v_mfma_f32_16x16x32_bf16 a[0:3], a[176:179], v[80:83], a[0:3]\n"
    "ds_read_b64_tr_b16 a[212:213], v211 offset:10272\n"
    "v_mfma_f32_16x16x32_bf16 a[0:3], a[180:183], v[84:87], a[0:3]\n"
    "ds_read_b64_tr_b16 a[206:207], v209 offset:35872\n"
    "v_mfma_f32_16x16x32_bf16 a[0:3], a[184:187], v[88:91], a[0:3]\n"
    "ds_read_b64_tr_b16 a[214:215], v211 offset:15392\n"
    "v_mfma_f32_16x16x32_bf16 a[0:3], a[188:191], v[92:95], a[0:3]\n"
    "ds_read_b64_tr_b16 a[216:217], v211 offset:20512\n"
    "v_mfma_f32_16x16x32_bf16 a[4:7], a[176:179], v[96:99], a[4:7]\n"
    "ds_read_b64_tr_b16 a[218:219], v211 offset:25632\n"
    "v_mfma_f32_16x16x32_bf16 a[4:7], a[180:183], v[100:103], a[4:7]\n"
    "ds_read_b64_tr_b16 a[220:221], v211 offset:30752\n"
    "v_mfma_f32_16x16x32_bf16 a[4:7], a[184:187], v[104:107], a[4:7]\n"
    "ds_read_b64_tr_b16 a[222:223], v211 offset:35872\n"
    "v_mfma_f32_16x16x32_bf16 a[4:7], a[188:191], v[108:111], a[4:7]\n"
    "v_mfma_f32_16x16x32_bf16 a[8:11], a[176:179], v[112:115], a[8:11]\n"
    "v_mfma_f32_16x16x32_bf16 a[8:11], a[180:183], v[116:119], a[8:11]\n"
    "v_mfma_f32_16x16x32_bf16 a[8:11], a[184:187], v[120:123], a[8:11]\n"
    "v_mfma_f32_16x16x32_bf16 a[8:11], a[188:191], v[124:127], a[8:11]\n"
    "v_mfma_f32_16x16x32_bf16 a[12:15], a[176:179], v[128:131], a[12:15]\n"
    "ds_read_b64_tr_b16 a[224:225], v208 offset:64\n"
    "v_mfma_f32_16x16x32_bf16 a[12:15], a[180:183], v[132:135], a[12:15]\n"
    "ds_read_b64_tr_b16 a[226:227], v208 offset:5184\n"
    "v_mfma_f32_16x16x32_bf16 a[12:15], a[184:187], v[136:139], a[12:15]\n"
    "ds_read_b64_tr_b16 a[228:229], v208 offset:10304\n"
    "v_mfma_f32_16x16x32_bf16 a[12:15], a[188:191], v[140:143], a[12:15]\n"
    "ds_read_b64_tr_b16 a[230:231], v208 offset:15424\n"
    "s_waitcnt lgkmcnt(9)\n"
    "v_mfma_f32_16x16x32_bf16 a[16:19], a[192:195], v[16:19], a[16:19]\n"
    "ds_read_b64_tr_b16 a[232:233], v208 offset:20544\n"
    "v_mfma_f32_16x16x32_bf16 a[16:19], a[196:199], v[20:23], a[16:19]\n"
    "ds_read_b64_tr_b16 a[234:235], v208 offset:25664\n"
    "v_mfma_f32_16x16x32_bf16 a[16:19], a[200:203], v[24:27], a[16:19]\n"
    "ds_read_b64_tr_b16 a[240:241], v210 offset:64\n"
    "v_mfma_f32_16x16x32_bf16 a[16:19], a[204:207], v[28:31], a[16:19]\n"
    "ds_read_b64_tr_b16 a[242:243], v210 offset:5184\n"
    "s_waitcnt lgkmcnt(8)\n"
    "v_mfma_f32_16x16x32_bf16 a[16:19], a[208:211], v[80:83], a[16:19]\n"
    "ds_read_b64_tr_b16 a[236:237], v208 offset:30784\n"
    "v_accvgpr_read_b32 v144, a0\n"
    "v_mfma_f32_16x16x32_bf16 a[16:19], a[212:215], v[84:87], a[16:19]\n"
    "ds_read_b64_tr_b16 a[244:245], v210 offset:10304\n"
    "v_accvgpr_read_b32 v145, a1\n"
    "v_mfma_f32_16x16x32_bf16 a[16:19], a[216:219], v[88:91], a[16:19]\n"
    "ds_read_b64_tr_b16 a[238:239], v208 offset:35904\n"
    "v_accvgpr_read_b32 v146, a2\n"
    "v_mfma_f32_16x16x32_bf16 a[16:19], a[220:223], v[92:95], a[16:19]\n"
    "ds_read_b64_tr_b16 a[246:247], v210 offset:15424\n"
    "v_accvgpr_read_b32 v147, a3\n"
    "v_mfma_f32_16x16x32_bf16 a[20:23], a[192:195], v[32:35], a[20:23]\n"
    "ds_read_b64_tr_b16 a[248:249], v210 offset:20544\n"
    "v_accvgpr_read_b32 v148, a4\n"
    "v_mfma_f32_16x16x32_bf16 a[20:23], a[196:199], v[36:39], a[20:23]\n"
    "ds_read_b64_tr_b16 a[250:251], v210 offset:25664\n"
    "v_accvgpr_read_b32 v149, a5\n"
    "v_mfma_f32_16x16x32_bf16 a[20:23], a[200:203], v[40:43], a[20:23]\n"
    "ds_read_b64_tr_b16 a[252:253], v210 offset:30784\n"
    "v_accvgpr_read_b32 v150, a6\n"
    "v_mfma_f32_16x16x32_bf16 a[20:23], a[204:207], v[44:47], a[20:23]\n"
    "ds_read_b64_tr_b16 a[254:255], v210 offset:35904\n"
    "v_accvgpr_read_b32 v151, a7\n"
    "v_mfma_f32_16x16x32_bf16 a[20:23], a[208:211], v[96:99], a[20:23]\n"
    "v_cvt_pk_bf16_f32 v152, v144, v145\n"
    "v_cvt_pk_bf16_f32 v153, v146, v147\n"
    "v_mfma_f32_16x16x32_bf16 a[20:23], a[212:215], v[100:103], a[20:23]\n"
    "v_cvt_pk_bf16_f32 v154, v148, v149\n"
    "v_cvt_pk_bf16_f32 v155, v150, v151\n"
    "v_mfma_f32_16x16x32_bf16 a[20:23], a[216:219], v[104:107], a[20:23]\n"
    "buffer_store_dwordx4 v[152:155], v226, s[44:47], s86 offen offset:0 sc1\n"
    "v_mfma_f32_16x16x32_bf16 a[20:23], a[220:223], v[108:111], a[20:23]\n"
    "ds_write_b128 v14, v[152:155] offset:0\n"
    "v_accvgpr_read_b32 v160, a8\n"
    "v_mfma_f32_16x16x32_bf16 a[24:27], a[192:195], v[48:51], a[24:27]\n"
    "v_accvgpr_read_b32 v161, a9\n"
    "v_accvgpr_read_b32 v162, a10\n"
    "v_mfma_f32_16x16x32_bf16 a[24:27], a[196:199], v[52:55], a[24:27]\n"
    "v_accvgpr_read_b32 v163, a11\n"
    "v_accvgpr_read_b32 v164, a12\n"
    "v_mfma_f32_16x16x32_bf16 a[24:27], a[200:203], v[56:59], a[24:27]\n"
    "v_accvgpr_read_b32 v165, a13\n"
    "v_accvgpr_read_b32 v166, a14\n"
    "v_mfma_f32_16x16x32_bf16 a[24:27], a[204:207], v[60:63], a[24:27]\n"
    "v_accvgpr_read_b32 v167, a15\n"
    "v_cvt_pk_bf16_f32 v168, v160, v161\n"
    "v_mfma_f32_16x16x32_bf16 a[24:27], a[208:211], v[112:115], a[24:27]\n"
    "v_cvt_pk_bf16_f32 v169, v162, v163\n"
    "v_cvt_pk_bf16_f32 v170, v164, v165\n"
    "v_mfma_f32_16x16x32_bf16 a[24:27], a[212:215], v[116:119], a[24:27]\n"
    "v_cvt_pk_bf16_f32 v171, v166, v167\n"
    "v_mfma_f32_16x16x32_bf16 a[24:27], a[216:219], v[120:123], a[24:27]\n"
    "buffer_store_dwordx4 v[168:171], v226, s[44:47], s87 offen offset:0 sc1\n"
    "v_mfma_f32_16x16x32_bf16 a[24:27], a[220:223], v[124:127], a[24:27]\n"
    "ds_write_b128 v15, v[168:171] offset:0\n"
    "v_mfma_f32_16x16x32_bf16 a[28:31], a[192:195], v[64:67], a[28:31]\n"
    "v_mfma_f32_16x16x32_bf16 a[28:31], a[196:199], v[68:71], a[28:31]\n"
    "v_mfma_f32_16x16x32_bf16 a[28:31], a[200:203], v[72:75], a[28:31]\n"
    "v_mfma_f32_16x16x32_bf16 a[28:31], a[204:207], v[76:79], a[28:31]\n"
    "v_mfma_f32_16x16x32_bf16 a[28:31], a[208:211], v[128:131], a[28:31]\n"
    "v_mfma_f32_16x16x32_bf16 a[28:31], a[212:215], v[132:135], a[28:31]\n"
    "v_mfma_f32_16x16x32_bf16 a[28:31], a[216:219], v[136:139], a[28:31]\n"
    "v_mfma_f32_16x16x32_bf16 a[28:31], a[220:223], v[140:143], a[28:31]\n"
    "ds_read_b64_tr_b16 a[160:161], v209 offset:96\n"
    "s_waitcnt lgkmcnt(8)\n"
    "v_mfma_f32_16x16x32_bf16 a[32:35], a[224:227], v[16:19], a[32:35]\n"
    "ds_read_b64_tr_b16 a[162:163], v209 offset:5216\n"
    "v_mfma_f32_16x16x32_bf16 a[32:35], a[228:231], v[20:23], a[32:35]\n"
    "ds_read_b64_tr_b16 a[164:165], v209 offset:10336\n"
    "v_mfma_f32_16x16x32_bf16 a[32:35], a[232:235], v[24:27], a[32:35]\n"
    "ds_read_b64_tr_b16 a[166:167], v209 offset:15456\n"
    "v_mfma_f32_16x16x32_bf16 a[32:35], a[236:239], v[28:31], a[32:35]\n"
    "ds_read_b64_tr_b16 a[168:169], v209 offset:20576\n"
    "s_waitcnt lgkmcnt(7)\n"
    "v_mfma_f32_16x16x32_bf16 a[32:35], a[240:243], v[80:83], a[32:35]\n"
    "ds_read_b64_tr_b16 a[170:171], v209 offset:25696\n"
    "v_accvgpr_read_b32 v176, a16\n"
    "v_mfma_f32_16x16x32_bf16 a[32:35], a[244:247], v[84:87], a[32:35]\n"
    "ds_read_b64_tr_b16 a[176:177], v211 offset:96\n"
    "v_accvgpr_read_b32 v177, a17\n"
    "v_mfma_f32_16x16x32_bf16 a[32:35], a[248:251], v[88:91], a[32:35]\n"
    "ds_read_b64_tr_b16 a[178:179], v211 offset:5216\n"
    "v_accvgpr_read_b32 v178, a18\n"
    "v_mfma_f32_16x16x32_bf16 a[32:35], a[252:255], v[92:95], a[32:35]\n"
    "ds_read_b64_tr_b16 a[172:173], v209 offset:30816\n"
    "v_accvgpr_read_b32 v179, a19\n"
    "v_mfma_f32_16x16x32_bf16 a[36:39], a[224:227], v[32:35], a[36:39]\n"
    "ds_read_b64_tr_b16 a[180:181], v211 offset:10336\n"
    "v_accvgpr_read_b32 v180, a20\n"
    "v_mfma_f32_16x16x32_bf16 a[36:39], a[228:231], v[36:39], a[36:39]\n"
    "ds_read_b64_tr_b16 a[174:175], v209 offset:35936\n"
    "v_accvgpr_read_b32 v181, a21\n"
    "v_mfma_f32_16x16x32_bf16 a[36:39], a[232:235], v[40:43], a[36:39]\n"
    "ds_read_b64_tr_b16 a[182:183], v211 offset:15456\n"
    "v_accvgpr_read_b32 v182, a22\n"
    "v_mfma_f32_16x16x32_bf16 a[36:39], a[236:239], v[44:47], a[36:39]\n"
    "ds_read_b64_tr_b16 a[184:185], v211 offset:20576\n"
    "v_accvgpr_read_b32 v183, a23\n"
    "v_mfma_f32_16x16x32_bf16 a[36:39], a[240:243], v[96:99], a[36:39]\n"
    "ds_read_b64_tr_b16 a[186:187], v211 offset:25696\n"
    "v_cvt_pk_bf16_f32 v184, v176, v177\n"
    "v_mfma_f32_16x16x32_bf16 a[36:39], a[244:247], v[100:103], a[36:39]\n"
    "ds_read_b64_tr_b16 a[188:189], v211 offset:30816\n"
    "v_cvt_pk_bf16_f32 v185, v178, v179\n"
    "v_mfma_f32_16x16x32_bf16 a[36:39], a[248:251], v[104:107], a[36:39]\n"
    "ds_read_b64_tr_b16 a[190:191], v211 offset:35936\n"
    "v_cvt_pk_bf16_f32 v186, v180, v181\n"
    "v_mfma_f32_16x16x32_bf16 a[36:39], a[252:255], v[108:111], a[36:39]\n"
    "v_cvt_pk_bf16_f32 v187, v182, v183\n"
    "v_mfma_f32_16x16x32_bf16 a[40:43], a[224:227], v[48:51], a[40:43]\n"
    "buffer_store_dwordx4 v[184:187], v226, s[44:47], s86 offen offset:1024 sc1\n"
    "v_mfma_f32_16x16x32_bf16 a[40:43], a[228:231], v[52:55], a[40:43]\n"
    "ds_write_b128 v14, v[184:187] offset:1024\n"
    "v_accvgpr_read_b32 v144, a24\n"
    "v_mfma_f32_16x16x32_bf16 a[40:43], a[232:235], v[56:59], a[40:43]\n"
    "v_accvgpr_read_b32 v145, a25\n"
    "v_accvgpr_read_b32 v146, a26\n"
    "v_mfma_f32_16x16x32_bf16 a[40:43], a[236:239], v[60:63], a[40:43]\n"
    "v_accvgpr_read_b32 v147, a27\n"
    "v_accvgpr_read_b32 v148, a28\n"
    "v_mfma_f32_16x16x32_bf16 a[40:43], a[240:243], v[112:115], a[40:43]\n"
    "v_accvgpr_read_b32 v149, a29\n"
    "v_accvgpr_read_b32 v150, a30\n"
    "v_mfma_f32_16x16x32_bf16 a[40:43], a[244:247], v[116:119], a[40:43]\n"
    "v_accvgpr_read_b32 v151, a31\n"
    "v_cvt_pk_bf16_f32 v152, v144, v145\n"
    "v_mfma_f32_16x16x32_bf16 a[40:43], a[248:251], v[120:123], a[40:43]\n"
    "v_cvt_pk_bf16_f32 v153, v146, v147\n"
    "v_cvt_pk_bf16_f32 v154, v148, v149\n"
    "v_mfma_f32_16x16x32_bf16 a[40:43], a[252:255], v[124:127], a[40:43]\n"
    "v_cvt_pk_bf16_f32 v155, v150, v151\n"
    "v_mfma_f32_16x16x32_bf16 a[44:47], a[224:227], v[64:67], a[44:47]\n"
    "buffer_store_dwordx4 v[152:155], v226, s[44:47], s87 offen offset:1024 sc1\n"
    "v_mfma_f32_16x16x32_bf16 a[44:47], a[228:231], v[68:71], a[44:47]\n"
    "ds_write_b128 v15, v[152:155] offset:1024\n"
    "v_mfma_f32_16x16x32_bf16 a[44:47], a[232:235], v[72:75], a[44:47]\n"
    "v_mfma_f32_16x16x32_bf16 a[44:47], a[236:239], v[76:79], a[44:47]\n"
    "v_mfma_f32_16x16x32_bf16 a[44:47], a[240:243], v[128:131], a[44:47]\n"
    "v_mfma_f32_16x16x32_bf16 a[44:47], a[244:247], v[132:135], a[44:47]\n"
    "v_mfma_f32_16x16x32_bf16 a[44:47], a[248:251], v[136:139], a[44:47]\n"
    "v_mfma_f32_16x16x32_bf16 a[44:47], a[252:255], v[140:143], a[44:47]\n"
    "ds_read_b64_tr_b16 a[192:193], v208 offset:128\n"
    "s_waitcnt lgkmcnt(8)\n"
    "v_mfma_f32_16x16x32_bf16 a[48:51], a[160:163], v[16:19], a[48:51]\n"
    "ds_read_b64_tr_b16 a[194:195], v208 offset:5248\n"
    "v_mfma_f32_16x16x32_bf16 a[48:51], a[164:167], v[20:23], a[48:51]\n"
    "ds_read_b64_tr_b16 a[196:197], v208 offset:10368\n"
    "v_mfma_f32_16x16x32_bf16 a[48:51], a[168:171], v[24:27], a[48:51]\n"
    "ds_read_b64_tr_b16 a[198:199], v208 offset:15488\n"
    "v_mfma_f32_16x16x32_bf16 a[48:51], a[172:175], v[28:31], a[48:51]\n"
    "ds_read_b64_tr_b16 a[200:201], v208 offset:20608\n"
    "s_waitcnt lgkmcnt(7)\n"
    "v_mfma_f32_16x16x32_bf16 a[48:51], a[176:179], v[80:83], a[48:51]\n"
    "ds_read_b64_tr_b16 a[202:203], v208 offset:25728\n"
    "v_accvgpr_read_b32 v160, a32\n"
    "v_mfma_f32_16x16x32_bf16 a[48:51], a[180:183], v[84:87], a[48:51]\n"
    "ds_read_b64_tr_b16 a[208:209], v210 offset:128\n"
    "v_accvgpr_read_b32 v161, a33\n"
    "v_mfma_f32_16x16x32_bf16 a[48:51], a[184:187], v[88:91], a[48:51]\n"
    "ds_read_b64_tr_b16 a[210:211], v210 offset:5248\n"
    "v_accvgpr_read_b32 v162, a34\n"
    "v_mfma_f32_16x16x32_bf16 a[48:51], a[188:191], v[92:95], a[48:51]\n"
    "ds_read_b64_tr_b16 a[204:205], v208 offset:30848\n"
    "v_accvgpr_read_b32 v163, a35\n"
    "v_mfma_f32_16x16x32_bf16 a[52:55], a[160:163], v[32:35], a[52:55]\n"
    "ds_read_b64_tr_b16 a[212:213], v210 offset:10368\n"
    "v_accvgpr_read_b32 v164, a36\n"
    "v_mfma_f32_16x16x32_bf16 a[52:55], a[164:167], v[36:39], a[52:55]\n"
    "ds_read_b64_tr_b16 a[206:207], v208 offset:35968\n"
    "v_accvgpr_read_b32 v165, a37\n"
    "v_mfma_f32_16x16x32_bf16 a[52:55], a[168:171], v[40:43], a[52:55]\n"
    "ds_read_b64_tr_b16 a[214:215], v210 offset:15488\n"
    "v_accvgpr_read_b32 v166, a38\n"
    "v_mfma_f32_16x16x32_bf16 a[52:55], a[172:175], v[44:47], a[52:55]\n"
    "ds_read_b64_tr_b16 a[216:217], v210 offset:20608\n"
    "v_accvgpr_read_b32 v167, a39\n"
    "v_mfma_f32_16x16x32_bf16 a[52:55], a[176:179], v[96:99], a[52:55]\n"
    "ds_read_b64_tr_b16 a[218:219], v210 offset:25728\n"
    "v_cvt_pk_bf16_f32 v168, v160, v161\n"
    "v_mfma_f32_16x16x32_bf16 a[52:55], a[180:183], v[100:103], a[52:55]\n"
    "ds_read_b64_tr_b16 a[220:221], v210 offset:30848\n"
    "v_cvt_pk_bf16_f32 v169, v162, v163\n"
    "v_mfma_f32_16x16x32_bf16 a[52:55], a[184:187], v[104:107], a[52:55]\n"
    "ds_read_b64_tr_b16 a[222:223], v210 offset:35968\n"
    "v_cvt_pk_bf16_f32 v170, v164, v165\n"
    "v_mfma_f32_16x16x32_bf16 a[52:55], a[188:191], v[108:111], a[52:55]\n"
    "v_cvt_pk_bf16_f32 v171, v166, v167\n"
    "v_mfma_f32_16x16x32_bf16 a[56:59], a[160:163], v[48:51], a[56:59]\n"
    "buffer_store_dwordx4 v[168:171], v226, s[44:47], s86 offen offset:2048 sc1\n"
    "v_mfma_f32_16x16x32_bf16 a[56:59], a[164:167], v[52:55], a[56:59]\n"
    "ds_write_b128 v14, v[168:171] offset:2048\n"
    "v_accvgpr_read_b32 v176, a40\n"
    "v_mfma_f32_16x16x32_bf16 a[56:59], a[168:171], v[56:59], a[56:59]\n"
    "v_accvgpr_read_b32 v177, a41\n"
    "v_accvgpr_read_b32 v178, a42\n"
    "v_mfma_f32_16x16x32_bf16 a[56:59], a[172:175], v[60:63], a[56:59]\n"
    "v_accvgpr_read_b32 v179, a43\n"
    "v_accvgpr_read_b32 v180, a44\n"
    "v_mfma_f32_16x16x32_bf16 a[56:59], a[176:179], v[112:115], a[56:59]\n"
    "v_accvgpr_read_b32 v181, a45\n"
    "v_accvgpr_read_b32 v182, a46\n"
    "v_mfma_f32_16x16x32_bf16 a[56:59], a[180:183], v[116:119], a[56:59]\n"
    "v_accvgpr_read_b32 v183, a47\n"
    "v_cvt_pk_bf16_f32 v184, v176, v177\n"
    "v_mfma_f32_16x16x32_bf16 a[56:59], a[184:187], v[120:123], a[56:59]\n"
    "v_cvt_pk_bf16_f32 v185, v178, v179\n"
    "v_cvt_pk_bf16_f32 v186, v180, v181\n"
    "v_mfma_f32_16x16x32_bf16 a[56:59], a[188:191], v[124:127], a[56:59]\n"
    "v_cvt_pk_bf16_f32 v187, v182, v183\n"
    "v_mfma_f32_16x16x32_bf16 a[60:63], a[160:163], v[64:67], a[60:63]\n"
    "buffer_store_dwordx4 v[184:187], v226, s[44:47], s87 offen offset:2048 sc1\n"
    "v_mfma_f32_16x16x32_bf16 a[60:63], a[164:167], v[68:71], a[60:63]\n"
    "ds_write_b128 v15, v[184:187] offset:2048\n"
    "v_mfma_f32_16x16x32_bf16 a[60:63], a[168:171], v[72:75], a[60:63]\n"
    "v_mfma_f32_16x16x32_bf16 a[60:63], a[172:175], v[76:79], a[60:63]\n"
    "v_mfma_f32_16x16x32_bf16 a[60:63], a[176:179], v[128:131], a[60:63]\n"
    "v_mfma_f32_16x16x32_bf16 a[60:63], a[180:183], v[132:135], a[60:63]\n"
    "v_mfma_f32_16x16x32_bf16 a[60:63], a[184:187], v[136:139], a[60:63]\n"
    "v_mfma_f32_16x16x32_bf16 a[60:63], a[188:191], v[140:143], a[60:63]\n"
    "ds_read_b64_tr_b16 a[224:225], v209 offset:160\n"
    "s_waitcnt lgkmcnt(8)\n"
    "v_mfma_f32_16x16x32_bf16 a[64:67], a[192:195], v[16:19], a[64:67]\n"
    "ds_read_b64_tr_b16 a[226:227], v209 offset:5280\n"
    "v_mfma_f32_16x16x32_bf16 a[64:67], a[196:199], v[20:23], a[64:67]\n"
    "ds_read_b64_tr_b16 a[228:229], v209 offset:10400\n"
    "v_mfma_f32_16x16x32_bf16 a[64:67], a[200:203], v[24:27], a[64:67]\n"
    "ds_read_b64_tr_b16 a[230:231], v209 offset:15520\n"
    "v_mfma_f32_16x16x32_bf16 a[64:67], a[204:207], v[28:31], a[64:67]\n"
    "ds_read_b64_tr_b16 a[232:233], v209 offset:20640\n"
    "s_waitcnt lgkmcnt(7)\n"
    "v_mfma_f32_16x16x32_bf16 a[64:67], a[208:211], v[80:83], a[64:67]\n"
    "ds_read_b64_tr_b16 a[234:235], v209 offset:25760\n"
    "v_accvgpr_read_b32 v144, a48\n"
    "v_mfma_f32_16x16x32_bf16 a[64:67], a[212:215], v[84:87], a[64:67]\n"
    "ds_read_b64_tr_b16 a[240:241], v211 offset:160\n"
    "v_accvgpr_read_b32 v145, a49\n"
    "v_mfma_f32_16x16x32_bf16 a[64:67], a[216:219], v[88:91], a[64:67]\n"
    "ds_read_b64_tr_b16 a[242:243], v211 offset:5280\n"
    "v_accvgpr_read_b32 v146, a50\n"
    "v_mfma_f32_16x16x32_bf16 a[64:67], a[220:223], v[92:95], a[64:67]\n"
    "ds_read_b64_tr_b16 a[236:237], v209 offset:30880\n"
    "v_accvgpr_read_b32 v147, a51\n"
    "v_mfma_f32_16x16x32_bf16 a[68:71], a[192:195], v[32:35], a[68:71]\n"
    "ds_read_b64_tr_b16 a[244:245], v211 offset:10400\n"
    "v_accvgpr_read_b32 v148, a52\n"
    "v_mfma_f32_16x16x32_bf16 a[68:71], a[196:199], v[36:39], a[68:71]\n"
    "ds_read_b64_tr_b16 a[238:239], v209 offset:36000\n"
    "v_accvgpr_read_b32 v149, a53\n"
    "v_mfma_f32_16x16x32_bf16 a[68:71], a[200:203], v[40:43], a[68:71]\n"
    "ds_read_b64_tr_b16 a[246:247], v211 offset:15520\n"
    "v_accvgpr_read_b32 v150, a54\n"
    "v_mfma_f32_16x16x32_bf16 a[68:71], a[204:207], v[44:47], a[68:71]\n"
    "ds_read_b64_tr_b16 a[248:249], v211 offset:20640\n"
    "v_accvgpr_read_b32 v151, a55\n"
    "v_mfma_f32_16x16x32_bf16 a[68:71], a[208:211], v[96:99], a[68:71]\n"
    "ds_read_b64_tr_b16 a[250:251], v211 offset:25760\n"
    "v_cvt_pk_bf16_f32 v152, v144, v145\n"
    "v_mfma_f32_16x16x32_bf16 a[68:71], a[212:215], v[100:103], a[68:71]\n"
    "ds_read_b64_tr_b16 a[252:253], v211 offset:30880\n"
    "v_cvt_pk_bf16_f32 v153, v146, v147\n"
    "v_mfma_f32_16x16x32_bf16 a[68:71], a[216:219], v[104:107], a[68:71]\n"
    "ds_read_b64_tr_b16 a[254:255], v211 offset:36000\n"
    "v_cvt_pk_bf16_f32 v154, v148, v149\n"
    "v_mfma_f32_16x16x32_bf16 a[68:71], a[220:223], v[108:111], a[68:71]\n"
    "v_cvt_pk_bf16_f32 v155, v150, v151\n"
    "v_mfma_f32_16x16x32_bf16 a[72:75], a[192:195], v[48:51], a[72:75]\n"
    "buffer_store_dwordx4 v[152:155], v226, s[44:47], s86 offen offset:3072 sc1\n"
    "v_mfma_f32_16x16x32_bf16 a[72:75], a[196:199], v[52:55], a[72:75]\n"
    "ds_write_b128 v14, v[152:155] offset:3072\n"
    "v_accvgpr_read_b32 v160, a56\n"
    "v_mfma_f32_16x16x32_bf16 a[72:75], a[200:203], v[56:59], a[72:75]\n"
    "v_accvgpr_read_b32 v161, a57\n"
    "v_accvgpr_read_b32 v162, a58\n"
    "v_mfma_f32_16x16x32_bf16 a[72:75], a[204:207], v[60:63], a[72:75]\n"
    "v_accvgpr_read_b32 v163, a59\n"
    "v_accvgpr_read_b32 v164, a60\n"
    "v_mfma_f32_16x16x32_bf16 a[72:75], a[208:211], v[112:115], a[72:75]\n"
    "v_accvgpr_read_b32 v165, a61\n"
    "v_accvgpr_read_b32 v166, a62\n"
    "v_mfma_f32_16x16x32_bf16 a[72:75], a[212:215], v[116:119], a[72:75]\n"
    "v_accvgpr_read_b32 v167, a63\n"
    "v_cvt_pk_bf16_f32 v168, v160, v161\n"
    "v_mfma_f32_16x16x32_bf16 a[72:75], a[216:219], v[120:123], a[72:75]\n"
    "v_cvt_pk_bf16_f32 v169, v162, v163\n"
    "v_cvt_pk_bf16_f32 v170, v164, v165\n"
    "v_mfma_f32_16x16x32_bf16 a[72:75], a[220:223], v[124:127], a[72:75]\n"
    "v_cvt_pk_bf16_f32 v171, v166, v167\n"
    "v_mfma_f32_16x16x32_bf16 a[76:79], a[192:195], v[64:67], a[76:79]\n"
    "buffer_store_dwordx4 v[168:171], v226, s[44:47], s87 offen offset:3072 sc1\n"
    "v_mfma_f32_16x16x32_bf16 a[76:79], a[196:199], v[68:71], a[76:79]\n"
    "ds_write_b128 v15, v[168:171] offset:3072\n"
    "v_mfma_f32_16x16x32_bf16 a[76:79], a[200:203], v[72:75], a[76:79]\n"
    "v_mfma_f32_16x16x32_bf16 a[76:79], a[204:207], v[76:79], a[76:79]\n"
    "v_mfma_f32_16x16x32_bf16 a[76:79], a[208:211], v[128:131], a[76:79]\n"
    "v_mfma_f32_16x16x32_bf16 a[76:79], a[212:215], v[132:135], a[76:79]\n"
    "v_mfma_f32_16x16x32_bf16 a[76:79], a[216:219], v[136:139], a[76:79]\n"
    "v_mfma_f32_16x16x32_bf16 a[76:79], a[220:223], v[140:143], a[76:79]\n"
    "ds_read_b64_tr_b16 a[160:161], v208 offset:192\n"
    "s_waitcnt lgkmcnt(8)\n"
    "v_mfma_f32_16x16x32_bf16 a[80:83], a[224:227], v[16:19], a[80:83]\n"
    "ds_read_b64_tr_b16 a[162:163], v208 offset:5312\n"
    "v_mfma_f32_16x16x32_bf16 a[80:83], a[228:231], v[20:23], a[80:83]\n"
    "ds_read_b64_tr_b16 a[164:165], v208 offset:10432\n"
    "v_mfma_f32_16x16x32_bf16 a[80:83], a[232:235], v[24:27], a[80:83]\n"
    "ds_read_b64_tr_b16 a[166:167], v208 offset:15552\n"
    "v_mfma_f32_16x16x32_bf16 a[80:83], a[236:239], v[28:31], a[80:83]\n"
    "ds_read_b64_tr_b16 a[168:169], v208 offset:20672\n"
    "s_waitcnt lgkmcnt(7)\n"
    "v_mfma_f32_16x16x32_bf16 a[80:83], a[240:243], v[80:83], a[80:83]\n"
    "ds_read_b64_tr_b16 a[170:171], v208 offset:25792\n"
    "v_accvgpr_read_b32 v176, a64\n"
    "v_mfma_f32_16x16x32_bf16 a[80:83], a[244:247], v[84:87], a[80:83]\n"
    "ds_read_b64_tr_b16 a[176:177], v210 offset:192\n"
    "v_accvgpr_read_b32 v177, a65\n"
    "v_mfma_f32_16x16x32_bf16 a[80:83], a[248:251], v[88:91], a[80:83]\n"
    "ds_read_b64_tr_b16 a[178:179], v210 offset:5312\n"
    "v_accvgpr_read_b32 v178, a66\n"
    "v_mfma_f32_16x16x32_bf16 a[80:83], a[252:255], v[92:95], a[80:83]\n"
    "ds_read_b64_tr_b16 a[172:173], v208 offset:30912\n"
    "v_accvgpr_read_b32 v179, a67\n"
    "v_mfma_f32_16x16x32_bf16 a[84:87], a[224:227], v[32:35], a[84:87]\n"
    "ds_read_b64_tr_b16 a[180:181], v210 offset:10432\n"
    "v_accvgpr_read_b32 v180, a68\n"
    "v_mfma_f32_16x16x32_bf16 a[84:87], a[228:231], v[36:39], a[84:87]\n"
    "ds_read_b64_tr_b16 a[174:175], v208 offset:36032\n"
    "v_accvgpr_read_b32 v181, a69\n"
    "v_mfma_f32_16x16x32_bf16 a[84:87], a[232:235], v[40:43], a[84:87]\n"
    "ds_read_b64_tr_b16 a[182:183], v210 offset:15552\n"
    "v_accvgpr_read_b32 v182, a70\n"
    "v_mfma_f32_16x16x32_bf16 a[84:87], a[236:239], v[44:47], a[84:87]\n"
    "ds_read_b64_tr_b16 a[184:185], v210 offset:20672\n"
    "v_accvgpr_read_b32 v183, a71\n"
    "v_mfma_f32_16x16x32_bf16 a[84:87], a[240:243], v[96:99], a[84:87]\n"
    "ds_read_b64_tr_b16 a[186:187], v210 offset:25792\n"
    "v_cvt_pk_bf16_f32 v184, v176, v177\n"
    "v_mfma_f32_16x16x32_bf16 a[84:87], a[244:247], v[100:103], a[84:87]\n"
    "ds_read_b64_tr_b16 a[188:189], v210 offset:30912\n"
    "v_cvt_pk_bf16_f32 v185, v178, v179\n"
    "v_mfma_f32_16x16x32_bf16 a[84:87], a[248:251], v[104:107], a[84:87]\n"
    "ds_read_b64_tr_b16 a[190:191], v210 offset:36032\n"
    "v_cvt_pk_bf16_f32 v186, v180, v181\n"
    "v_mfma_f32_16x16x32_bf16 a[84:87], a[252:255], v[108:111], a[84:87]\n"
    "v_cvt_pk_bf16_f32 v187, v182, v183\n"
    "s_add_u32 s86, s86, 4096\n"
    "v_mfma_f32_16x16x32_bf16 a[88:91], a[224:227], v[48:51], a[88:91]\n"
    "buffer_store_dwordx4 v[184:187], v226, s[44:47], s86 offen offset:0 sc1\n"
    "v_mfma_f32_16x16x32_bf16 a[88:91], a[228:231], v[52:55], a[88:91]\n"
    "ds_write_b128 v14, v[184:187] offset:4096\n"
    "v_accvgpr_read_b32 v144, a72\n"
    "v_mfma_f32_16x16x32_bf16 a[88:91], a[232:235], v[56:59], a[88:91]\n"
    "v_accvgpr_read_b32 v145, a73\n"
    "v_accvgpr_read_b32 v146, a74\n"
    "v_mfma_f32_16x16x32_bf16 a[88:91], a[236:239], v[60:63], a[88:91]\n"
    "v_accvgpr_read_b32 v147, a75\n"
    "v_accvgpr_read_b32 v148, a76\n"
    "v_mfma_f32_16x16x32_bf16 a[88:91], a[240:243], v[112:115], a[88:91]\n"
    "v_accvgpr_read_b32 v149, a77\n"
    "v_accvgpr_read_b32 v150, a78\n"
    "v_mfma_f32_16x16x32_bf16 a[88:91], a[244:247], v[116:119], a[88:91]\n"
    "v_accvgpr_read_b32 v151, a79\n"
    "v_cvt_pk_bf16_f32 v152, v144, v145\n"
    "v_mfma_f32_16x16x32_bf16 a[88:91], a[248:251], v[120:123], a[88:91]\n"
    "v_cvt_pk_bf16_f32 v153, v146, v147\n"
    "v_cvt_pk_bf16_f32 v154, v148, v149\n"
    "v_mfma_f32_16x16x32_bf16 a[88:91], a[252:255], v[124:127], a[88:91]\n"
    "v_cvt_pk_bf16_f32 v155, v150, v151\n"
    "s_add_u32 s87, s87, 4096\n"
    "v_mfma_f32_16x16x32_bf16 a[92:95], a[224:227], v[64:67], a[92:95]\n"
    "buffer_store_dwordx4 v[152:155], v226, s[44:47], s87 offen offset:0 sc1\n"
    "v_mfma_f32_16x16x32_bf16 a[92:95], a[228:231], v[68:71], a[92:95]\n"
    "ds_write_b128 v15, v[152:155] offset:4096\n"
    "v_mfma_f32_16x16x32_bf16 a[92:95], a[232:235], v[72:75], a[92:95]\n"
    "v_mfma_f32_16x16x32_bf16 a[92:95], a[236:239], v[76:79], a[92:95]\n"
    "v_mfma_f32_16x16x32_bf16 a[92:95], a[240:243], v[128:131], a[92:95]\n"
    "v_mfma_f32_16x16x32_bf16 a[92:95], a[244:247], v[132:135], a[92:95]\n"
    "v_mfma_f32_16x16x32_bf16 a[92:95], a[248:251], v[136:139], a[92:95]\n"
    "v_mfma_f32_16x16x32_bf16 a[92:95], a[252:255], v[140:143], a[92:95]\n"
    "ds_read_b64_tr_b16 a[192:193], v209 offset:224\n"
    "s_waitcnt lgkmcnt(8)\n"
    "v_mfma_f32_16x16x32_bf16 a[96:99], a[160:163], v[16:19], a[96:99]\n"
    "ds_read_b64_tr_b16 a[194:195], v209 offset:5344\n"
    "v_mfma_f32_16x16x32_bf16 a[96:99], a[164:167], v[20:23], a[96:99]\n"
    "ds_read_b64_tr_b16 a[196:197], v209 offset:10464\n"
    "v_mfma_f32_16x16x32_bf16 a[96:99], a[168:171], v[24:27], a[96:99]\n"
    "ds_read_b64_tr_b16 a[198:199], v209 offset:15584\n"
    "v_mfma_f32_16x16x32_bf16 a[96:99], a[172:175], v[28:31], a[96:99]\n"
    "ds_read_b64_tr_b16 a[200:201], v209 offset:20704\n"
    "s_waitcnt lgkmcnt(7)\n"
    "v_mfma_f32_16x16x32_bf16 a[96:99], a[176:179], v[80:83], a[96:99]\n"
    "ds_read_b64_tr_b16 a[202:203], v209 offset:25824\n"
    "v_accvgpr_read_b32 v160, a80\n"
    "v_mfma_f32_16x16x32_bf16 a[96:99], a[180:183], v[84:87], a[96:99]\n"
    "ds_read_b64_tr_b16 a[208:209], v211 offset:224\n"
    "v_accvgpr_read_b32 v161, a81\n"
    "v_mfma_f32_16x16x32_bf16 a[96:99], a[184:187], v[88:91], a[96:99]\n"
    "ds_read_b64_tr_b16 a[210:211], v211 offset:5344\n"
    "v_accvgpr_read_b32 v162, a82\n"
    "v_mfma_f32_16x16x32_bf16 a[96:99], a[188:191], v[92:95], a[96:99]\n"
    "ds_read_b64_tr_b16 a[204:205], v209 offset:30944\n"
    "v_accvgpr_read_b32 v163, a83\n"
    "v_mfma_f32_16x16x32_bf16 a[100:103], a[160:163], v[32:35], a[100:103]\n"
    "ds_read_b64_tr_b16 a[212:213], v211 offset:10464\n"
    "v_accvgpr_read_b32 v164, a84\n"
    "v_mfma_f32_16x16x32_bf16 a[100:103], a[164:167], v[36:39], a[100:103]\n"
    "ds_read_b64_tr_b16 a[206:207], v209 offset:36064\n"
    "v_accvgpr_read_b32 v165, a85\n"
    "v_mfma_f32_16x16x32_bf16 a[100:103], a[168:171], v[40:43], a[100:103]\n"
    "ds_read_b64_tr_b16 a[214:215], v211 offset:15584\n"
    "v_accvgpr_read_b32 v166, a86\n"
    "v_mfma_f32_16x16x32_bf16 a[100:103], a[172:175], v[44:47], a[100:103]\n"
    "ds_read_b64_tr_b16 a[216:217], v211 offset:20704\n"
    "v_accvgpr_read_b32 v167, a87\n"
    "v_mfma_f32_16x16x32_bf16 a[100:103], a[176:179], v[96:99], a[100:103]\n"
    "ds_read_b64_tr_b16 a[218:219], v211 offset:25824\n"
    "v_cvt_pk_bf16_f32 v168, v160, v161\n"
    "v_mfma_f32_16x16x32_bf16 a[100:103], a[180:183], v[100:103], a[100:103]\n"
    "ds_read_b64_tr_b16 a[220:221], v211 offset:30944\n"
    "v_cvt_pk_bf16_f32 v169, v162, v163\n"
    "v_mfma_f32_16x16x32_bf16 a[100:103], a[184:187], v[104:107], a[100:103]\n"
    "ds_read_b64_tr_b16 a[222:223], v211 offset:36064\n"
    "v_cvt_pk_bf16_f32 v170, v164, v165\n"
    "v_mfma_f32_16x16x32_bf16 a[100:103], a[188:191], v[108:111], a[100:103]\n"
    "v_cvt_pk_bf16_f32 v171, v166, v167\n"
    "v_mfma_f32_16x16x32_bf16 a[104:107], a[160:163], v[48:51], a[104:107]\n"
    "buffer_store_dwordx4 v[168:171], v226, s[44:47], s86 offen offset:1024 sc1\n"
    "v_mfma_f32_16x16x32_bf16 a[104:107], a[164:167], v[52:55], a[104:107]\n"
    "ds_write_b128 v14, v[168:171] offset:5120\n"
    "v_accvgpr_read_b32 v176, a88\n"
    "v_mfma_f32_16x16x32_bf16 a[104:107], a[168:171], v[56:59], a[104:107]\n"
    "v_accvgpr_read_b32 v177, a89\n"
    "v_accvgpr_read_b32 v178, a90\n"
    "v_mfma_f32_16x16x32_bf16 a[104:107], a[172:175], v[60:63], a[104:107]\n"
    "v_accvgpr_read_b32 v179, a91\n"
    "v_accvgpr_read_b32 v180, a92\n"
    "v_mfma_f32_16x16x32_bf16 a[104:107], a[176:179], v[112:115], a[104:107]\n"
    "v_accvgpr_read_b32 v181, a93\n"
    "v_accvgpr_read_b32 v182, a94\n"
    "v_mfma_f32_16x16x32_bf16 a[104:107], a[180:183], v[116:119], a[104:107]\n"
    "v_accvgpr_read_b32 v183, a95\n"
    "v_cvt_pk_bf16_f32 v184, v176, v177\n"
    "v_mfma_f32_16x16x32_bf16 a[104:107], a[184:187], v[120:123], a[104:107]\n"
    "v_cvt_pk_bf16_f32 v185, v178, v179\n"
    "v_cvt_pk_bf16_f32 v186, v180, v181\n"
    "v_mfma_f32_16x16x32_bf16 a[104:107], a[188:191], v[124:127], a[104:107]\n"
    "v_cvt_pk_bf16_f32 v187, v182, v183\n"
    "v_mfma_f32_16x16x32_bf16 a[108:111], a[160:163], v[64:67], a[108:111]\n"
    "buffer_store_dwordx4 v[184:187], v226, s[44:47], s87 offen offset:1024 sc1\n"
    "v_mfma_f32_16x16x32_bf16 a[108:111], a[164:167], v[68:71], a[108:111]\n"
    "ds_write_b128 v15, v[184:187] offset:5120\n"
    "v_mfma_f32_16x16x32_bf16 a[108:111], a[168:171], v[72:75], a[108:111]\n"
    "v_mfma_f32_16x16x32_bf16 a[108:111], a[172:175], v[76:79], a[108:111]\n"
    "v_mfma_f32_16x16x32_bf16 a[108:111], a[176:179], v[128:131], a[108:111]\n"
    "v_mfma_f32_16x16x32_bf16 a[108:111], a[180:183], v[132:135], a[108:111]\n"
    "v_mfma_f32_16x16x32_bf16 a[108:111], a[184:187], v[136:139], a[108:111]\n"
    "v_mfma_f32_16x16x32_bf16 a[108:111], a[188:191], v[140:143], a[108:111]\n"
    "ds_read_b64_tr_b16 a[224:225], v208 offset:256\n"
    "s_waitcnt lgkmcnt(8)\n"
    "v_mfma_f32_16x16x32_bf16 a[112:115], a[192:195], v[16:19], a[112:115]\n"
    "ds_read_b64_tr_b16 a[226:227], v208 offset:5376\n"
    "v_mfma_f32_16x16x32_bf16 a[112:115], a[196:199], v[20:23], a[112:115]\n"
    "ds_read_b64_tr_b16 a[228:229], v208 offset:10496\n"
    "v_mfma_f32_16x16x32_bf16 a[112:115], a[200:203], v[24:27], a[112:115]\n"
    "ds_read_b64_tr_b16 a[230:231], v208 offset:15616\n"
    "v_mfma_f32_16x16x32_bf16 a[112:115], a[204:207], v[28:31], a[112:115]\n"
    "ds_read_b64_tr_b16 a[232:233], v208 offset:20736\n"
    "s_waitcnt lgkmcnt(7)\n"
    "v_mfma_f32_16x16x32_bf16 a[112:115], a[208:211], v[80:83], a[112:115]\n"
    "ds_read_b64_tr_b16 a[234:235], v208 offset:25856\n"
    "v_accvgpr_read_b32 v144, a96\n"
    "v_mfma_f32_16x16x32_bf16 a[112:115], a[212:215], v[84:87], a[112:115]\n"
    "ds_read_b64_tr_b16 a[240:241], v210 offset:256\n"
    "v_accvgpr_read_b32 v145, a97\n"
    "v_mfma_f32_16x16x32_bf16 a[112:115], a[216:219], v[88:91], a[112:115]\n"
    "ds_read_b64_tr_b16 a[242:243], v210 offset:5376\n"
    "v_accvgpr_read_b32 v146, a98\n"
    "v_mfma_f32_16x16x32_bf16 a[112:115], a[220:223], v[92:95], a[112:115]\n"
    "ds_read_b64_tr_b16 a[236:237], v208 offset:30976\n"
    "v_accvgpr_read_b32 v147, a99\n"
    "v_mfma_f32_16x16x32_bf16 a[116:119], a[192:195], v[32:35], a[116:119]\n"
    "ds_read_b64_tr_b16 a[244:245], v210 offset:10496\n"
    "v_accvgpr_read_b32 v148, a100\n"
    "v_mfma_f32_16x16x32_bf16 a[116:119], a[196:199], v[36:39], a[116:119]\n"
    "ds_read_b64_tr_b16 a[238:239], v208 offset:36096\n"
    "v_accvgpr_read_b32 v149, a101\n"
    "v_mfma_f32_16x16x32_bf16 a[116:119], a[200:203], v[40:43], a[116:119]\n"
    "ds_read_b64_tr_b16 a[246:247], v210 offset:15616\n"
    "v_accvgpr_read_b32 v150, a102\n"
    "v_mfma_f32_16x16x32_bf16 a[116:119], a[204:207], v[44:47], a[116:119]\n"
    "ds_read_b64_tr_b16 a[248:249], v210 offset:20736\n"
    "v_accvgpr_read_b32 v151, a103\n"
    "v_mfma_f32_16x16x32_bf16 a[116:119], a[208:211], v[96:99], a[116:119]\n"
    "ds_read_b64_tr_b16 a[250:251], v210 offset:25856\n"
    "v_cvt_pk_bf16_f32 v152, v144, v145\n"
    "v_mfma_f32_16x16x32_bf16 a[116:119], a[212:215], v[100:103], a[116:119]\n"
    "ds_read_b64_tr_b16 a[252:253], v210 offset:30976\n"
    "v_cvt_pk_bf16_f32 v153, v146, v147\n"
    "v_mfma_f32_16x16x32_bf16 a[116:119], a[216:219], v[104:107], a[116:119]\n"
    "ds_read_b64_tr_b16 a[254:255], v210 offset:36096\n"
    "v_cvt_pk_bf16_f32 v154, v148, v149\n"
    "v_mfma_f32_16x16x32_bf16 a[116:119], a[220:223], v[108:111], a[116:119]\n"
    "v_cvt_pk_bf16_f32 v155, v150, v151\n"
    "v_mfma_f32_16x16x32_bf16 a[120:123], a[192:195], v[48:51], a[120:123]\n"
    "buffer_store_dwordx4 v[152:155], v226, s[44:47], s86 offen offset:2048 sc1\n"
    "v_mfma_f32_16x16x32_bf16 a[120:123], a[196:199], v[52:55], a[120:123]\n"
    "ds_write_b128 v14, v[152:155] offset:6144\n"
    "v_accvgpr_read_b32 v160, a104\n"
    "v_mfma_f32_16x16x32_bf16 a[120:123], a[200:203], v[56:59], a[120:123]\n"
    "v_accvgpr_read_b32 v161, a105\n"
    "v_accvgpr_read_b32 v162, a106\n"
    "v_mfma_f32_16x16x32_bf16 a[120:123], a[204:207], v[60:63], a[120:123]\n"
    "v_accvgpr_read_b32 v163, a107\n"
    "v_accvgpr_read_b32 v164, a108\n"
    "v_mfma_f32_16x16x32_bf16 a[120:123], a[208:211], v[112:115], a[120:123]\n"
    "v_accvgpr_read_b32 v165, a109\n"
    "v_accvgpr_read_b32 v166, a110\n"
    "v_mfma_f32_16x16x32_bf16 a[120:123], a[212:215], v[116:119], a[120:123]\n"
    "v_accvgpr_read_b32 v167, a111\n"
    "v_cvt_pk_bf16_f32 v168, v160, v161\n"
    "v_mfma_f32_16x16x32_bf16 a[120:123], a[216:219], v[120:123], a[120:123]\n"
    "v_cvt_pk_bf16_f32 v169, v162, v163\n"
    "v_cvt_pk_bf16_f32 v170, v164, v165\n"
    "v_mfma_f32_16x16x32_bf16 a[120:123], a[220:223], v[124:127], a[120:123]\n"
    "v_cvt_pk_bf16_f32 v171, v166, v167\n"
    "v_mfma_f32_16x16x32_bf16 a[124:127], a[192:195], v[64:67], a[124:127]\n"
    "buffer_store_dwordx4 v[168:171], v226, s[44:47], s87 offen offset:2048 sc1\n"
    "v_mfma_f32_16x16x32_bf16 a[124:127], a[196:199], v[68:71], a[124:127]\n"
    "ds_write_b128 v15, v[168:171] offset:6144\n"
    "v_mfma_f32_16x16x32_bf16 a[124:127], a[200:203], v[72:75], a[124:127]\n"
    "v_mfma_f32_16x16x32_bf16 a[124:127], a[204:207], v[76:79], a[124:127]\n"
    "v_mfma_f32_16x16x32_bf16 a[124:127], a[208:211], v[128:131], a[124:127]\n"
    "v_mfma_f32_16x16x32_bf16 a[124:127], a[212:215], v[132:135], a[124:127]\n"
    "v_mfma_f32_16x16x32_bf16 a[124:127], a[216:219], v[136:139], a[124:127]\n"
    "v_mfma_f32_16x16x32_bf16 a[124:127], a[220:223], v[140:143], a[124:127]\n"
    "ds_read_b64_tr_b16 a[160:161], v209 offset:288\n"
    "s_waitcnt lgkmcnt(8)\n"
    "v_mfma_f32_16x16x32_bf16 a[128:131], a[224:227], v[16:19], a[128:131]\n"
    "ds_read_b64_tr_b16 a[162:163], v209 offset:5408\n"
    "v_mfma_f32_16x16x32_bf16 a[128:131], a[228:231], v[20:23], a[128:131]\n"
    "ds_read_b64_tr_b16 a[164:165], v209 offset:10528\n"
    "v_mfma_f32_16x16x32_bf16 a[128:131], a[232:235], v[24:27], a[128:131]\n"
    "ds_read_b64_tr_b16 a[166:167], v209 offset:15648\n"
    "v_mfma_f32_16x16x32_bf16 a[128:131], a[236:239], v[28:31], a[128:131]\n"
    "ds_read_b64_tr_b16 a[168:169], v209 offset:20768\n"
    "s_waitcnt lgkmcnt(7)\n"
    "v_mfma_f32_16x16x32_bf16 a[128:131], a[240:243], v[80:83], a[128:131]\n"
    "ds_read_b64_tr_b16 a[170:171], v209 offset:25888\n"
    "v_accvgpr_read_b32 v176, a112\n"
    "v_mfma_f32_16x16x32_bf16 a[128:131], a[244:247], v[84:87], a[128:131]\n"
    "ds_read_b64_tr_b16 a[176:177], v211 offset:288\n"
    "v_accvgpr_read_b32 v177, a113\n"
    "v_mfma_f32_16x16x32_bf16 a[128:131], a[248:251], v[88:91], a[128:131]\n"
    "ds_read_b64_tr_b16 a[178:179], v211 offset:5408\n"
    "v_accvgpr_read_b32 v178, a114\n"
    "v_mfma_f32_16x16x32_bf16 a[128:131], a[252:255], v[92:95], a[128:131]\n"
    "ds_read_b64_tr_b16 a[172:173], v209 offset:31008\n"
    "v_accvgpr_read_b32 v179, a115\n"
    "v_mfma_f32_16x16x32_bf16 a[132:135], a[224:227], v[32:35], a[132:135]\n"
    "ds_read_b64_tr_b16 a[180:181], v211 offset:10528\n"
    "v_accvgpr_read_b32 v180, a116\n"
    "v_mfma_f32_16x16x32_bf16 a[132:135], a[228:231], v[36:39], a[132:135]\n"
    "ds_read_b64_tr_b16 a[174:175], v209 offset:36128\n"
    "v_accvgpr_read_b32 v181, a117\n"
    "v_mfma_f32_16x16x32_bf16 a[132:135], a[232:235], v[40:43], a[132:135]\n"
    "ds_read_b64_tr_b16 a[182:183], v211 offset:15648\n"
    "v_accvgpr_read_b32 v182, a118\n"
    "v_mfma_f32_16x16x32_bf16 a[132:135], a[236:239], v[44:47], a[132:135]\n"
    "ds_read_b64_tr_b16 a[184:185], v211 offset:20768\n"
    "v_accvgpr_read_b32 v183, a119\n"
    "v_mfma_f32_16x16x32_bf16 a[132:135], a[240:243], v[96:99], a[132:135]\n"
    "ds_read_b64_tr_b16 a[186:187], v211 offset:25888\n"
    "v_cvt_pk_bf16_f32 v184, v176, v177\n"
    "v_mfma_f32_16x16x32_bf16 a[132:135], a[244:247], v[100:103], a[132:135]\n"
    "ds_read_b64_tr_b16 a[188:189], v211 offset:31008\n"
    "v_cvt_pk_bf16_f32 v185, v178, v179\n"
    "v_mfma_f32_16x16x32_bf16 a[132:135], a[248:251], v[104:107], a[132:135]\n"
    "ds_read_b64_tr_b16 a[190:191], v211 offset:36128\n"
    "v_cvt_pk_bf16_f32 v186, v180, v181\n"
    "v_mfma_f32_16x16x32_bf16 a[132:135], a[252:255], v[108:111], a[132:135]\n"
    "v_cvt_pk_bf16_f32 v187, v182, v183\n"
    "v_mfma_f32_16x16x32_bf16 a[136:139], a[224:227], v[48:51], a[136:139]\n"
    "buffer_store_dwordx4 v[184:187], v226, s[44:47], s86 offen offset:3072 sc1\n"
    "v_mfma_f32_16x16x32_bf16 a[136:139], a[228:231], v[52:55], a[136:139]\n"
    "ds_write_b128 v14, v[184:187] offset:7168\n"
    "v_accvgpr_read_b32 v144, a120\n"
    "v_mfma_f32_16x16x32_bf16 a[136:139], a[232:235], v[56:59], a[136:139]\n"
    "v_accvgpr_read_b32 v145, a121\n"
    "v_accvgpr_read_b32 v146, a122\n"
    "v_mfma_f32_16x16x32_bf16 a[136:139], a[236:239], v[60:63], a[136:139]\n"
    "v_accvgpr_read_b32 v147, a123\n"
    "v_accvgpr_read_b32 v148, a124\n"
    "v_mfma_f32_16x16x32_bf16 a[136:139], a[240:243], v[112:115], a[136:139]\n"
    "v_accvgpr_read_b32 v149, a125\n"
    "v_accvgpr_read_b32 v150, a126\n"
    "v_mfma_f32_16x16x32_bf16 a[136:139], a[244:247], v[116:119], a[136:139]\n"
    "v_accvgpr_read_b32 v151, a127\n"
    "v_cvt_pk_bf16_f32 v152, v144, v145\n"
    "v_mfma_f32_16x16x32_bf16 a[136:139], a[248:251], v[120:123], a[136:139]\n"
    "v_cvt_pk_bf16_f32 v153, v146, v147\n"
    "v_cvt_pk_bf16_f32 v154, v148, v149\n"
    "v_mfma_f32_16x16x32_bf16 a[136:139], a[252:255], v[124:127], a[136:139]\n"
    "v_cvt_pk_bf16_f32 v155, v150, v151\n"
    "v_mfma_f32_16x16x32_bf16 a[140:143], a[224:227], v[64:67], a[140:143]\n"
    "buffer_store_dwordx4 v[152:155], v226, s[44:47], s87 offen offset:3072 sc1\n"
    "v_mfma_f32_16x16x32_bf16 a[140:143], a[228:231], v[68:71], a[140:143]\n"
    "ds_write_b128 v15, v[152:155] offset:7168\n"
    "v_mfma_f32_16x16x32_bf16 a[140:143], a[232:235], v[72:75], a[140:143]\n"
    "v_mfma_f32_16x16x32_bf16 a[140:143], a[236:239], v[76:79], a[140:143]\n"
    "v_mfma_f32_16x16x32_bf16 a[140:143], a[240:243], v[128:131], a[140:143]\n"
    "v_mfma_f32_16x16x32_bf16 a[140:143], a[244:247], v[132:135], a[140:143]\n"
    "v_mfma_f32_16x16x32_bf16 a[140:143], a[248:251], v[136:139], a[140:143]\n"
    "v_mfma_f32_16x16x32_bf16 a[140:143], a[252:255], v[140:143], a[140:143]\n"
    "s_waitcnt lgkmcnt(7)\n"
    "v_mfma_f32_16x16x32_bf16 a[144:147], a[160:163], v[16:19], a[144:147]\n"
    "v_mfma_f32_16x16x32_bf16 a[144:147], a[164:167], v[20:23], a[144:147]\n"
    "v_mfma_f32_16x16x32_bf16 a[144:147], a[168:171], v[24:27], a[144:147]\n"
    "v_mfma_f32_16x16x32_bf16 a[144:147], a[172:175], v[28:31], a[144:147]\n"
    "s_waitcnt lgkmcnt(2)\n"
    "v_mfma_f32_16x16x32_bf16 a[144:147], a[176:179], v[80:83], a[144:147]\n"
    "v_accvgpr_read_b32 v160, a128\n"
    "v_accvgpr_read_b32 v161, a129\n"
    "v_mfma_f32_16x16x32_bf16 a[144:147], a[180:183], v[84:87], a[144:147]\n"
    "v_accvgpr_read_b32 v162, a130\n"
    "v_accvgpr_read_b32 v163, a131\n"
    "v_mfma_f32_16x16x32_bf16 a[144:147], a[184:187], v[88:91], a[144:147]\n"
    "v_accvgpr_read_b32 v164, a132\n"
    "v_accvgpr_read_b32 v165, a133\n"
    "v_mfma_f32_16x16x32_bf16 a[144:147], a[188:191], v[92:95], a[144:147]\n"
    "v_accvgpr_read_b32 v166, a134\n"
    "v_accvgpr_read_b32 v167, a135\n"
    "v_mfma_f32_16x16x32_bf16 a[148:151], a[160:163], v[32:35], a[148:151]\n"
    "v_cvt_pk_bf16_f32 v168, v160, v161\n"
    "v_cvt_pk_bf16_f32 v169, v162, v163\n"
    "v_mfma_f32_16x16x32_bf16 a[148:151], a[164:167], v[36:39], a[148:151]\n"
    "v_cvt_pk_bf16_f32 v170, v164, v165\n"
    "v_cvt_pk_bf16_f32 v171, v166, v167\n"
    "v_mfma_f32_16x16x32_bf16 a[148:151], a[168:171], v[40:43], a[148:151]\n"
    "s_add_u32 s86, s86, 4096\n"
    "v_mfma_f32_16x16x32_bf16 a[148:151], a[172:175], v[44:47], a[148:151]\n"
    "buffer_store_dwordx4 v[168:171], v226, s[44:47], s86 offen offset:0 sc1\n"
    "v_mfma_f32_16x16x32_bf16 a[148:151], a[176:179], v[96:99], a[148:151]\n"
    "ds_write_b128 v14, v[168:171] offset:8192\n"
    "v_accvgpr_read_b32 v176, a136\n"
    "v_mfma_f32_16x16x32_bf16 a[148:151], a[180:183], v[100:103], a[148:151]\n"
    "v_accvgpr_read_b32 v177, a137\n"
    "v_accvgpr_read_b32 v178, a138\n"
    "v_mfma_f32_16x16x32_bf16 a[148:151], a[184:187], v[104:107], a[148:151]\n"
    "v_accvgpr_read_b32 v179, a139\n"
    "v_accvgpr_read_b32 v180, a140\n"
    "v_mfma_f32_16x16x32_bf16 a[148:151], a[188:191], v[108:111], a[148:151]\n"
    "v_accvgpr_read_b32 v181, a141\n"
    "v_accvgpr_read_b32 v182, a142\n"
    "v_mfma_f32_16x16x32_bf16 a[152:155], a[160:163], v[48:51], a[152:155]\n"
    "v_accvgpr_read_b32 v183, a143\n"
    "v_cvt_pk_bf16_f32 v184, v176, v177\n"
    "v_mfma_f32_16x16x32_bf16 a[152:155], a[164:167], v[52:55], a[152:155]\n"
    "v_cvt_pk_bf16_f32 v185, v178, v179\n"
    "v_cvt_pk_bf16_f32 v186, v180, v181\n"
    "v_mfma_f32_16x16x32_bf16 a[152:155], a[168:171], v[56:59], a[152:155]\n"
    "v_cvt_pk_bf16_f32 v187, v182, v183\n"
    "s_add_u32 s87, s87, 4096\n"
    "v_mfma_f32_16x16x32_bf16 a[152:155], a[172:175], v[60:63], a[152:155]\n"
    "buffer_store_dwordx4 v[184:187], v226, s[44:47], s87 offen offset:0 sc1\n"
    "v_mfma_f32_16x16x32_bf16 a[152:155], a[176:179], v[112:115], a[152:155]\n"
    "ds_write_b128 v15, v[184:187] offset:8192\n"
    "v_mfma_f32_16x16x32_bf16 a[152:155], a[180:183], v[116:119], a[152:155]\n"
    "v_mfma_f32_16x16x32_bf16 a[152:155], a[184:187], v[120:123], a[152:155]\n"
    "v_mfma_f32_16x16x32_bf16 a[152:155], a[188:191], v[124:127], a[152:155]\n"
    "v_mfma_f32_16x16x32_bf16 a[156:159], a[160:163], v[64:67], a[156:159]\n"
    "v_mfma_f32_16x16x32_bf16 a[156:159], a[164:167], v[68:71], a[156:159]\n"
    "v_mfma_f32_16x16x32_bf16 a[156:159], a[168:171], v[72:75], a[156:159]\n"
    "v_mfma_f32_16x16x32_bf16 a[156:159], a[172:175], v[76:79], a[156:159]\n"
    "v_mfma_f32_16x16x32_bf16 a[156:159], a[176:179], v[128:131], a[156:159]\n"
    "v_mfma_f32_16x16x32_bf16 a[156:159], a[180:183], v[132:135], a[156:159]\n"
    "v_mfma_f32_16x16x32_bf16 a[156:159], a[184:187], v[136:139], a[156:159]\n"
    "v_mfma_f32_16x16x32_bf16 a[156:159], a[188:191], v[140:143], a[156:159]\n"
    "v_accvgpr_read_b32 v144, a144\n"
    "v_accvgpr_read_b32 v145, a145\n"
    "v_accvgpr_read_b32 v146, a146\n"
    "v_accvgpr_read_b32 v147, a147\n"
    "v_accvgpr_read_b32 v148, a148\n"
    "v_accvgpr_read_b32 v149, a149\n"
    "v_accvgpr_read_b32 v150, a150\n"
    "v_accvgpr_read_b32 v151, a151\n"
    "v_cvt_pk_bf16_f32 v152, v144, v145\n"
    "v_cvt_pk_bf16_f32 v153, v146, v147\n"
    "v_cvt_pk_bf16_f32 v154, v148, v149\n"
    "v_cvt_pk_bf16_f32 v155, v150, v151\n"
    "buffer_store_dwordx4 v[152:155], v226, s[44:47], s86 offen offset:1024 sc1\n"
    "ds_write_b128 v14, v[152:155] offset:9216\n"
    "v_accvgpr_read_b32 v160, a152\n"
    "v_accvgpr_read_b32 v161, a153\n"
    "v_accvgpr_read_b32 v162, a154\n"
    "v_accvgpr_read_b32 v163, a155\n"
    "v_accvgpr_read_b32 v164, a156\n"
    "v_accvgpr_read_b32 v165, a157\n"
    "v_accvgpr_read_b32 v166, a158\n"
    "v_accvgpr_read_b32 v167, a159\n"
    "v_cvt_pk_bf16_f32 v168, v160, v161\n"
    "v_cvt_pk_bf16_f32 v169, v162, v163\n"
    "v_cvt_pk_bf16_f32 v170, v164, v165\n"
    "v_cvt_pk_bf16_f32 v171, v166, v167\n"
    "buffer_store_dwordx4 v[168:171], v226, s[44:47], s87 offen offset:1024 sc1\n"
    "ds_write_b128 v15, v[168:171] offset:9216\n"
    "s_branch L_publish\n"
    "L_ffn:\n"
    "s_nop 15\n"
    "s_mov_b32 s86, s22\n"
    "s_mov_b32 s87, s23\n"
    "v_accvgpr_read_b32 v144, a0\n"
    "v_accvgpr_read_b32 v145, a1\n"
    "v_accvgpr_read_b32 v146, a2\n"
    "v_accvgpr_read_b32 v147, a3\n"
    "v_accvgpr_read_b32 v148, a4\n"
    "v_accvgpr_read_b32 v149, a5\n"
    "v_accvgpr_read_b32 v150, a6\n"
    "v_accvgpr_read_b32 v151, a7\n"
    "v_cvt_pk_bf16_f32 v152, v144, v145\n"
    "v_cvt_pk_bf16_f32 v153, v146, v147\n"
    "v_cvt_pk_bf16_f32 v154, v148, v149\n"
    "v_cvt_pk_bf16_f32 v155, v150, v151\n"
    "buffer_store_dwordx4 v[152:155], v226, s[44:47], s86 offen offset:0 sc1\n"
    "ds_write_b128 v14, v[152:155] offset:0\n"
    "v_accvgpr_read_b32 v160, a8\n"
    "v_accvgpr_read_b32 v161, a9\n"
    "v_accvgpr_read_b32 v162, a10\n"
    "v_accvgpr_read_b32 v163, a11\n"
    "v_accvgpr_read_b32 v164, a12\n"
    "v_accvgpr_read_b32 v165, a13\n"
    "v_accvgpr_read_b32 v166, a14\n"
    "v_accvgpr_read_b32 v167, a15\n"
    "v_cvt_pk_bf16_f32 v168, v160, v161\n"
    "v_cvt_pk_bf16_f32 v169, v162, v163\n"
    "v_cvt_pk_bf16_f32 v170, v164, v165\n"
    "v_cvt_pk_bf16_f32 v171, v166, v167\n"
    "buffer_store_dwordx4 v[168:171], v226, s[44:47], s87 offen offset:0 sc1\n"
    "ds_write_b128 v15, v[168:171] offset:0\n"
    "v_accvgpr_read_b32 v176, a16\n"
    "v_accvgpr_read_b32 v177, a17\n"
    "v_accvgpr_read_b32 v178, a18\n"
    "v_accvgpr_read_b32 v179, a19\n"
    "v_accvgpr_read_b32 v180, a20\n"
    "v_accvgpr_read_b32 v181, a21\n"
    "v_accvgpr_read_b32 v182, a22\n"
    "v_accvgpr_read_b32 v183, a23\n"
    "v_cvt_pk_bf16_f32 v184, v176, v177\n"
    "v_cvt_pk_bf16_f32 v185, v178, v179\n"
    "v_cvt_pk_bf16_f32 v186, v180, v181\n"
    "v_cvt_pk_bf16_f32 v187, v182, v183\n"
    "buffer_store_dwordx4 v[184:187], v226, s[44:47], s86 offen offset:1024 sc1\n"
    "ds_write_b128 v14, v[184:187] offset:1024\n"
    "v_accvgpr_read_b32 v144, a24\n"
    "v_accvgpr_read_b32 v145, a25\n"
    "v_accvgpr_read_b32 v146, a26\n"
    "v_accvgpr_read_b32 v147, a27\n"
    "v_accvgpr_read_b32 v148, a28\n"
    "v_accvgpr_read_b32 v149, a29\n"
    "v_accvgpr_read_b32 v150, a30\n"
    "v_accvgpr_read_b32 v151, a31\n"
    "v_cvt_pk_bf16_f32 v152, v144, v145\n"
    "v_cvt_pk_bf16_f32 v153, v146, v147\n"
    "v_cvt_pk_bf16_f32 v154, v148, v149\n"
    "v_cvt_pk_bf16_f32 v155, v150, v151\n"
    "buffer_store_dwordx4 v[152:155], v226, s[44:47], s87 offen offset:1024 sc1\n"
    "ds_write_b128 v15, v[152:155] offset:1024\n"
    "v_accvgpr_read_b32 v160, a32\n"
    "v_accvgpr_read_b32 v161, a33\n"
    "v_accvgpr_read_b32 v162, a34\n"
    "v_accvgpr_read_b32 v163, a35\n"
    "v_accvgpr_read_b32 v164, a36\n"
    "v_accvgpr_read_b32 v165, a37\n"
    "v_accvgpr_read_b32 v166, a38\n"
    "v_accvgpr_read_b32 v167, a39\n"
    "v_cvt_pk_bf16_f32 v168, v160, v161\n"
    "v_cvt_pk_bf16_f32 v169, v162, v163\n"
    "v_cvt_pk_bf16_f32 v170, v164, v165\n"
    "v_cvt_pk_bf16_f32 v171, v166, v167\n"
    "buffer_store_dwordx4 v[168:171], v226, s[44:47], s86 offen offset:2048 sc1\n"
    "ds_write_b128 v14, v[168:171] offset:2048\n"
    "v_accvgpr_read_b32 v176, a40\n"
    "v_accvgpr_read_b32 v177, a41\n"
    "v_accvgpr_read_b32 v178, a42\n"
    "v_accvgpr_read_b32 v179, a43\n"
    "v_accvgpr_read_b32 v180, a44\n"
    "v_accvgpr_read_b32 v181, a45\n"
    "v_accvgpr_read_b32 v182, a46\n"
    "v_accvgpr_read_b32 v183, a47\n"
    "v_cvt_pk_bf16_f32 v184, v176, v177\n"
    "v_cvt_pk_bf16_f32 v185, v178, v179\n"
    "v_cvt_pk_bf16_f32 v186, v180, v181\n"
    "v_cvt_pk_bf16_f32 v187, v182, v183\n"
    "buffer_store_dwordx4 v[184:187], v226, s[44:47], s87 offen offset:2048 sc1\n"
    "ds_write_b128 v15, v[184:187] offset:2048\n"
    "v_accvgpr_read_b32 v144, a48\n"
    "v_accvgpr_read_b32 v145, a49\n"
    "v_accvgpr_read_b32 v146, a50\n"
    "v_accvgpr_read_b32 v147, a51\n"
    "v_accvgpr_read_b32 v148, a52\n"
    "v_accvgpr_read_b32 v149, a53\n"
    "v_accvgpr_read_b32 v150, a54\n"
    "v_accvgpr_read_b32 v151, a55\n"
    "v_cvt_pk_bf16_f32 v152, v144, v145\n"
    "v_cvt_pk_bf16_f32 v153, v146, v147\n"
    "v_cvt_pk_bf16_f32 v154, v148, v149\n"
    "v_cvt_pk_bf16_f32 v155, v150, v151\n"
    "buffer_store_dwordx4 v[152:155], v226, s[44:47], s86 offen offset:3072 sc1\n"
    "ds_write_b128 v14, v[152:155] offset:3072\n"
    "v_accvgpr_read_b32 v160, a56\n"
    "v_accvgpr_read_b32 v161, a57\n"
    "v_accvgpr_read_b32 v162, a58\n"
    "v_accvgpr_read_b32 v163, a59\n"
    "v_accvgpr_read_b32 v164, a60\n"
    "v_accvgpr_read_b32 v165, a61\n"
    "v_accvgpr_read_b32 v166, a62\n"
    "v_accvgpr_read_b32 v167, a63\n"
    "v_cvt_pk_bf16_f32 v168, v160, v161\n"
    "v_cvt_pk_bf16_f32 v169, v162, v163\n"
    "v_cvt_pk_bf16_f32 v170, v164, v165\n"
    "v_cvt_pk_bf16_f32 v171, v166, v167\n"
    "buffer_store_dwordx4 v[168:171], v226, s[44:47], s87 offen offset:3072 sc1\n"
    "ds_write_b128 v15, v[168:171] offset:3072\n"
    "v_accvgpr_read_b32 v176, a64\n"
    "v_accvgpr_read_b32 v177, a65\n"
    "v_accvgpr_read_b32 v178, a66\n"
    "v_accvgpr_read_b32 v179, a67\n"
    "v_accvgpr_read_b32 v180, a68\n"
    "v_accvgpr_read_b32 v181, a69\n"
    "v_accvgpr_read_b32 v182, a70\n"
    "v_accvgpr_read_b32 v183, a71\n"
    "v_cvt_pk_bf16_f32 v184, v176, v177\n"
    "v_cvt_pk_bf16_f32 v185, v178, v179\n"
    "v_cvt_pk_bf16_f32 v186, v180, v181\n"
    "v_cvt_pk_bf16_f32 v187, v182, v183\n"
    "s_add_u32 s86, s86, 4096\n"
    "buffer_store_dwordx4 v[184:187], v226, s[44:47], s86 offen offset:0 sc1\n"
    "ds_write_b128 v14, v[184:187] offset:4096\n"
    "v_accvgpr_read_b32 v144, a72\n"
    "v_accvgpr_read_b32 v145, a73\n"
    "v_accvgpr_read_b32 v146, a74\n"
    "v_accvgpr_read_b32 v147, a75\n"
    "v_accvgpr_read_b32 v148, a76\n"
    "v_accvgpr_read_b32 v149, a77\n"
    "v_accvgpr_read_b32 v150, a78\n"
    "v_accvgpr_read_b32 v151, a79\n"
    "v_cvt_pk_bf16_f32 v152, v144, v145\n"
    "v_cvt_pk_bf16_f32 v153, v146, v147\n"
    "v_cvt_pk_bf16_f32 v154, v148, v149\n"
    "v_cvt_pk_bf16_f32 v155, v150, v151\n"
    "s_add_u32 s87, s87, 4096\n"
    "buffer_store_dwordx4 v[152:155], v226, s[44:47], s87 offen offset:0 sc1\n"
    "ds_write_b128 v15, v[152:155] offset:4096\n"
    "v_accvgpr_read_b32 v160, a80\n"
    "v_accvgpr_read_b32 v161, a81\n"
    "v_accvgpr_read_b32 v162, a82\n"
    "v_accvgpr_read_b32 v163, a83\n"
    "v_accvgpr_read_b32 v164, a84\n"
    "v_accvgpr_read_b32 v165, a85\n"
    "v_accvgpr_read_b32 v166, a86\n"
    "v_accvgpr_read_b32 v167, a87\n"
    "v_cvt_pk_bf16_f32 v168, v160, v161\n"
    "v_cvt_pk_bf16_f32 v169, v162, v163\n"
    "v_cvt_pk_bf16_f32 v170, v164, v165\n"
    "v_cvt_pk_bf16_f32 v171, v166, v167\n"
    "buffer_store_dwordx4 v[168:171], v226, s[44:47], s86 offen offset:1024 sc1\n"
    "ds_write_b128 v14, v[168:171] offset:5120\n"
    "v_accvgpr_read_b32 v176, a88\n"
    "v_accvgpr_read_b32 v177, a89\n"
    "v_accvgpr_read_b32 v178, a90\n"
    "v_accvgpr_read_b32 v179, a91\n"
    "v_accvgpr_read_b32 v180, a92\n"
    "v_accvgpr_read_b32 v181, a93\n"
    "v_accvgpr_read_b32 v182, a94\n"
    "v_accvgpr_read_b32 v183, a95\n"
    "v_cvt_pk_bf16_f32 v184, v176, v177\n"
    "v_cvt_pk_bf16_f32 v185, v178, v179\n"
    "v_cvt_pk_bf16_f32 v186, v180, v181\n"
    "v_cvt_pk_bf16_f32 v187, v182, v183\n"
    "buffer_store_dwordx4 v[184:187], v226, s[44:47], s87 offen offset:1024 sc1\n"
    "ds_write_b128 v15, v[184:187] offset:5120\n"
    "v_accvgpr_read_b32 v144, a96\n"
    "v_accvgpr_read_b32 v145, a97\n"
    "v_accvgpr_read_b32 v146, a98\n"
    "v_accvgpr_read_b32 v147, a99\n"
    "v_accvgpr_read_b32 v148, a100\n"
    "v_accvgpr_read_b32 v149, a101\n"
    "v_accvgpr_read_b32 v150, a102\n"
    "v_accvgpr_read_b32 v151, a103\n"
    "v_cvt_pk_bf16_f32 v152, v144, v145\n"
    "v_cvt_pk_bf16_f32 v153, v146, v147\n"
    "v_cvt_pk_bf16_f32 v154, v148, v149\n"
    "v_cvt_pk_bf16_f32 v155, v150, v151\n"
    "buffer_store_dwordx4 v[152:155], v226, s[44:47], s86 offen offset:2048 sc1\n"
    "ds_write_b128 v14, v[152:155] offset:6144\n"
    "v_accvgpr_read_b32 v160, a104\n"
    "v_accvgpr_read_b32 v161, a105\n"
    "v_accvgpr_read_b32 v162, a106\n"
    "v_accvgpr_read_b32 v163, a107\n"
    "v_accvgpr_read_b32 v164, a108\n"
    "v_accvgpr_read_b32 v165, a109\n"
    "v_accvgpr_read_b32 v166, a110\n"
    "v_accvgpr_read_b32 v167, a111\n"
    "v_cvt_pk_bf16_f32 v168, v160, v161\n"
    "v_cvt_pk_bf16_f32 v169, v162, v163\n"
    "v_cvt_pk_bf16_f32 v170, v164, v165\n"
    "v_cvt_pk_bf16_f32 v171, v166, v167\n"
    "buffer_store_dwordx4 v[168:171], v226, s[44:47], s87 offen offset:2048 sc1\n"
    "ds_write_b128 v15, v[168:171] offset:6144\n"
    "v_accvgpr_read_b32 v176, a112\n"
    "v_accvgpr_read_b32 v177, a113\n"
    "v_accvgpr_read_b32 v178, a114\n"
    "v_accvgpr_read_b32 v179, a115\n"
    "v_accvgpr_read_b32 v180, a116\n"
    "v_accvgpr_read_b32 v181, a117\n"
    "v_accvgpr_read_b32 v182, a118\n"
    "v_accvgpr_read_b32 v183, a119\n"
    "v_cvt_pk_bf16_f32 v184, v176, v177\n"
    "v_cvt_pk_bf16_f32 v185, v178, v179\n"
    "v_cvt_pk_bf16_f32 v186, v180, v181\n"
    "v_cvt_pk_bf16_f32 v187, v182, v183\n"
    "buffer_store_dwordx4 v[184:187], v226, s[44:47], s86 offen offset:3072 sc1\n"
    "ds_write_b128 v14, v[184:187] offset:7168\n"
    "v_accvgpr_read_b32 v144, a120\n"
    "v_accvgpr_read_b32 v145, a121\n"
    "v_accvgpr_read_b32 v146, a122\n"
    "v_accvgpr_read_b32 v147, a123\n"
    "v_accvgpr_read_b32 v148, a124\n"
    "v_accvgpr_read_b32 v149, a125\n"
    "v_accvgpr_read_b32 v150, a126\n"
    "v_accvgpr_read_b32 v151, a127\n"
    "v_cvt_pk_bf16_f32 v152, v144, v145\n"
    "v_cvt_pk_bf16_f32 v153, v146, v147\n"
    "v_cvt_pk_bf16_f32 v154, v148, v149\n"
    "v_cvt_pk_bf16_f32 v155, v150, v151\n"
    "buffer_store_dwordx4 v[152:155], v226, s[44:47], s87 offen offset:3072 sc1\n"
    "ds_write_b128 v15, v[152:155] offset:7168\n"
    "v_accvgpr_read_b32 v160, a128\n"
    "v_accvgpr_read_b32 v161, a129\n"
    "v_accvgpr_read_b32 v162, a130\n"
    "v_accvgpr_read_b32 v163, a131\n"
    "v_accvgpr_read_b32 v164, a132\n"
    "v_accvgpr_read_b32 v165, a133\n"
    "v_accvgpr_read_b32 v166, a134\n"
    "v_accvgpr_read_b32 v167, a135\n"
    "v_cvt_pk_bf16_f32 v168, v160, v161\n"
    "v_cvt_pk_bf16_f32 v169, v162, v163\n"
    "v_cvt_pk_bf16_f32 v170, v164, v165\n"
    "v_cvt_pk_bf16_f32 v171, v166, v167\n"
    "s_add_u32 s86, s86, 4096\n"
    "buffer_store_dwordx4 v[168:171], v226, s[44:47], s86 offen offset:0 sc1\n"
    "ds_write_b128 v14, v[168:171] offset:8192\n"
    "v_accvgpr_read_b32 v176, a136\n"
    "v_accvgpr_read_b32 v177, a137\n"
    "v_accvgpr_read_b32 v178, a138\n"
    "v_accvgpr_read_b32 v179, a139\n"
    "v_accvgpr_read_b32 v180, a140\n"
    "v_accvgpr_read_b32 v181, a141\n"
    "v_accvgpr_read_b32 v182, a142\n"
    "v_accvgpr_read_b32 v183, a143\n"
    "v_cvt_pk_bf16_f32 v184, v176, v177\n"
    "v_cvt_pk_bf16_f32 v185, v178, v179\n"
    "v_cvt_pk_bf16_f32 v186, v180, v181\n"
    "v_cvt_pk_bf16_f32 v187, v182, v183\n"
    "s_add_u32 s87, s87, 4096\n"
    "buffer_store_dwordx4 v[184:187], v226, s[44:47], s87 offen offset:0 sc1\n"
    "ds_write_b128 v15, v[184:187] offset:8192\n"
    "v_accvgpr_read_b32 v144, a144\n"
    "v_accvgpr_read_b32 v145, a145\n"
    "v_accvgpr_read_b32 v146, a146\n"
    "v_accvgpr_read_b32 v147, a147\n"
    "v_accvgpr_read_b32 v148, a148\n"
    "v_accvgpr_read_b32 v149, a149\n"
    "v_accvgpr_read_b32 v150, a150\n"
    "v_accvgpr_read_b32 v151, a151\n"
    "v_cvt_pk_bf16_f32 v152, v144, v145\n"
    "v_cvt_pk_bf16_f32 v153, v146, v147\n"
    "v_cvt_pk_bf16_f32 v154, v148, v149\n"
    "v_cvt_pk_bf16_f32 v155, v150, v151\n"
    "buffer_store_dwordx4 v[152:155], v226, s[44:47], s86 offen offset:1024 sc1\n"
    "ds_write_b128 v14, v[152:155] offset:9216\n"
    "v_accvgpr_read_b32 v160, a152\n"
    "v_accvgpr_read_b32 v161, a153\n"
    "v_accvgpr_read_b32 v162, a154\n"
    "v_accvgpr_read_b32 v163, a155\n"
    "v_accvgpr_read_b32 v164, a156\n"
    "v_accvgpr_read_b32 v165, a157\n"
    "v_accvgpr_read_b32 v166, a158\n"
    "v_accvgpr_read_b32 v167, a159\n"
    "v_cvt_pk_bf16_f32 v168, v160, v161\n"
    "v_cvt_pk_bf16_f32 v169, v162, v163\n"
    "v_cvt_pk_bf16_f32 v170, v164, v165\n"
    "v_cvt_pk_bf16_f32 v171, v166, v167\n"
    "buffer_store_dwordx4 v[168:171], v226, s[44:47], s87 offen offset:1024 sc1\n"
    "ds_write_b128 v15, v[168:171] offset:9216\n"
    "L_publish:\n"
    "s_lshl_b32 s82, s68, 6\n"
    "s_add_u32 s82, s82, s65\n"
    "s_mul_i32 s82, s82, 10240\n"
    "s_lshl_b32 s83, s64, 10\n"
    "s_add_u32 s18, s82, s83\n"
    "s_lshl_b32 s85, s66, 8\n"
    "s_lshl_b32 s86, s65, 5\n"
    "s_add_u32 s85, s85, s86\n"
    "s_mul_i32 s85, s85, 1280\n"
    "s_mul_i32 s86, s67, 320\n"
    "s_add_u32 s85, s85, s86\n"
    "s_lshl_b32 s86, s64, 5\n"
    "s_add_u32 s85, s85, s86\n"
    "s_add_u32 s12, s85, 0\n"
    "s_add_u32 s13, s12, 20480\n"
    "s_add_u32 s14, s85, 128\n"
    "s_add_u32 s15, s14, 20480\n"
    "s_add_u32 s16, s85, 256\n"
    "s_add_u32 s17, s16, 20480\n"
    "s_add_u32 s19, s18, 8192\n"
    "s_cmp_lt_u32 s64, 2\n"
    "s_cselect_b32 s19, s19, s75\n"
    "s_cselect_b32 s16, s16, s75\n"
    "s_cselect_b32 s17, s17, s75\n"
    "buffer_load_dwordx2 v[2:3], v224, s[48:51], s12 offen offset:0\n"
    "buffer_load_dwordx2 v[4:5], v224, s[48:51], s13 offen offset:0\n"
    "buffer_load_dwordx2 v[6:7], v224, s[48:51], s14 offen offset:0\n"
    "buffer_load_dwordx2 v[8:9], v224, s[48:51], s15 offen offset:0\n"
    "buffer_load_dwordx2 v[10:11], v224, s[48:51], s16 offen offset:0\n"
    "buffer_load_dwordx2 v[12:13], v224, s[48:51], s17 offen offset:0\n"
    "s_waitcnt vmcnt(6)\n"
    "s_waitcnt lgkmcnt(0)\n"
    "s_barrier\n"
    "s_cmp_eq_u32 s64, 0\n"
    "s_cbranch_scc0 L_arrivedx\n"
    "v_mov_b32 v248, 0\n"
    "v_mov_b32 v249, 1\n"
    "s_mov_b64 s[96:97], exec\n"
    "s_mov_b64 exec, 1\n"
    "global_atomic_add v254, v248, v249, s[98:99] sc0\n"
    "s_mov_b64 exec, s[96:97]\n"
    "L_arrivedx:\n"
    "s_cmp_eq_u32 s64, 0\n"
    "s_cbranch_scc0 L_polledx\n"
    "s_waitcnt vmcnt(0)\n"
    "v_readfirstlane_b32 s89, v254\n"
    "s_nop 3\n"
    "s_cmp_ge_u32 s89, 7\n"
    "s_cbranch_scc1 L_polledx\n"
    "v_mov_b32 v248, 0\n"
    "s_mov_b64 s[96:97], exec\n"
    "s_mov_b64 exec, 1\n"
    "s_mov_b32 s88, 0x200000\n"
    "L_pollx:\n"
    "global_load_dword v250, v248, s[98:99] sc1\n"
    "s_waitcnt vmcnt(0)\n"
    "v_readfirstlane_b32 s89, v250\n"
    "s_nop 3\n"
    "s_cmp_ge_u32 s89, 8\n"
    "s_cbranch_scc1 L_pollokx\n"
    "s_sleep 1\n"
    "s_sub_u32 s88, s88, 1\n"
    "s_cbranch_scc0 L_pollx\n"
    "L_pollokx:\n"
    "s_mov_b64 exec, s[96:97]\n"
    "L_polledx:\n"
    "s_barrier\n"
    "s_mov_b32 s88, 0xffff0000\n"
    "s_mov_b32 s89, 0xbfb8aa3b\n"
    "s_lshl_b32 s85, s64, 10\n"
    "s_add_u32 s85, s85, 81920\n"
    "v_add_u32 v14, s85, v226\n"
    "ds_read_b128 v[160:163], v14 offset:0\n"
    "ds_read_b128 v[164:167], v14 offset:4096\n"
    "ds_read_b128 v[168:171], v14 offset:8192\n"
    "s_add_u32 s84, s18, 0\n"
    "s_cmp_eq_u32 s65, 0\n"
    "s_cselect_b32 s84, s75, s84\n"
    "buffer_load_dwordx4 v[16:19], v226, s[44:47], s84 offen offset:0 sc1\n"
    "s_add_u32 s84, s18, 81920\n"
    "s_cmp_eq_u32 s65, 1\n"
    "s_cselect_b32 s84, s75, s84\n"
    "buffer_load_dwordx4 v[20:23], v226, s[44:47], s84 offen offset:0 sc1\n"
    "s_add_u32 s84, s18, 163840\n"
    "s_cmp_eq_u32 s65, 2\n"
    "s_cselect_b32 s84, s75, s84\n"
    "buffer_load_dwordx4 v[24:27], v226, s[44:47], s84 offen offset:0 sc1\n"
    "s_add_u32 s84, s18, 245760\n"
    "s_cmp_eq_u32 s65, 3\n"
    "s_cselect_b32 s84, s75, s84\n"
    "buffer_load_dwordx4 v[28:31], v226, s[44:47], s84 offen offset:0 sc1\n"
    "s_add_u32 s84, s18, 327680\n"
    "s_cmp_eq_u32 s65, 4\n"
    "s_cselect_b32 s84, s75, s84\n"
    "buffer_load_dwordx4 v[32:35], v226, s[44:47], s84 offen offset:0 sc1\n"
    "s_add_u32 s84, s18, 409600\n"
    "s_cmp_eq_u32 s65, 5\n"
    "s_cselect_b32 s84, s75, s84\n"
    "buffer_load_dwordx4 v[36:39], v226, s[44:47], s84 offen offset:0 sc1\n"
    "s_add_u32 s84, s18, 491520\n"
    "s_cmp_eq_u32 s65, 6\n"
    "s_cselect_b32 s84, s75, s84\n"
    "buffer_load_dwordx4 v[40:43], v226, s[44:47], s84 offen offset:0 sc1\n"
    "s_add_u32 s84, s18, 573440\n"
    "s_cmp_eq_u32 s65, 7\n"
    "s_cselect_b32 s84, s75, s84\n"
    "buffer_load_dwordx4 v[44:47], v226, s[44:47], s84 offen offset:0 sc1\n"
    "s_add_u32 s84, s18, 4096\n"
    "s_cmp_eq_u32 s65, 0\n"
    "s_cselect_b32 s84, s75, s84\n"
    "buffer_load_dwordx4 v[48:51], v226, s[44:47], s84 offen offset:0 sc1\n"
    "s_add_u32 s84, s18, 86016\n"
    "s_cmp_eq_u32 s65, 1\n"
    "s_cselect_b32 s84, s75, s84\n"
    "buffer_load_dwordx4 v[52:55], v226, s[44:47], s84 offen offset:0 sc1\n"
    "s_add_u32 s84, s18, 167936\n"
    "s_cmp_eq_u32 s65, 2\n"
    "s_cselect_b32 s84, s75, s84\n"
    "buffer_load_dwordx4 v[56:59], v226, s[44:47], s84 offen offset:0 sc1\n"
    "s_add_u32 s84, s18, 249856\n"
    "s_cmp_eq_u32 s65, 3\n"
    "s_cselect_b32 s84, s75, s84\n"
    "buffer_load_dwordx4 v[60:63], v226, s[44:47], s84 offen offset:0 sc1\n"
    "s_add_u32 s84, s18, 331776\n"
    "s_cmp_eq_u32 s65, 4\n"
    "s_cselect_b32 s84, s75, s84\n"
    "buffer_load_dwordx4 v[64:67], v226, s[44:47], s84 offen offset:0 sc1\n"
    "s_add_u32 s84, s18, 413696\n"
    "s_cmp_eq_u32 s65, 5\n"
    "s_cselect_b32 s84, s75, s84\n"
    "buffer_load_dwordx4 v[68:71], v226, s[44:47], s84 offen offset:0 sc1\n"
    "s_add_u32 s84, s18, 495616\n"
    "s_cmp_eq_u32 s65, 6\n"
    "s_cselect_b32 s84, s75, s84\n"
    "buffer_load_dwordx4 v[72:75], v226, s[44:47], s84 offen offset:0 sc1\n"
    "s_add_u32 s84, s18, 577536\n"
    "s_cmp_eq_u32 s65, 7\n"
    "s_cselect_b32 s84, s75, s84\n"
    "buffer_load_dwordx4 v[76:79], v226, s[44:47], s84 offen offset:0 sc1\n"
    "s_add_u32 s84, s19, 0\n"
    "s_cmp_eq_u32 s65, 0\n"
    "s_cselect_b32 s84, s75, s84\n"
    "buffer_load_dwordx4 v[80:83], v226, s[44:47], s84 offen offset:0 sc1\n"
    "s_add_u32 s84, s19, 81920\n"
    "s_cmp_eq_u32 s65, 1\n"
    "s_cselect_b32 s84, s75, s84\n"
    "buffer_load_dwordx4 v[84:87], v226, s[44:47], s84 offen offset:0 sc1\n"
    "s_add_u32 s84, s19, 163840\n"
    "s_cmp_eq_u32 s65, 2\n"
    "s_cselect_b32 s84, s75, s84\n"
    "buffer_load_dwordx4 v[88:91], v226, s[44:47], s84 offen offset:0 sc1\n"
    "s_add_u32 s84, s19, 245760\n"
    "s_cmp_eq_u32 s65, 3\n"
    "s_cselect_b32 s84, s75, s84\n"
    "buffer_load_dwordx4 v[92:95], v226, s[44:47], s84 offen offset:0 sc1\n"
    "s_add_u32 s84, s19, 327680\n"
    "s_cmp_eq_u32 s65, 4\n"
    "s_cselect_b32 s84, s75, s84\n"
    "buffer_load_dwordx4 v[96:99], v226, s[44:47], s84 offen offset:0 sc1\n"
    "s_add_u32 s84, s19, 409600\n"
    "s_cmp_eq_u32 s65, 5\n"
    "s_cselect_b32 s84, s75, s84\n"
    "buffer_load_dwordx4 v[100:103], v226, s[44:47], s84 offen offset:0 sc1\n"
    "s_add_u32 s84, s19, 491520\n"
    "s_cmp_eq_u32 s65, 6\n"
    "s_cselect_b32 s84, s75, s84\n"
    "buffer_load_dwordx4 v[104:107], v226, s[44:47], s84 offen offset:0 sc1\n"
    "s_add_u32 s84, s19, 573440\n"
    "s_cmp_eq_u32 s65, 7\n"
    "s_cselect_b32 s84, s75, s84\n"
    "buffer_load_dwordx4 v[108:111], v226, s[44:47], s84 offen offset:0 sc1\n"
    "s_waitcnt vmcnt(16)\n"
    "v_lshlrev_b32 v112, 16, v16\n"
    "v_and_b32 v113, s88, v16\n"
    "v_lshlrev_b32 v114, 16, v17\n"
    "v_and_b32 v115, s88, v17\n"
    "v_lshlrev_b32 v116, 16, v18\n"
    "v_and_b32 v117, s88, v18\n"
    "v_lshlrev_b32 v118, 16, v19\n"
    "v_and_b32 v119, s88, v19\n"
    "v_lshlrev_b32 v120, 16, v20\n"
    "v_and_b32 v121, s88, v20\n"
    "v_lshlrev_b32 v122, 16, v21\n"
    "v_and_b32 v123, s88, v21\n"
    "v_lshlrev_b32 v124, 16, v22\n"
    "v_and_b32 v125, s88, v22\n"
    "v_lshlrev_b32 v126, 16, v23\n"
    "v_and_b32 v127, s88, v23\n"
    "v_pk_add_f32 v[112:113], v[112:113], v[120:121]\n"
    "v_pk_add_f32 v[114:115], v[114:115], v[122:123]\n"
    "v_pk_add_f32 v[116:117], v[116:117], v[124:125]\n"
    "v_pk_add_f32 v[118:119], v[118:119], v[126:127]\n"
    "v_lshlrev_b32 v120, 16, v24\n"
    "v_and_b32 v121, s88, v24\n"
    "v_lshlrev_b32 v122, 16, v25\n"
    "v_and_b32 v123, s88, v25\n"
    "v_lshlrev_b32 v124, 16, v26\n"
    "v_and_b32 v125, s88, v26\n"
    "v_lshlrev_b32 v126, 16, v27\n"
    "v_and_b32 v127, s88, v27\n"
    "v_pk_add_f32 v[112:113], v[112:113], v[120:121]\n"
    "v_pk_add_f32 v[114:115], v[114:115], v[122:123]\n"
    "v_pk_add_f32 v[116:117], v[116:117], v[124:125]\n"
    "v_pk_add_f32 v[118:119], v[118:119], v[126:127]\n"
    "v_lshlrev_b32 v120, 16, v28\n"
    "v_and_b32 v121, s88, v28\n"
    "v_lshlrev_b32 v122, 16, v29\n"
    "v_and_b32 v123, s88, v29\n"
    "v_lshlrev_b32 v124, 16, v30\n"
    "v_and_b32 v125, s88, v30\n"
    "v_lshlrev_b32 v126, 16, v31\n"
    "v_and_b32 v127, s88, v31\n"
    "v_pk_add_f32 v[112:113], v[112:113], v[120:121]\n"
    "v_pk_add_f32 v[114:115], v[114:115], v[122:123]\n"
    "v_pk_add_f32 v[116:117], v[116:117], v[124:125]\n"
    "v_pk_add_f32 v[118:119], v[118:119], v[126:127]\n"
    "v_lshlrev_b32 v120, 16, v32\n"
    "v_and_b32 v121, s88, v32\n"
    "v_lshlrev_b32 v122, 16, v33\n"
    "v_and_b32 v123, s88, v33\n"
    "v_lshlrev_b32 v124, 16, v34\n"
    "v_and_b32 v125, s88, v34\n"
    "v_lshlrev_b32 v126, 16, v35\n"
    "v_and_b32 v127, s88, v35\n"
    "v_pk_add_f32 v[112:113], v[112:113], v[120:121]\n"
    "v_pk_add_f32 v[114:115], v[114:115], v[122:123]\n"
    "v_pk_add_f32 v[116:117], v[116:117], v[124:125]\n"
    "v_pk_add_f32 v[118:119], v[118:119], v[126:127]\n"
    "v_lshlrev_b32 v120, 16, v36\n"
    "v_and_b32 v121, s88, v36\n"
    "v_lshlrev_b32 v122, 16, v37\n"
    "v_and_b32 v123, s88, v37\n"
    "v_lshlrev_b32 v124, 16, v38\n"
    "v_and_b32 v125, s88, v38\n"
    "v_lshlrev_b32 v126, 16, v39\n"
    "v_and_b32 v127, s88, v39\n"
    "v_pk_add_f32 v[112:113], v[112:113], v[120:121]\n"
    "v_pk_add_f32 v[114:115], v[114:115], v[122:123]\n"
    "v_pk_add_f32 v[116:117], v[116:117], v[124:125]\n"
    "v_pk_add_f32 v[118:119], v[118:119], v[126:127]\n"
    "v_lshlrev_b32 v128, 16, v40\n"
    "v_and_b32 v129, s88, v40\n"
    "v_lshlrev_b32 v130, 16, v41\n"
    "v_and_b32 v131, s88, v41\n"
    "v_lshlrev_b32 v132, 16, v42\n"
    "v_and_b32 v133, s88, v42\n"
    "v_lshlrev_b32 v134, 16, v43\n"
    "v_and_b32 v135, s88, v43\n"
    "v_lshlrev_b32 v120, 16, v44\n"
    "v_and_b32 v121, s88, v44\n"
    "v_lshlrev_b32 v122, 16, v45\n"
    "v_and_b32 v123, s88, v45\n"
    "v_lshlrev_b32 v124, 16, v46\n"
    "v_and_b32 v125, s88, v46\n"
    "v_lshlrev_b32 v126, 16, v47\n"
    "v_and_b32 v127, s88, v47\n"
    "v_pk_add_f32 v[128:129], v[128:129], v[120:121]\n"
    "v_pk_add_f32 v[130:131], v[130:131], v[122:123]\n"
    "v_pk_add_f32 v[132:133], v[132:133], v[124:125]\n"
    "v_pk_add_f32 v[134:135], v[134:135], v[126:127]\n"
    "s_waitcnt lgkmcnt(2)\n"
    "v_lshlrev_b32 v120, 16, v160\n"
    "v_and_b32 v121, s88, v160\n"
    "v_lshlrev_b32 v122, 16, v161\n"
    "v_and_b32 v123, s88, v161\n"
    "v_lshlrev_b32 v124, 16, v162\n"
    "v_and_b32 v125, s88, v162\n"
    "v_lshlrev_b32 v126, 16, v163\n"
    "v_and_b32 v127, s88, v163\n"
    "s_cmp_lt_u32 s65, 6\n"
    "s_cbranch_scc0 L_owno0\n"
    "v_pk_add_f32 v[112:113], v[112:113], v[120:121]\n"
    "v_pk_add_f32 v[114:115], v[114:115], v[122:123]\n"
    "v_pk_add_f32 v[116:117], v[116:117], v[124:125]\n"
    "v_pk_add_f32 v[118:119], v[118:119], v[126:127]\n"
    "s_branch L_ownd0\n"
    "L_owno0:\n"
    "v_pk_add_f32 v[128:129], v[128:129], v[120:121]\n"
    "v_pk_add_f32 v[130:131], v[130:131], v[122:123]\n"
    "v_pk_add_f32 v[132:133], v[132:133], v[124:125]\n"
    "v_pk_add_f32 v[134:135], v[134:135], v[126:127]\n"
    "L_ownd0:\n"
    "v_mul_f32 v136, s89, v128\n"
    "v_mul_f32 v137, s89, v129\n"
    "v_mul_f32 v138, s89, v130\n"
    "v_mul_f32 v139, s89, v131\n"
    "v_mul_f32 v140, s89, v132\n"
    "v_mul_f32 v141, s89, v133\n"
    "v_mul_f32 v142, s89, v134\n"
    "v_mul_f32 v143, s89, v135\n"
    "v_exp_f32 v136, v136\n"
    "v_exp_f32 v137, v137\n"
    "v_exp_f32 v138, v138\n"
    "v_exp_f32 v139, v139\n"
    "v_exp_f32 v140, v140\n"
    "v_exp_f32 v141, v141\n"
    "v_exp_f32 v142, v142\n"
    "v_exp_f32 v143, v143\n"
    "v_pk_mul_f32 v[112:113], v[112:113], v[128:129]\n"
    "v_pk_mul_f32 v[114:115], v[114:115], v[130:131]\n"
    "v_pk_mul_f32 v[116:117], v[116:117], v[132:133]\n"
    "v_pk_mul_f32 v[118:119], v[118:119], v[134:135]\n"
    "v_add_f32 v136, 1.0, v136\n"
    "v_add_f32 v137, 1.0, v137\n"
    "v_add_f32 v138, 1.0, v138\n"
    "v_add_f32 v139, 1.0, v139\n"
    "v_add_f32 v140, 1.0, v140\n"
    "v_add_f32 v141, 1.0, v141\n"
    "v_add_f32 v142, 1.0, v142\n"
    "v_add_f32 v143, 1.0, v143\n"
    "v_rcp_f32 v136, v136\n"
    "v_rcp_f32 v137, v137\n"
    "v_rcp_f32 v138, v138\n"
    "v_rcp_f32 v139, v139\n"
    "v_rcp_f32 v140, v140\n"
    "v_rcp_f32 v141, v141\n"
    "v_rcp_f32 v142, v142\n"
    "v_rcp_f32 v143, v143\n"
    "v_lshlrev_b32 v120, 16, v2\n"
    "v_and_b32 v121, s88, v2\n"
    "v_lshlrev_b32 v122, 16, v3\n"
    "v_and_b32 v123, s88, v3\n"
    "v_lshlrev_b32 v124, 16, v4\n"
    "v_and_b32 v125, s88, v4\n"
    "v_lshlrev_b32 v126, 16, v5\n"
    "v_and_b32 v127, s88, v5\n"
    "v_pk_fma_f32 v[112:113], v[112:113], v[136:137], v[120:121]\n"
    "v_pk_fma_f32 v[114:115], v[114:115], v[138:139], v[122:123]\n"
    "v_pk_fma_f32 v[116:117], v[116:117], v[140:141], v[124:125]\n"
    "v_pk_fma_f32 v[118:119], v[118:119], v[142:143], v[126:127]\n"
    "v_cvt_pk_bf16_f32 v144, v112, v113\n"
    "v_cvt_pk_bf16_f32 v145, v114, v115\n"
    "buffer_store_dwordx2 v[144:145], v224, s[52:55], s12 offen offset:0\n"
    "v_cvt_pk_bf16_f32 v146, v116, v117\n"
    "v_cvt_pk_bf16_f32 v147, v118, v119\n"
    "buffer_store_dwordx2 v[146:147], v224, s[52:55], s13 offen offset:0\n"
    "s_waitcnt vmcnt(10)\n"
    "v_lshlrev_b32 v112, 16, v48\n"
    "v_and_b32 v113, s88, v48\n"
    "v_lshlrev_b32 v114, 16, v49\n"
    "v_and_b32 v115, s88, v49\n"
    "v_lshlrev_b32 v116, 16, v50\n"
    "v_and_b32 v117, s88, v50\n"
    "v_lshlrev_b32 v118, 16, v51\n"
    "v_and_b32 v119, s88, v51\n"
    "v_lshlrev_b32 v120, 16, v52\n"
    "v_and_b32 v121, s88, v52\n"
    "v_lshlrev_b32 v122, 16, v53\n"
    "v_and_b32 v123, s88, v53\n"
    "v_lshlrev_b32 v124, 16, v54\n"
    "v_and_b32 v125, s88, v54\n"
    "v_lshlrev_b32 v126, 16, v55\n"
    "v_and_b32 v127, s88, v55\n"
    "v_pk_add_f32 v[112:113], v[112:113], v[120:121]\n"
    "v_pk_add_f32 v[114:115], v[114:115], v[122:123]\n"
    "v_pk_add_f32 v[116:117], v[116:117], v[124:125]\n"
    "v_pk_add_f32 v[118:119], v[118:119], v[126:127]\n"
    "v_lshlrev_b32 v120, 16, v56\n"
    "v_and_b32 v121, s88, v56\n"
    "v_lshlrev_b32 v122, 16, v57\n"
    "v_and_b32 v123, s88, v57\n"
    "v_lshlrev_b32 v124, 16, v58\n"
    "v_and_b32 v125, s88, v58\n"
    "v_lshlrev_b32 v126, 16, v59\n"
    "v_and_b32 v127, s88, v59\n"
    "v_pk_add_f32 v[112:113], v[112:113], v[120:121]\n"
    "v_pk_add_f32 v[114:115], v[114:115], v[122:123]\n"
    "v_pk_add_f32 v[116:117], v[116:117], v[124:125]\n"
    "v_pk_add_f32 v[118:119], v[118:119], v[126:127]\n"
    "v_lshlrev_b32 v120, 16, v60\n"
    "v_and_b32 v121, s88, v60\n"
    "v_lshlrev_b32 v122, 16, v61\n"
    "v_and_b32 v123, s88, v61\n"
    "v_lshlrev_b32 v124, 16, v62\n"
    "v_and_b32 v125, s88, v62\n"
    "v_lshlrev_b32 v126, 16, v63\n"
    "v_and_b32 v127, s88, v63\n"
    "v_pk_add_f32 v[112:113], v[112:113], v[120:121]\n"
    "v_pk_add_f32 v[114:115], v[114:115], v[122:123]\n"
    "v_pk_add_f32 v[116:117], v[116:117], v[124:125]\n"
    "v_pk_add_f32 v[118:119], v[118:119], v[126:127]\n"
    "v_lshlrev_b32 v120, 16, v64\n"
    "v_and_b32 v121, s88, v64\n"
    "v_lshlrev_b32 v122, 16, v65\n"
    "v_and_b32 v123, s88, v65\n"
    "v_lshlrev_b32 v124, 16, v66\n"
    "v_and_b32 v125, s88, v66\n"
    "v_lshlrev_b32 v126, 16, v67\n"
    "v_and_b32 v127, s88, v67\n"
    "v_pk_add_f32 v[112:113], v[112:113], v[120:121]\n"
    "v_pk_add_f32 v[114:115], v[114:115], v[122:123]\n"
    "v_pk_add_f32 v[116:117], v[116:117], v[124:125]\n"
    "v_pk_add_f32 v[118:119], v[118:119], v[126:127]\n"
    "v_lshlrev_b32 v120, 16, v68\n"
    "v_and_b32 v121, s88, v68\n"
    "v_lshlrev_b32 v122, 16, v69\n"
    "v_and_b32 v123, s88, v69\n"
    "v_lshlrev_b32 v124, 16, v70\n"
    "v_and_b32 v125, s88, v70\n"
    "v_lshlrev_b32 v126, 16, v71\n"
    "v_and_b32 v127, s88, v71\n"
    "v_pk_add_f32 v[112:113], v[112:113], v[120:121]\n"
    "v_pk_add_f32 v[114:115], v[114:115], v[122:123]\n"
    "v_pk_add_f32 v[116:117], v[116:117], v[124:125]\n"
    "v_pk_add_f32 v[118:119], v[118:119], v[126:127]\n"
    "v_lshlrev_b32 v128, 16, v72\n"
    "v_and_b32 v129, s88, v72\n"
    "v_lshlrev_b32 v130, 16, v73\n"
    "v_and_b32 v131, s88, v73\n"
    "v_lshlrev_b32 v132, 16, v74\n"
    "v_and_b32 v133, s88, v74\n"
    "v_lshlrev_b32 v134, 16, v75\n"
    "v_and_b32 v135, s88, v75\n"
    "v_lshlrev_b32 v120, 16, v76\n"
    "v_and_b32 v121, s88, v76\n"
    "v_lshlrev_b32 v122, 16, v77\n"
    "v_and_b32 v123, s88, v77\n"
    "v_lshlrev_b32 v124, 16, v78\n"
    "v_and_b32 v125, s88, v78\n"
    "v_lshlrev_b32 v126, 16, v79\n"
    "v_and_b32 v127, s88, v79\n"
    "v_pk_add_f32 v[128:129], v[128:129], v[120:121]\n"
    "v_pk_add_f32 v[130:131], v[130:131], v[122:123]\n"
    "v_pk_add_f32 v[132:133], v[132:133], v[124:125]\n"
    "v_pk_add_f32 v[134:135], v[134:135], v[126:127]\n"
    "s_waitcnt lgkmcnt(1)\n"
    "v_lshlrev_b32 v120, 16, v164\n"
    "v_and_b32 v121, s88, v164\n"
    "v_lshlrev_b32 v122, 16, v165\n"
    "v_and_b32 v123, s88, v165\n"
    "v_lshlrev_b32 v124, 16, v166\n"
    "v_and_b32 v125, s88, v166\n"
    "v_lshlrev_b32 v126, 16, v167\n"
    "v_and_b32 v127, s88, v167\n"
    "s_cmp_lt_u32 s65, 6\n"
    "s_cbranch_scc0 L_owno1\n"
    "v_pk_add_f32 v[112:113], v[112:113], v[120:121]\n"
    "v_pk_add_f32 v[114:115], v[114:115], v[122:123]\n"
    "v_pk_add_f32 v[116:117], v[116:117], v[124:125]\n"
    "v_pk_add_f32 v[118:119], v[118:119], v[126:127]\n"
    "s_branch L_ownd1\n"
    "L_owno1:\n"
    "v_pk_add_f32 v[128:129], v[128:129], v[120:121]\n"
    "v_pk_add_f32 v[130:131], v[130:131], v[122:123]\n"
    "v_pk_add_f32 v[132:133], v[132:133], v[124:125]\n"
    "v_pk_add_f32 v[134:135], v[134:135], v[126:127]\n"
    "L_ownd1:\n"
    "v_mul_f32 v136, s89, v128\n"
    "v_mul_f32 v137, s89, v129\n"
    "v_mul_f32 v138, s89, v130\n"
    "v_mul_f32 v139, s89, v131\n"
    "v_mul_f32 v140, s89, v132\n"
    "v_mul_f32 v141, s89, v133\n"
    "v_mul_f32 v142, s89, v134\n"
    "v_mul_f32 v143, s89, v135\n"
    "v_exp_f32 v136, v136\n"
    "v_exp_f32 v137, v137\n"
    "v_exp_f32 v138, v138\n"
    "v_exp_f32 v139, v139\n"
    "v_exp_f32 v140, v140\n"
    "v_exp_f32 v141, v141\n"
    "v_exp_f32 v142, v142\n"
    "v_exp_f32 v143, v143\n"
    "v_pk_mul_f32 v[112:113], v[112:113], v[128:129]\n"
    "v_pk_mul_f32 v[114:115], v[114:115], v[130:131]\n"
    "v_pk_mul_f32 v[116:117], v[116:117], v[132:133]\n"
    "v_pk_mul_f32 v[118:119], v[118:119], v[134:135]\n"
    "v_add_f32 v136, 1.0, v136\n"
    "v_add_f32 v137, 1.0, v137\n"
    "v_add_f32 v138, 1.0, v138\n"
    "v_add_f32 v139, 1.0, v139\n"
    "v_add_f32 v140, 1.0, v140\n"
    "v_add_f32 v141, 1.0, v141\n"
    "v_add_f32 v142, 1.0, v142\n"
    "v_add_f32 v143, 1.0, v143\n"
    "v_rcp_f32 v136, v136\n"
    "v_rcp_f32 v137, v137\n"
    "v_rcp_f32 v138, v138\n"
    "v_rcp_f32 v139, v139\n"
    "v_rcp_f32 v140, v140\n"
    "v_rcp_f32 v141, v141\n"
    "v_rcp_f32 v142, v142\n"
    "v_rcp_f32 v143, v143\n"
    "v_lshlrev_b32 v120, 16, v6\n"
    "v_and_b32 v121, s88, v6\n"
    "v_lshlrev_b32 v122, 16, v7\n"
    "v_and_b32 v123, s88, v7\n"
    "v_lshlrev_b32 v124, 16, v8\n"
    "v_and_b32 v125, s88, v8\n"
    "v_lshlrev_b32 v126, 16, v9\n"
    "v_and_b32 v127, s88, v9\n"
    "v_pk_fma_f32 v[112:113], v[112:113], v[136:137], v[120:121]\n"
    "v_pk_fma_f32 v[114:115], v[114:115], v[138:139], v[122:123]\n"
    "v_pk_fma_f32 v[116:117], v[116:117], v[140:141], v[124:125]\n"
    "v_pk_fma_f32 v[118:119], v[118:119], v[142:143], v[126:127]\n"
    "v_cvt_pk_bf16_f32 v148, v112, v113\n"
    "v_cvt_pk_bf16_f32 v149, v114, v115\n"
    "buffer_store_dwordx2 v[148:149], v224, s[52:55], s14 offen offset:0\n"
    "v_cvt_pk_bf16_f32 v150, v116, v117\n"
    "v_cvt_pk_bf16_f32 v151, v118, v119\n"
    "buffer_store_dwordx2 v[150:151], v224, s[52:55], s15 offen offset:0\n"
    "s_waitcnt vmcnt(4)\n"
    "v_lshlrev_b32 v112, 16, v80\n"
    "v_and_b32 v113, s88, v80\n"
    "v_lshlrev_b32 v114, 16, v81\n"
    "v_and_b32 v115, s88, v81\n"
    "v_lshlrev_b32 v116, 16, v82\n"
    "v_and_b32 v117, s88, v82\n"
    "v_lshlrev_b32 v118, 16, v83\n"
    "v_and_b32 v119, s88, v83\n"
    "v_lshlrev_b32 v120, 16, v84\n"
    "v_and_b32 v121, s88, v84\n"
    "v_lshlrev_b32 v122, 16, v85\n"
    "v_and_b32 v123, s88, v85\n"
    "v_lshlrev_b32 v124, 16, v86\n"
    "v_and_b32 v125, s88, v86\n"
    "v_lshlrev_b32 v126, 16, v87\n"
    "v_and_b32 v127, s88, v87\n"
    "v_pk_add_f32 v[112:113], v[112:113], v[120:121]\n"
    "v_pk_add_f32 v[114:115], v[114:115], v[122:123]\n"
    "v_pk_add_f32 v[116:117], v[116:117], v[124:125]\n"
    "v_pk_add_f32 v[118:119], v[118:119], v[126:127]\n"
    "v_lshlrev_b32 v120, 16, v88\n"
    "v_and_b32 v121, s88, v88\n"
    "v_lshlrev_b32 v122, 16, v89\n"
    "v_and_b32 v123, s88, v89\n"
    "v_lshlrev_b32 v124, 16, v90\n"
    "v_and_b32 v125, s88, v90\n"
    "v_lshlrev_b32 v126, 16, v91\n"
    "v_and_b32 v127, s88, v91\n"
    "v_pk_add_f32 v[112:113], v[112:113], v[120:121]\n"
    "v_pk_add_f32 v[114:115], v[114:115], v[122:123]\n"
    "v_pk_add_f32 v[116:117], v[116:117], v[124:125]\n"
    "v_pk_add_f32 v[118:119], v[118:119], v[126:127]\n"
    "v_lshlrev_b32 v120, 16, v92\n"
    "v_and_b32 v121, s88, v92\n"
    "v_lshlrev_b32 v122, 16, v93\n"
    "v_and_b32 v123, s88, v93\n"
    "v_lshlrev_b32 v124, 16, v94\n"
    "v_and_b32 v125, s88, v94\n"
    "v_lshlrev_b32 v126, 16, v95\n"
    "v_and_b32 v127, s88, v95\n"
    "v_pk_add_f32 v[112:113], v[112:113], v[120:121]\n"
    "v_pk_add_f32 v[114:115], v[114:115], v[122:123]\n"
    "v_pk_add_f32 v[116:117], v[116:117], v[124:125]\n"
    "v_pk_add_f32 v[118:119], v[118:119], v[126:127]\n"
    "v_lshlrev_b32 v120, 16, v96\n"
    "v_and_b32 v121, s88, v96\n"
    "v_lshlrev_b32 v122, 16, v97\n"
    "v_and_b32 v123, s88, v97\n"
    "v_lshlrev_b32 v124, 16, v98\n"
    "v_and_b32 v125, s88, v98\n"
    "v_lshlrev_b32 v126, 16, v99\n"
    "v_and_b32 v127, s88, v99\n"
    "v_pk_add_f32 v[112:113], v[112:113], v[120:121]\n"
    "v_pk_add_f32 v[114:115], v[114:115], v[122:123]\n"
    "v_pk_add_f32 v[116:117], v[116:117], v[124:125]\n"
    "v_pk_add_f32 v[118:119], v[118:119], v[126:127]\n"
    "v_lshlrev_b32 v120, 16, v100\n"
    "v_and_b32 v121, s88, v100\n"
    "v_lshlrev_b32 v122, 16, v101\n"
    "v_and_b32 v123, s88, v101\n"
    "v_lshlrev_b32 v124, 16, v102\n"
    "v_and_b32 v125, s88, v102\n"
    "v_lshlrev_b32 v126, 16, v103\n"
    "v_and_b32 v127, s88, v103\n"
    "v_pk_add_f32 v[112:113], v[112:113], v[120:121]\n"
    "v_pk_add_f32 v[114:115], v[114:115], v[122:123]\n"
    "v_pk_add_f32 v[116:117], v[116:117], v[124:125]\n"
    "v_pk_add_f32 v[118:119], v[118:119], v[126:127]\n"
    "v_lshlrev_b32 v128, 16, v104\n"
    "v_and_b32 v129, s88, v104\n"
    "v_lshlrev_b32 v130, 16, v105\n"
    "v_and_b32 v131, s88, v105\n"
    "v_lshlrev_b32 v132, 16, v106\n"
    "v_and_b32 v133, s88, v106\n"
    "v_lshlrev_b32 v134, 16, v107\n"
    "v_and_b32 v135, s88, v107\n"
    "v_lshlrev_b32 v120, 16, v108\n"
    "v_and_b32 v121, s88, v108\n"
    "v_lshlrev_b32 v122, 16, v109\n"
    "v_and_b32 v123, s88, v109\n"
    "v_lshlrev_b32 v124, 16, v110\n"
    "v_and_b32 v125, s88, v110\n"
    "v_lshlrev_b32 v126, 16, v111\n"
    "v_and_b32 v127, s88, v111\n"
    "v_pk_add_f32 v[128:129], v[128:129], v[120:121]\n"
    "v_pk_add_f32 v[130:131], v[130:131], v[122:123]\n"
    "v_pk_add_f32 v[132:133], v[132:133], v[124:125]\n"
    "v_pk_add_f32 v[134:135], v[134:135], v[126:127]\n"
    "s_waitcnt lgkmcnt(0)\n"
    "v_lshlrev_b32 v120, 16, v168\n"
    "v_and_b32 v121, s88, v168\n"
    "v_lshlrev_b32 v122, 16, v169\n"
    "v_and_b32 v123, s88, v169\n"
    "v_lshlrev_b32 v124, 16, v170\n"
    "v_and_b32 v125, s88, v170\n"
    "v_lshlrev_b32 v126, 16, v171\n"
    "v_and_b32 v127, s88, v171\n"
    "s_cmp_lt_u32 s65, 6\n"
    "s_cbranch_scc0 L_owno2\n"
    "v_pk_add_f32 v[112:113], v[112:113], v[120:121]\n"
    "v_pk_add_f32 v[114:115], v[114:115], v[122:123]\n"
    "v_pk_add_f32 v[116:117], v[116:117], v[124:125]\n"
    "v_pk_add_f32 v[118:119], v[118:119], v[126:127]\n"
    "s_branch L_ownd2\n"
    "L_owno2:\n"
    "v_pk_add_f32 v[128:129], v[128:129], v[120:121]\n"
    "v_pk_add_f32 v[130:131], v[130:131], v[122:123]\n"
    "v_pk_add_f32 v[132:133], v[132:133], v[124:125]\n"
    "v_pk_add_f32 v[134:135], v[134:135], v[126:127]\n"
    "L_ownd2:\n"
    "v_mul_f32 v136, s89, v128\n"
    "v_mul_f32 v137, s89, v129\n"
    "v_mul_f32 v138, s89, v130\n"
    "v_mul_f32 v139, s89, v131\n"
    "v_mul_f32 v140, s89, v132\n"
    "v_mul_f32 v141, s89, v133\n"
    "v_mul_f32 v142, s89, v134\n"
    "v_mul_f32 v143, s89, v135\n"
    "v_exp_f32 v136, v136\n"
    "v_exp_f32 v137, v137\n"
    "v_exp_f32 v138, v138\n"
    "v_exp_f32 v139, v139\n"
    "v_exp_f32 v140, v140\n"
    "v_exp_f32 v141, v141\n"
    "v_exp_f32 v142, v142\n"
    "v_exp_f32 v143, v143\n"
    "v_pk_mul_f32 v[112:113], v[112:113], v[128:129]\n"
    "v_pk_mul_f32 v[114:115], v[114:115], v[130:131]\n"
    "v_pk_mul_f32 v[116:117], v[116:117], v[132:133]\n"
    "v_pk_mul_f32 v[118:119], v[118:119], v[134:135]\n"
    "v_add_f32 v136, 1.0, v136\n"
    "v_add_f32 v137, 1.0, v137\n"
    "v_add_f32 v138, 1.0, v138\n"
    "v_add_f32 v139, 1.0, v139\n"
    "v_add_f32 v140, 1.0, v140\n"
    "v_add_f32 v141, 1.0, v141\n"
    "v_add_f32 v142, 1.0, v142\n"
    "v_add_f32 v143, 1.0, v143\n"
    "v_rcp_f32 v136, v136\n"
    "v_rcp_f32 v137, v137\n"
    "v_rcp_f32 v138, v138\n"
    "v_rcp_f32 v139, v139\n"
    "v_rcp_f32 v140, v140\n"
    "v_rcp_f32 v141, v141\n"
    "v_rcp_f32 v142, v142\n"
    "v_rcp_f32 v143, v143\n"
    "v_lshlrev_b32 v120, 16, v10\n"
    "v_and_b32 v121, s88, v10\n"
    "v_lshlrev_b32 v122, 16, v11\n"
    "v_and_b32 v123, s88, v11\n"
    "v_lshlrev_b32 v124, 16, v12\n"
    "v_and_b32 v125, s88, v12\n"
    "v_lshlrev_b32 v126, 16, v13\n"
    "v_and_b32 v127, s88, v13\n"
    "v_pk_fma_f32 v[112:113], v[112:113], v[136:137], v[120:121]\n"
    "v_pk_fma_f32 v[114:115], v[114:115], v[138:139], v[122:123]\n"
    "v_pk_fma_f32 v[116:117], v[116:117], v[140:141], v[124:125]\n"
    "v_pk_fma_f32 v[118:119], v[118:119], v[142:143], v[126:127]\n"
    "v_cvt_pk_bf16_f32 v152, v112, v113\n"
    "v_cvt_pk_bf16_f32 v153, v114, v115\n"
    "buffer_store_dwordx2 v[152:153], v224, s[52:55], s16 offen offset:0\n"
    "v_cvt_pk_bf16_f32 v154, v116, v117\n"
    "v_cvt_pk_bf16_f32 v155, v118, v119\n"
    "buffer_store_dwordx2 v[154:155], v224, s[52:55], s17 offen offset:0\n"
    "L_k3end:\n"
    :: "s"(blockIdx.x), "s"(__builtin_amdgcn_kernarg_segment_ptr()), "v"(threadIdx.x)
    : "memory", "vcc", "scc", "v1","v2","v3","v4","v5","v6","v7","v8","v9","v10","v11","v12","v13","v14","v15","v16","v17","v18","v19","v20","v21","v22","v23","v24","v25","v26","v27","v28","v29","v30","v31","v32","v33","v34","v35","v36","v37","v38","v39","v40","v41","v42","v43","v44","v45","v46","v47","v48","v49","v50","v51","v52","v53","v54","v55","v56","v57","v58","v59","v60","v61","v62","v63","v64","v65","v66","v67","v68","v69","v70","v71","v72","v73","v74","v75","v76","v77","v78","v79","v80","v81","v82","v83","v84","v85","v86","v87","v88","v89","v90","v91","v92","v93","v94","v95","v96","v97","v98","v99","v100","v101","v102","v103","v104","v105","v106","v107","v108","v109","v110","v111","v112","v113","v114","v115","v116","v117","v118","v119","v120","v121","v122","v123","v124","v125","v126","v127","v128","v129","v130","v131","v132","v133","v134","v135","v136","v137","v138","v139","v140","v141","v142","v143","v144","v145","v146","v147","v148","v149","v150","v151","v152","v153","v154","v155","v156","v157","v158","v159","v160","v161","v162","v163","v164","v165","v166","v167","v168","v169","v170","v171","v172","v173","v174","v175","v176","v177","v178","v179","v180","v181","v182","v183","v184","v185","v186","v187","v188","v189","v190","v191","v192","v193","v194","v195","v196","v197","v198","v199","v200","v201","v202","v203","v204","v205","v206","v207","v208","v209","v210","v211","v212","v213","v214","v215","v216","v217","v218","v219","v220","v221","v222","v223","v224","v225","v226","v227","v228","v229","v230","v231","v232","v233","v234","v235","v236","v237","v238","v239","v240","v241","v242","v243","v244","v245","v246","v247","v248","v249","v250","v251","v252","v253","v254","v255", "a0","a1","a2","a3","a4","a5","a6","a7","a8","a9","a10","a11","a12","a13","a14","a15","a16","a17","a18","a19","a20","a21","a22","a23","a24","a25","a26","a27","a28","a29","a30","a31","a32","a33","a34","a35","a36","a37","a38","a39","a40","a41","a42","a43","a44","a45","a46","a47","a48","a49","a50","a51","a52","a53","a54","a55","a56","a57","a58","a59","a60","a61","a62","a63","a64","a65","a66","a67","a68","a69","a70","a71","a72","a73","a74","a75","a76","a77","a78","a79","a80","a81","a82","a83","a84","a85","a86","a87","a88","a89","a90","a91","a92","a93","a94","a95","a96","a97","a98","a99","a100","a101","a102","a103","a104","a105","a106","a107","a108","a109","a110","a111","a112","a113","a114","a115","a116","a117","a118","a119","a120","a121","a122","a123","a124","a125","a126","a127","a128","a129","a130","a131","a132","a133","a134","a135","a136","a137","a138","a139","a140","a141","a142","a143","a144","a145","a146","a147","a148","a149","a150","a151","a152","a153","a154","a155","a156","a157","a158","a159","a160","a161","a162","a163","a164","a165","a166","a167","a168","a169","a170","a171","a172","a173","a174","a175","a176","a177","a178","a179","a180","a181","a182","a183","a184","a185","a186","a187","a188","a189","a190","a191","a192","a193","a194","a195","a196","a197","a198","a199","a200","a201","a202","a203","a204","a205","a206","a207","a208","a209","a210","a211","a212","a213","a214","a215","a216","a217","a218","a219","a220","a221","a222","a223","a224","a225","a226","a227","a228","a229","a230","a231","a232","a233","a234","a235","a236","a237","a238","a239","a240","a241","a242","a243","a244","a245","a246","a247","a248","a249","a250","a251","a252","a253","a254","a255", "s3","s4","s5","s6","s7","s8","s9","s10","s11","s12","s13","s14","s15","s16","s17","s18","s19","s20","s21","s22","s23","s24","s25","s26","s27","s28","s29","s30","s31","s34","s35","s36","s37","s38","s39","s40","s41","s42","s43","s44","s45","s46","s47","s48","s49","s50","s51","s52","s53","s54","s55","s56","s57","s58","s59","s60","s61","s62","s63","s64","s65","s66","s67","s68","s69","s70","s71","s72","s73","s74","s75","s76","s77","s78","s79","s80","s81","s82","s83","s84","s85","s86","s87","s88","s89","s90","s91","s92","s93","s94","s95","s96","s97","s98","s99");
}

extern "C" void kernel_launch(void* const* d_in, const int* in_sizes, int n_in, void* d_out, int out_size, void* d_ws, size_t ws_size, hipStream_t stream) {
  const u16* x = (const u16*)d_in[0]; const u16* kc = (const u16*)d_in[1]; const u16* vc = (const u16*)d_in[2];
  const u16* wq = (const u16*)d_in[3]; const u16* wo = (const u16*)d_in[4];
  const u16* w1 = (const u16*)d_in[6]; const u16* w3 = (const u16*)d_in[7]; const u16* w2 = (const u16*)d_in[8];
  char* ws = (char*)d_ws;
  u16* ot = (u16*)ws;        ws += (size_t)S * HD * 2;
  u16* xnt = (u16*)ws;       ws += (size_t)S * D * 2;
  u16* ht = (u16*)ws;        ws += (size_t)S * F * 2;
  float* slab = (float*)ws;  ws += (size_t)32 * 8 * 81920;
  unsigned* cnt = (unsigned*)ws; ws += 64 * 256;
  unsigned* dbg = (unsigned*)((char*)d_ws + 200 * 1024 * 1024);
  (void)hipFuncSetAttribute((const void*)k2_attn, hipFuncAttributeMaxDynamicSharedMemorySize, 163840);
  k_ln<<<S / 8, 128, 0, stream>>>(x, xnt, cnt);
  k2_attn<<<256, 256, 163840, stream>>>(xnt, wq, kc, vc, ot, dbg, w1, w3, ht, wo, w2, slab, x, (u16*)d_out, cnt);
}
```
